```python
import torch

def _softmax(x, axis=-1):
    return torch.softmax(x, dim=axis)
def _gelu(x):
    return torch.nn.functional.gelu(x, approximate="tanh")
def _silu(x):
    return torch.nn.functional.silu(x)
def _sigmoid(x):
    return torch.sigmoid(x)
def _layernorm(x, axis=-1, eps=1e-5):
    m = x.mean(dim=axis, keepdim=True)
    v = x.var(dim=axis, keepdim=True, unbiased=False)
    return (x - m) / torch.sqrt(v + eps)
def _scatter_add(tgt, idx, src, axis=0):
    return tgt.clone().index_add_(axis, idx, src)
def _segment_sum(src, seg, n):
    out = torch.zeros((n,) + src.shape[1:], dtype=src.dtype)
    return out.index_add_(0, seg, src)
def _pad(x, pw):
    flat = tuple(v for pair in reversed(pw) for v in pair)
    return torch.nn.functional.pad(x, flat)

kernel_name = 'sequential_preLN__compressed_kv_cla__swiglu_ffn__prompt'

def setup_inputs():

    _w = (lambda a: (lambda t: t.to(torch.bfloat16) if t.is_floating_point() else t)(torch.as_tensor(a)))
    def _A_setup():
        B, H, Sq, Sk, D, M = 1, 32, 2048, 2048, 128, 576
        Dc = 4 * D
        return {
            "x": _w((lambda *s: torch.randn(*s, dtype=torch.bfloat16) * 0.05)(B, Sq, M)),
            "c_kv": _w((lambda *s: torch.randn(*s, dtype=torch.bfloat16) * 0.4)(B, Sk, Dc)),
            "w_q": _w((lambda *s: torch.randn(*s, dtype=torch.bfloat16) * 1.6)(M, H, D) * (M ** -0.5)),
            "w_uk": _w((lambda *s: torch.randn(*s, dtype=torch.bfloat16) * 1.6)(Dc, H, D) * (Dc ** -0.5)),
            "w_uv": _w((lambda *s: torch.randn(*s, dtype=torch.bfloat16) * 0.4)(Dc, H, D) * (Dc ** -0.5)),
            "w_o": _w((lambda *s: torch.randn(*s, dtype=torch.bfloat16) * 0.4)(H, D, M) * ((H * D) ** -0.5)),
        }
    _A_in = _A_setup()

    def _B_setup():
        B, S, D, F = 1, 2048, 576, 4608
        return {
            "x": _w((lambda *s: torch.randn(*s, dtype=torch.bfloat16) * 0.05)(B, S, D)),
            "w_gate": _w((lambda *s: torch.randn(*s, dtype=torch.bfloat16) * 0.05)(D, F)),
            "w_up": _w((lambda *s: torch.randn(*s, dtype=torch.bfloat16) * 0.05)(D, F)),
            "w_down": _w((lambda *s: torch.randn(*s, dtype=torch.bfloat16) * 0.05)(F, D)),
        }
    _B_in = _B_setup()

    return {
        "A_x": _A_in["x"],
        "A_c_kv": _A_in["c_kv"],
        "A_w_q": _A_in["w_q"],
        "A_w_uk": _A_in["w_uk"],
        "A_w_uv": _A_in["w_uv"],
        "A_w_o": _A_in["w_o"],
        "B_x": _B_in["x"],
        "B_w_gate": _B_in["w_gate"],
        "B_w_up": _B_in["w_up"],
        "B_w_down": _B_in["w_down"],
    }

def reference(A_x, A_c_kv, A_w_q, A_w_uk, A_w_uv, A_w_o, B_x, B_w_gate, B_w_up, B_w_down):
    def _A_ref(x, c_kv, w_q, w_uk, w_uv, w_o):
        B, Sq, M = x.shape[0], x.shape[1], x.shape[2]
        Sk = c_kv.shape[1]
        H, D = w_q.shape[1], w_q.shape[2]
        q = torch.einsum("bqm,mhd->bhqd", x, w_q)
        k = torch.einsum("bkc,chd->bhkd", c_kv, w_uk)
        v = torch.einsum("bkc,chd->bhkd", c_kv, w_uv)
        scores = torch.einsum("bhqd,bhkd->bhqk", q, k) * (D ** -0.5)
        off = Sk - Sq
        qi = torch.arange(Sq)[:, None]
        kj = torch.arange(Sk)[None, :]
        cmask = kj <= (qi + off)
        scores = torch.where(cmask[None, None], scores, -1e9)
        p = _softmax(scores, axis=-1)
        ctx = torch.einsum("bhqk,bhkd->bhqd", p, v)
        return torch.einsum("bhqd,hdm->bqm", ctx, w_o)

    def _B_ref(x, w_gate, w_up, w_down):
        g = _silu(x @ w_gate)
        u = x @ w_up
        return (g * u) @ w_down

    x = A_x
    h = x + _A_ref(_layernorm(x, axis=-1), A_c_kv, A_w_q, A_w_uk, A_w_uv, A_w_o)
    h2 = h + _B_ref(_layernorm(h, axis=-1), B_w_gate, B_w_up, B_w_down)
    return h2
```

```cpp
#include <hip/hip_runtime.h>
#include <hip/hip_bf16.h>
typedef unsigned short u16;
__device__ __forceinline__ float bf2f(u16 v){ return __uint_as_float(((unsigned)v)<<16); }
__device__ __forceinline__ u16 f2bf(float f){ unsigned u=__float_as_uint(f); u += 0x7fffu + ((u>>16)&1u); return (u16)(u>>16); }
__device__ __forceinline__ float rbf(float f){ return bf2f(f2bf(f)); }

__global__ void ln_k(const u16* x, u16* y, int ncol){
  int row=blockIdx.x; int t=threadIdx.x; __shared__ float red[256];
  float s=0; for(int c=t;c<ncol;c+=256) s+=bf2f(x[row*ncol+c]);
  red[t]=s; __syncthreads(); for(int o=128;o>0;o>>=1){ if(t<o) red[t]+=red[t+o]; __syncthreads(); }
  float mean=red[0]/ncol; __syncthreads();
  float q=0; for(int c=t;c<ncol;c+=256){ float d=bf2f(x[row*ncol+c])-mean; q+=d*d; }
  red[t]=q; __syncthreads(); for(int o=128;o>0;o>>=1){ if(t<o) red[t]+=red[t+o]; __syncthreads(); }
  float var=red[0]/ncol;
  float mb=rbf(mean), vb=rbf(var), den=rbf(sqrtf(rbf(vb+1e-5f)));
  for(int c=t;c<ncol;c+=256){ float d=rbf(bf2f(x[row*ncol+c])-mb); y[row*ncol+c]=f2bf(d/den); }
}
__global__ void gemm_k(const u16* A,const u16* B,u16* C,const u16* res,int M,int N,int K,int mode){
  __shared__ float As[16][65], Bs[16][65];
  int tx=threadIdx.x&15, ty=threadIdx.x>>4; int m0=blockIdx.y*64,n0=blockIdx.x*64;
  float acc[4][4]={};
  for(int k0=0;k0<K;k0+=16){
    for(int i=threadIdx.x;i<1024;i+=256){ int r=i>>4,c=i&15; As[c][r]=bf2f(A[(size_t)(m0+r)*K+k0+c]); int kr=i>>6,nc=i&63; Bs[kr][nc]=bf2f(B[(size_t)(k0+kr)*N+n0+nc]); }
    __syncthreads();
    for(int k=0;k<16;++k){ float a[4],b[4]; for(int i=0;i<4;++i){a[i]=As[k][ty*4+i]; b[i]=Bs[k][tx*4+i];} for(int i=0;i<4;++i)for(int j=0;j<4;++j)acc[i][j]+=a[i]*b[j]; }
    __syncthreads();
  }
  for(int i=0;i<4;++i)for(int j=0;j<4;++j){ size_t o=(size_t)(m0+ty*4+i)*N+n0+tx*4+j; float v=acc[i][j]; if(mode==1) v=bf2f(res[o])+rbf(v); C[o]=f2bf(v);}
}
__global__ void attn_k(const u16* q,const u16* k,const u16* v,u16* ctx){
  int row=blockIdx.x,h=blockIdx.y,t=threadIdx.x; __shared__ float qs[128]; __shared__ float p[2048]; __shared__ float red[128];
  qs[t]=bf2f(q[(size_t)row*4096+h*128+t]); __syncthreads();
  float mx=-1e30f;
  for(int j=t;j<=row;j+=128){ float s=0; const u16* kr=k+(size_t)j*4096+h*128; for(int d=0;d<128;++d) s+=qs[d]*bf2f(kr[d]); s*=0.08838834764831845f; p[j]=s; mx=fmaxf(mx,s);}
  red[t]=mx; __syncthreads(); for(int o=64;o>0;o>>=1){ if(t<o) red[t]=fmaxf(red[t],red[t+o]); __syncthreads(); } mx=red[0]; __syncthreads();
  float l=0; for(int j=t;j<=row;j+=128){ float e=__expf(p[j]-mx); p[j]=e; l+=e; }
  red[t]=l; __syncthreads(); for(int o=64;o>0;o>>=1){ if(t<o) red[t]+=red[t+o]; __syncthreads(); } l=red[0];
  float a=0; for(int j=0;j<=row;++j) a+=p[j]*bf2f(v[(size_t)j*4096+h*128+t]);
  ctx[(size_t)row*4096+h*128+t]=f2bf(a/l);
}
__global__ void swiglu_k(const u16* g,const u16* u,u16* a,int n){ int i=blockIdx.x*256+threadIdx.x; if(i<n){ float x=bf2f(g[i]); float s=rbf(x/(1.f+__expf(-x))); a[i]=f2bf(s*bf2f(u[i])); } }

extern "C" void kernel_launch(void* const* d_in, const int* in_sizes, int n_in, void* d_out, int out_size, void* d_ws, size_t ws_size, hipStream_t stream){
  const u16* x=(const u16*)d_in[0]; const u16* ckv=(const u16*)d_in[1]; const u16* wq=(const u16*)d_in[2]; const u16* wuk=(const u16*)d_in[3]; const u16* wuv=(const u16*)d_in[4]; const u16* wo=(const u16*)d_in[5];
  const u16* wg=(const u16*)d_in[7]; const u16* wu=(const u16*)d_in[8]; const u16* wd=(const u16*)d_in[9];
  u16* ws=(u16*)d_ws; size_t o=0;
  u16* xn=ws+o; o+=2048*576; u16* q=ws+o; o+=2048*4096; u16* k=ws+o; o+=2048*4096; u16* v=ws+o; o+=2048*4096; u16* ctx=ws+o; o+=2048*4096;
  u16* h=ws+o; o+=2048*576; u16* hn=ws+o; o+=2048*576; u16* g=ws+o; o+=2048*4608; u16* u=ws+o; o+=2048*4608; u16* a=ws+o; o+=2048*4608;
  ln_k<<<2048,256,0,stream>>>(x,xn,576);
  gemm_k<<<dim3(64,32),256,0,stream>>>(xn,wq,q,nullptr,2048,4096,576,0);
  gemm_k<<<dim3(64,32),256,0,stream>>>(ckv,wuk,k,nullptr,2048,4096,512,0);
  gemm_k<<<dim3(64,32),256,0,stream>>>(ckv,wuv,v,nullptr,2048,4096,512,0);
  attn_k<<<dim3(2048,32),128,0,stream>>>(q,k,v,ctx);
  gemm_k<<<dim3(9,32),256,0,stream>>>(ctx,wo,h,x,2048,576,4096,1);
  ln_k<<<2048,256,0,stream>>>(h,hn,576);
  gemm_k<<<dim3(72,32),256,0,stream>>>(hn,wg,g,nullptr,2048,4608,576,0);
  gemm_k<<<dim3(72,32),256,0,stream>>>(hn,wu,u,nullptr,2048,4608,576,0);
  swiglu_k<<<(2048*4608+255)/256,256,0,stream>>>(g,u,a,2048*4608);
  gemm_k<<<dim3(9,32),256,0,stream>>>(a,wd,(u16*)d_out,h,2048,576,4608,1);
}
```

```cpp
#include <hip/hip_runtime.h>
#include <utility>
typedef unsigned short u16;
typedef short bf16x8 __attribute__((ext_vector_type(8)));
typedef short s16x4 __attribute__((ext_vector_type(4)));
typedef float f32x16 __attribute__((ext_vector_type(16)));
typedef float f32x4 __attribute__((ext_vector_type(4)));
typedef float f32x2 __attribute__((ext_vector_type(2)));
typedef unsigned u32x4 __attribute__((ext_vector_type(4)));
typedef unsigned u32x2 __attribute__((ext_vector_type(2)));
typedef __bf16 bf16x2_t __attribute__((ext_vector_type(2)));
#define LAS __attribute__((address_space(3)))
#define DI __device__ __forceinline__
#define SBAR() __builtin_amdgcn_sched_barrier(0)
#define MFMA32(a, b, c) __builtin_amdgcn_mfma_f32_32x32x16_bf16((a), (b), (c), 0, 0, 0)
#define MFMA16(a, b, c) __builtin_amdgcn_mfma_f32_16x16x32_bf16((a), (b), (c), 0, 0, 0)
#define MFMA16_ACC(c, a, b) asm volatile("v_mfma_f32_16x16x32_bf16 %0, %1, %2, %0" : "+v"(c) : "v"(a), "v"(b))
#define MFMA16_NEW(c, a, b) asm volatile("v_mfma_f32_16x16x32_bf16 %0, %1, %2, 0" : "=v"(c) : "v"(a), "v"(b))
template <int... Is> using iseq = std::integer_sequence<int, Is...>;
template <int N> using mkseq = std::make_integer_sequence<int, N>;
typedef __amdgpu_buffer_rsrc_t srd_t;

DI unsigned rfl(unsigned x) { return __builtin_amdgcn_readfirstlane(x); }
DI float bf2f(unsigned v) { return __uint_as_float(v << 16); }
DI float bflo(unsigned w) { return __uint_as_float(w << 16); }
DI float bfhi(unsigned w) { return __uint_as_float(w & 0xffff0000u); }
DI unsigned cvtpk(float lo, float hi) { f32x2 v = {lo, hi}; bf16x2_t b = __builtin_convertvector(v, bf16x2_t); return __builtin_bit_cast(unsigned, b); }
DI float rbf(float f) { return bflo(cvtpk(f, f)); }
DI srd_t mksrd(const void* p, unsigned bytes) { return __builtin_amdgcn_make_buffer_rsrc((void*)p, (short)0, (int)bytes, 0x00020000); }
DI void dma16(unsigned m0v, unsigned voff, srd_t srd, unsigned soff) {
  asm volatile("s_mov_b32 m0, %0\n\ts_nop 0\n\tbuffer_load_dwordx4 %1, %2, %3 offen lds" ::"s"(m0v), "v"(voff), "s"(srd), "s"(soff) : "memory");
}
template <int N> DI void wait_vm() { asm volatile("s_waitcnt vmcnt(%0)" ::"i"(N) : "memory"); }
template <int N> DI void wait_bar() { asm volatile("s_waitcnt vmcnt(%0) lgkmcnt(0)\n\ts_barrier" ::"i"(N) : "memory"); }
DI bf16x8 lds128(const LAS char* p) { return *(const LAS bf16x8*)p; }
typedef short v4i16_t __attribute__((ext_vector_type(4)));
DI s16x4 ldstr(const LAS char* p) { return __builtin_bit_cast(s16x4, __builtin_amdgcn_ds_read_tr16_b64_v4i16((LAS v4i16_t*)p)); }
DI bf16x8 cat8(s16x4 a, s16x4 b) { return __builtin_shufflevector(a, b, 0, 1, 2, 3, 4, 5, 6, 7); }
DI u32x4 pack8(const f32x16& x, int s) {
  u32x4 w = {cvtpk(x[8 * s], x[8 * s + 1]), cvtpk(x[8 * s + 2], x[8 * s + 3]), cvtpk(x[8 * s + 4], x[8 * s + 5]), cvtpk(x[8 * s + 6], x[8 * s + 7])};
  return w;
}
#define ST_AUX ""
DI void st128(u32x4 v, srd_t srd, unsigned voff, unsigned soff) {
  asm volatile("buffer_store_dwordx4 %0, %1, %2, %3 offen" ST_AUX "\n\ts_nop 1" ::"v"(v), "v"(voff), "s"(srd), "s"(soff) : "memory");
}
template <int CTRL> DI float dpp_add(float x) { return x + __builtin_bit_cast(float, __builtin_amdgcn_update_dpp(0, __builtin_bit_cast(int, x), CTRL, 0xf, 0xf, true)); }
DI float swap32_add(float x) { auto r = __builtin_amdgcn_permlane32_swap(__float_as_uint(x), __float_as_uint(x), false, false); return __uint_as_float(r[0]) + __uint_as_float(r[1]); }
DI float swap16_add(float x) { auto r = __builtin_amdgcn_permlane16_swap(__float_as_uint(x), __float_as_uint(x), false, false); return __uint_as_float(r[0]) + __uint_as_float(r[1]); }
DI float swap32_max(float x) { auto r = __builtin_amdgcn_permlane32_swap(__float_as_uint(x), __float_as_uint(x), false, false); return __builtin_fmaxf(__uint_as_float(r[0]), __uint_as_float(r[1])); }
DI float wave_sum(float x) {
  x = dpp_add<0xB1>(x); x = dpp_add<0x4E>(x); x = dpp_add<0x141>(x); x = dpp_add<0x140>(x);
  x = swap16_add(x); return swap32_add(x);
}

constexpr int SEQ = 2048, DM = 576, DC = 512, NH = 32, HD = 128, FF = 4608, HDM = NH * HD;
constexpr size_t WS_XN = 0;
constexpr size_t WS_KF = WS_XN + (size_t)SEQ * DM * 2;
constexpr size_t WS_VF = WS_KF + (size_t)SEQ * HDM * 2;
constexpr size_t WS_CTX = WS_VF + (size_t)SEQ * HDM * 2;
constexpr size_t WS_QS = WS_CTX + (size_t)SEQ * HDM * 2;
constexpr size_t WS_WO = WS_QS + (size_t)256 * 32768;
constexpr size_t WS_WGU = WS_WO + (size_t)HDM * DM * 2;
constexpr size_t WS_WD = WS_WGU + (size_t)DM * FF * 4;
constexpr size_t WS_H = WS_WD + (size_t)FF * DM * 2;
constexpr size_t WS_ST = WS_H + (size_t)SEQ * DM * 2;
constexpr size_t WS_CS = WS_ST + (size_t)SEQ * 128;
constexpr size_t WS_ACT = WS_CS + (size_t)288 * 128;
constexpr size_t WS_END = WS_ACT + (size_t)SEQ * FF * 2;

#define TSTAMP(s) do {} while (0)

namespace k1 {
constexpr int SLOT = 49152, XSCR = 3 * SLOT, LDS_BYTES = XSCR + 9216, LA = 3  , BB = 2  ;
constexpr int cstart(int c) { return c <= 8 ? 16 * c : 128 + 32 * (c - 8); }
constexpr int chunk_of(int g) { return g < 128 ? g >> 4 : 8 + ((g - 128) >> 5); }
constexpr int bslot(int c) { return cstart(c) - BB; }
constexpr int dcnt(int c) { return c < 8 ? 6 : (c < 12 ? 4 : 0); }
constexpr int kt_of(int g) { const int c = chunk_of(g), i = g - cstart(c); return c < 7 ? (i & 7) : c == 7 ? (i >> 1) : (i & 1); }
constexpr int sloc_of(int g) { const int c = chunk_of(g), i = g - cstart(c); return c < 7 ? (i >> 3) : c == 7 ? (i & 1) : (i >> 1); }
constexpr int kstep_of(int g) { const int c = chunk_of(g); return c < 8 ? 2 * c + sloc_of(g) : sloc_of(g); }
constexpr bool w_first(int g) { const int c = chunk_of(g), i = g - cstart(c); return c < 7 ? (i & 7) == 0 : (c == 7 && i < 2); }
constexpr int acc_of(int g) { const int c = chunk_of(g); return c < 8 ? kt_of(g) : 8 + 2 * (c - 8) + kt_of(g); }
constexpr int dma_at(int g) { for (int c = 0; c < 10; ++c) { const int j = g - (c ? bslot(c) : 0); if (j >= 0 && j < dcnt(c + 2)) return (c + 2) * 8 + j; } return -1; }
constexpr int pk_at(int g) {
  for (int u = 0; u < 4; ++u) { if (g == 112 + 4 * u + 6) return 2 * u; if (g == 112 + 4 * u + 7) return 2 * u + 1; }
  for (int u = 4; u < 7; ++u) { if (g == cstart(u + 5) + 3) return 2 * u; if (g == cstart(u + 5) + 4) return 2 * u + 1; }
  return -1;
}
constexpr int st_at(int g) {
  if (g < 128 || (g & 7) != 5) return -1;
  constexpr int order[16] = {0, 1, 2, 3, 8, 4, 9, 5, 10, 6, 11, 7, 12, 13, -1, -1};
  return order[(g - 128) >> 3];
}
constexpr int LN0 = 6, NLN = 48, LNRD = 72, LNST = 76;
constexpr int ln_at(int g) { return (g >= LN0 && g < LN0 + NLN) ? g - LN0 : -1; }
constexpr int WARM = 163, CS0 = 192, CSM = 238;
constexpr int cs_at(int g) { return (g >= CS0 && g < CS0 + 36 && ((g - CS0) & 3) == 0) ? (g - CS0) >> 2 : -1; }
constexpr int late_ops(int g) { return (st_at(g) >= 0 ? 1 : 0) + (g == LNST ? 2 : 0) + (g == WARM ? 1 : 0) + (cs_at(g) >= 0 ? 1 : 0); }
constexpr int ops_between(int from, int to) { int n = 0; for (int g = from + 1; g < to; ++g) n += (dma_at(g) >= 0 ? 1 : 0) + late_ops(g); return n; }
constexpr int vm_wait(int c) {
  if (c == 0) return 6;
  if (c == 1) return ops_between(-1, bslot(1));
  const int from = (c == 2 ? 0 : bslot(c - 2)) + dcnt(c) - 1;
  return late_ops(from) + ops_between(from, bslot(c));
}
struct St {
  bf16x8 wf[16][2]; bf16x8 af[256]; f32x4 acc[16][2]; u32x4 pk[16];
  unsigned wp, ap[4];
  unsigned wb, ab0, ab1, voffA0, voffA1[4], voffW, m0w, sA, sW, vst, sO;
  srd_t rA, rK, rV, rO, rXN;
  u32x4 x0, x1; float v[16], t0, t1, t2; unsigned lane, sXN; bool two;
  srd_t rWm, rCs; unsigned sWm, sCs; bf16x8 cw[9], sel; f32x4 ca;
};
template <int C, int J> DI void dma(St& s) {
  constexpr unsigned slot = (C % 3) * SLOT;
  if constexpr (C < 8) {
    if constexpr (J < 2) dma16(s.m0w * 2048u + slot + 32768u + J * 1024u, s.voffA0, s.rA, s.sA + s.m0w * 16384u + J * 8192u + C * 128u);
    else {
      constexpr unsigned mat = (J >> 1) & 1, jj = J & 1;
      dma16(s.m0w * 2048u + slot + mat * 16384u + jj * 1024u, s.voffW, mat ? s.rV : s.rK, s.sW + s.m0w * 65536u + jj * 32768u + C * 524288u);
    }
  } else dma16(s.m0w * 4096u + slot + J * 1024u, s.voffA1[J], s.rA, s.sA + (128u + (C - 8) * 32u + J) * 1024u + s.m0w * 4096u);
}
DI const LAS char* lp(unsigned a) { return (const LAS char*)(size_t)a; }
template <int G> DI void reads(St& s) {
  if constexpr (G < 256) {
    constexpr int c = chunk_of(G), kt = kt_of(G), sl = sloc_of(G), ks = kstep_of(G);
    if constexpr (w_first(G)) {
      s.wf[ks][0] = cat8(ldstr(lp(s.wp) + sl * 8192), ldstr(lp(s.wp) + sl * 8192 + 1024));
      s.wf[ks][1] = cat8(ldstr(lp(s.wp ^ 16u) + sl * 8192), ldstr(lp(s.wp ^ 16u) + sl * 8192 + 1024));
    }
    if constexpr (c < 8) s.af[G] = lds128(lp(s.ap[sl]) + (kt >> 1) * 4096 + (kt & 1) * 1024);
    else s.af[G] = lds128(lp(s.ap[sl & 3]) + (sl >> 2) * 256 + kt * 8192);
  }
}
template <int C> DI void set_bases(St& s) {
  constexpr unsigned slot = (C % 3) * SLOT;
  if constexpr (C < 8) {
    unsigned w = s.wb + slot, a0 = s.ab0 + slot, a1 = (s.ab0 ^ 64u) + slot;
    asm volatile("" : "+v"(w), "+v"(a0), "+v"(a1));
    s.wp = w; s.ap[0] = a0; s.ap[1] = a1;
  } else {
    unsigned a0 = s.ab1 + slot, a1 = (s.ab1 ^ 64u) + slot, a2 = (s.ab1 ^ 128u) + slot, a3 = (s.ab1 ^ 192u) + slot;
    asm volatile("" : "+v"(a0), "+v"(a1), "+v"(a2), "+v"(a3));
    s.ap[0] = a0; s.ap[1] = a1; s.ap[2] = a2; s.ap[3] = a3;
  }
}
template <bool ISV, int ID> DI void pack(St& s) {
  constexpr int u = ID >> 1, j = ID & 1;
  const f32x4 a = ISV ? s.acc[2 * u][j] : s.acc[2 * u + j][0], b = ISV ? s.acc[2 * u + 1][j] : s.acc[2 * u + j][1];
  s.pk[ID] = u32x4{cvtpk(a[0], a[1]), cvtpk(a[2], a[3]), cvtpk(b[0], b[1]), cvtpk(b[2], b[3])};
  asm volatile("" : "+v"(s.pk[ID]));
}
template <int ID> DI void store(St& s) { st128(s.pk[ID], s.rO, s.vst, s.sO + (ID >> 1) * 8192u + (ID & 1) * 128u); }
template <int ST> DI void ln_stage(St& s) {
  if constexpr (ST < 4) {
    if constexpr (ST == 0) { asm volatile("" : "+v"(s.x0), "+v"(s.x1)); s.t0 = 0.f; s.t2 = 0.f; }
    s.v[2 * ST] = bflo(s.x0[ST]); s.v[2 * ST + 1] = bfhi(s.x0[ST]); s.t0 += s.v[2 * ST] + s.v[2 * ST + 1];
  } else if constexpr (ST < 8) { constexpr int i = ST - 4; s.v[8 + 2 * i] = bflo(s.x1[i]); s.v[9 + 2 * i] = bfhi(s.x1[i]); s.t2 += s.v[8 + 2 * i] + s.v[9 + 2 * i]; }
  else if constexpr (ST == 8) s.t0 = dpp_add<0xB1>(s.t0 + (s.two ? s.t2 : 0.f));
  else if constexpr (ST == 9) s.t0 = dpp_add<0x141>(dpp_add<0x4E>(s.t0));
  else if constexpr (ST == 10) s.t0 = dpp_add<0x140>(s.t0);
  else if constexpr (ST == 11) s.t0 = swap16_add(s.t0);
  else if constexpr (ST == 12) { s.t0 = swap32_add(s.t0) * (1.f / DM); s.t1 = 0.f; s.t2 = 0.f; }
  else if constexpr (ST < 21) { constexpr int i = 2 * (ST - 13); float& q = i < 8 ? s.t1 : s.t2; const float d = s.v[i] - s.t0, e = s.v[i + 1] - s.t0; q += d * d; q += e * e; }
  else if constexpr (ST == 21) s.t1 = dpp_add<0xB1>(s.t1 + (s.two ? s.t2 : 0.f));
  else if constexpr (ST == 22) s.t1 = dpp_add<0x141>(dpp_add<0x4E>(s.t1));
  else if constexpr (ST == 23) s.t1 = dpp_add<0x140>(s.t1);
  else if constexpr (ST == 24) s.t1 = swap16_add(s.t1);
  else if constexpr (ST == 25) s.t1 = swap32_add(s.t1) * (1.f / DM);
  else if constexpr (ST == 26) { s.t0 = rbf(s.t0); s.t1 = rbf(s.t1); }
  else if constexpr (ST == 27) s.t1 = rbf(s.t1 + 1e-5f);
  else if constexpr (ST == 28) s.t1 = rbf(__builtin_amdgcn_sqrtf(s.t1));
  else if constexpr (ST == 29) s.t1 = __builtin_amdgcn_rcpf(s.t1);
  else if constexpr (ST < 46) {
    constexpr int i = (ST - 30) >> 1; u32x4& o = i < 4 ? s.x0 : s.x1;
    if constexpr (((ST - 30) & 1) == 0) o[i & 3] = cvtpk(s.v[2 * i] - s.t0, s.v[2 * i + 1] - s.t0);
    else o[i & 3] = cvtpk(bflo(o[i & 3]) * s.t1, bfhi(o[i & 3]) * s.t1);
    asm volatile("" : "+v"(o));
  } else if constexpr (ST == 46) *(LAS u32x4*)(size_t)(XSCR + s.lane * 128u + ((s.m0w ^ (s.lane & 7)) << 4)) = s.x0;
  else { if (s.two) *(LAS u32x4*)(size_t)(XSCR + 8192u + s.lane * 128u + ((s.m0w ^ (s.lane & 7)) << 4)) = s.x1; }
  if constexpr (ST < 30) asm volatile("" : "+v"(s.t0), "+v"(s.t1), "+v"(s.t2));
}
template <bool ISV, int G> DI void slot(St& s) {
  constexpr int cn = chunk_of(G) + 1;
  constexpr bool isbar = cn < 12 && G == bslot(cn);
  if constexpr (isbar) {
    wait_bar<vm_wait(cn)>(); set_bases<cn>(s); SBAR();
    reads<G + BB>(s); reads<G + BB + 1>(s);
  } else if constexpr (!(cn < 12 && G + LA >= cstart(cn) && G < bslot(cn))) reads<G + LA>(s);
  if constexpr (dma_at(G) >= 0) dma<(dma_at(G) >> 3), (dma_at(G) & 7)>(s);
  constexpr int ai = acc_of(G), ks = kstep_of(G);
#pragma unroll
  for (int ct = 0; ct < 2; ++ct) {
    const bf16x8 a = ISV ? s.af[G] : s.wf[ks][ct], bq = ISV ? s.wf[ks][ct] : s.af[G];
    if constexpr (ks == 0) MFMA16_NEW(s.acc[ai][ct], a, bq); else MFMA16_ACC(s.acc[ai][ct], a, bq);
  }
  if constexpr (pk_at(G) >= 0) pack<ISV, pk_at(G)>(s);
  if constexpr (st_at(G) >= 0) store<st_at(G)>(s);
  if constexpr (ln_at(G) >= 0) ln_stage<ln_at(G)>(s);
  if constexpr (G == LNRD) {
    s.x0 = *(const LAS u32x4*)(size_t)(XSCR + s.m0w * 1024u + s.lane * 16u); s.x1 = *(const LAS u32x4*)(size_t)(XSCR + 8192u + s.lane * 16u);
  }
  if constexpr (G == LNST) {
    const unsigned vo = (s.lane >> 3) * 512u + (((s.lane ^ (s.lane >> 3)) & 7) << 4);
    st128(s.x0, s.rXN, vo, s.sXN + s.m0w * 4096u); st128(s.x1, s.rXN, vo, s.m0w == 0 ? s.sXN + 32768u : 0x10000000u);
  }
  if constexpr (G == WARM)
    asm volatile("s_mov_b32 m0, %0\n\ts_nop 0\n\tbuffer_load_dword %1, %2, %3 offen lds" ::"s"(XSCR + 4096u + s.m0w * 256u), "v"(s.lane * 128u), "s"(s.rWm), "s"(s.sWm) : "memory");
  if constexpr (cs_at(G) >= 0)
    asm volatile("buffer_load_dwordx4 %0, %1, %2, %3 offen" : "=v"(s.cw[cs_at(G)]) : "v"((s.lane >> 3) * (FF * 2u) + (s.lane & 7) * 16u), "s"(s.rCs), "s"(s.sCs + cs_at(G) * 8u * (FF * 2u)) : "memory");
  if constexpr (G == CSM - 1) {
    u32x4 e;
#pragma unroll
    for (int i = 0; i < 4; ++i) e[i] = ((s.lane & 15) == 2 * i ? 0x3f80u : 0u) | ((s.lane & 15) == 2 * i + 1 ? 0x3f800000u : 0u);
    s.sel = __builtin_bit_cast(bf16x8, e);
    asm volatile("s_waitcnt vmcnt(%9)" : "+v"(s.cw[0]), "+v"(s.cw[1]), "+v"(s.cw[2]), "+v"(s.cw[3]), "+v"(s.cw[4]), "+v"(s.cw[5]), "+v"(s.cw[6]), "+v"(s.cw[7]), "+v"(s.cw[8])
                 : "i"(ops_between(CS0 + 32, CSM)) : "memory");
  }
  if constexpr (G >= CSM && G < CSM + 9) { const f32x4 z4 = {}; s.ca = MFMA16(s.cw[G - CSM], s.sel, G == CSM ? z4 : s.ca); }
  if constexpr (G == CSM + 12) {
    f32x4 t;
#pragma unroll
    for (int i = 0; i < 4; ++i) t[i] = swap32_add(s.ca[i]);
    if ((s.lane & 40) == 0) {
#pragma unroll
      for (int i = 0; i < 4; ++i) *(LAS float*)(size_t)(XSCR + s.m0w * 256u + (s.lane >> 4) * 128u + (s.lane & 7) * 4u + i * 32u) = t[i];
    }
  }
  SBAR();
}
template <bool ISV, int... Gs> DI void run(St& s, iseq<Gs...>) {
  reads<0>(s); reads<1>(s); reads<2>(s); SBAR();
  (slot<ISV, Gs>(s), ...);
  asm volatile("s_nop 7\n\ts_nop 3" ::: "memory"); SBAR();
  pack<ISV, 14>(s); store<14>(s); pack<ISV, 15>(s); store<15>(s);
}
template <int C, int... Js> DI void dma_all(St& s, iseq<Js...>) { (dma<C, Js>(s), ...); }
}

__global__ __launch_bounds__(512, 2) void k1_kvproj(const u16* __restrict__ ckv, const u16* __restrict__ wuk, const u16* __restrict__ wuv, const u16* __restrict__ x,
    const u16* __restrict__ wq, const u16* __restrict__ wo, const u16* __restrict__ wg, const u16* __restrict__ wu, const u16* __restrict__ wd, char* __restrict__ ws) {
  using namespace k1;
  const unsigned tid = threadIdx.x, lane = tid & 63, w = rfl(tid >> 6), r = lane & 15, g = lane >> 4;
  const unsigned b = blockIdx.x, xcd = b & 7, ix = b >> 3, h = xcd * 4 + (ix & 3), rb = ix >> 2;
  St s;
  s.rA = mksrd(ckv, SEQ * DC * 2); s.rK = mksrd(wuk, DC * HDM * 2); s.rV = mksrd(wuv, DC * HDM * 2);
  s.rO = mksrd(ws + (w < 4 ? WS_KF : WS_VF), SEQ * HDM * 2); s.rXN = mksrd(ws + WS_XN, SEQ * DM * 2);
  s.m0w = w; s.two = lane < 8; s.lane = lane; s.sXN = (b >> 2) * 36864u + (b & 3) * 128u;
  const unsigned q2 = (lane >> 2) & 3, p2 = lane & 3, rrow = 16u * (r >> 3) + (r & 7);
  s.voffA0 = (lane >> 3) * 1024u + (((lane & 7) ^ (4u * (w & 1) + g)) << 4);
#pragma unroll
  for (int j = 0; j < 4; ++j) s.voffA1[j] = (lane ^ (8u * (w >> 2) + ((4u * w + j) & 7u))) << 4;
  s.voffW = q2 * 8192u + g * 64u + ((p2 ^ (w & 1u)) << 4);
  s.sA = rb * 256u * 1024u; s.sW = h * 256u;
  s.wb = (w >> 2) * 16384u + (w & 3) * 256u + g * 2048u + q2 * 64u + (p2 >> 1) * 32u + (g & 1) * 16u + (p2 & 1) * 8u;
  s.ab0 = 32768u + rrow * 128u + ((g ^ (r >> 1)) << 4);
  s.ab1 = rrow * 1024u + ((g ^ r) << 4);
  s.vst = (g >> 1) * (w < 4 ? 1024u : 4096u) + (rrow + 32u * (g & 1)) * 16u;
  s.sO = (h * 64u + rb * 8u) * 8192u + (w < 4 ? w * 2048u : (w - 4) * 1024u);
  {
    const unsigned W = b * 8u + w;
    s.rWm = mksrd(W < 576u ? wq : W < 1152u ? wo : wd, W < 1152u ? DM * HDM * 2 : FF * DM * 2);
    s.sWm = (W < 576u ? W : W < 1152u ? W - 576u : W - 1152u) * 8192u;
    s.rCs = mksrd(b < 72u ? wg : wu, DM * FF * 2);
    s.sCs = b < 144u ? (b < 72u ? b : b - 72u) * 128u + w * 72u * (FF * 2u) : 0x10000000u;
  }
  {
    const srd_t rX = mksrd(x, SEQ * DM * 2);
    asm volatile("buffer_load_dwordx4 %0, %2, %4, %5 offen\n\tbuffer_load_dwordx4 %1, %3, %4, %5 offen offset:1024"
                 : "=&v"(s.x0), "=&v"(s.x1) : "v"(lane * 16u), "v"((lane & 7) * 16u), "s"(rX), "s"((b * 8u + w) * (DM * 2u)) : "memory");
  }
  dma_all<0>(s, mkseq<6>{});
  __builtin_amdgcn_s_barrier();
  dma_all<1>(s, mkseq<6>{});
  wait_bar<vm_wait(0)>(); set_bases<0>(s); SBAR();
  if (w < 4) run<false>(s, mkseq<256>{}); else run<true>(s, mkseq<256>{});
  asm volatile("s_waitcnt lgkmcnt(0)\n\ts_barrier" ::: "memory");
  if (w == 0 && b < 144u) {
    float t = 0.f;
#pragma unroll
    for (int i = 0; i < 8; ++i) t += *(const LAS float*)(size_t)(XSCR + i * 256u + lane * 4u);
    *(float*)(ws + WS_CS + (b * 64u + lane) * 4u) = t;
  }
}

#define ALA 3
#define EXP2(x) __builtin_amdgcn_exp2f(x)
namespace k2 {
constexpr unsigned KR = 0, VR = 65536, SP = 131072, QR = 98304  , LDS_BYTES = 163840;
constexpr float THR = 8.f;
constexpr float QSCALE = 0.08838834764831845f * 1.4426950408889634f;
DI const LAS char* lp(unsigned a) { return (const LAS char*)(size_t)a; }
DI float max3f(float a, float b, float c) { float r; asm("v_max3_f32 %0, %1, %2, %3" : "=v"(r) : "v"(a), "v"(b), "v"(c)); return r; }
DI float fadd_s(float a, float b) { float r; asm("v_add_f32_e32 %0, %1, %2" : "=v"(r) : "v"(a), "v"(b)); return r; }
DI float fsub_s(float a, float b) { float r; asm("v_sub_f32_e32 %0, %1, %2" : "=v"(r) : "v"(a), "v"(b)); return r; }
DI float fmul_s(float a, float b) { float r; asm("v_mul_f32_e32 %0, %1, %2" : "=v"(r) : "v"(a), "v"(b)); return r; }

constexpr int QLA = 3, QBB = 2;
constexpr int qb(int c) { return 16 * c - QBB; }
constexpr int qfirst(int c) { return c == 2 ? 0 : c == 3 ? 6 : qb(c - 3); }
constexpr int qop_chunk(int g) { for (int c = 2; c < 9; ++c) { const int j = g - qfirst(c); if (j >= 0 && j < 6) return c * 8 + j; } return -1; }
constexpr int qkv_at(int g) { return (g >= 88 && g < 136 && ((g - 88) & 3) == 0) ? (g - 88) >> 2 : -1; }
constexpr int qops(int g) { return (qop_chunk(g) >= 0) + (qkv_at(g) >= 0); }
constexpr int qwait(int c) {
  if (c == 0) return 6;
  int n = 0; const int from = c == 1 ? 0 : qfirst(c) + 6;
  for (int g = from; g < qb(c); ++g) n += qops(g);
  return n;
}
struct St {
  bf16x8 xf[36], wf[144];
  bf16x8 qf[8], fr[40]; f32x16 o[4], sc[2][2], negm; u32x4 pf[4];
  float m, l, ls[2], mx0, mx1, f; bool pend;
  unsigned w, lane, vl16, wq_rd, vW, vX, sX, sW, kb, vb, sKV, m0k, m0v, kso, vso;
  srd_t rW, rX, rK, rV, rC;
  unsigned tsv, nts;
};
template <int C, int J> DI void qload(St& s) {
  if constexpr (J < 2) dma16(QR + (C & 3) * 16384u + s.w * 2048u + J * 1024u, s.vW, s.rW, s.sW + (C * 64u + J * 4u) * 8192u);
  else asm volatile("buffer_load_dwordx4 %0, %1, %2, %3 offen offset:%4" : "=v"(s.xf[4 * C + J - 2]) : "v"(s.vX), "s"(s.rX), "s"(s.sX + C * 4096u), "i"((J - 2) * 1024) : "memory");
}
template <int C> DI void qbar(St& s) {
  asm volatile("s_waitcnt vmcnt(%4) lgkmcnt(0)\n\ts_barrier" : "+v"(s.xf[4 * C]), "+v"(s.xf[4 * C + 1]), "+v"(s.xf[4 * C + 2]), "+v"(s.xf[4 * C + 3]) : "i"(qwait(C)) : "memory");
}
constexpr int q_t(int g) { return g < 128 ? (g & 3) : ((g - 128) >> 2); }
constexpr int q_s(int g) { return g < 128 ? ((g >> 2) & 3) : (g & 3); }
template <int G> DI void qread(St& s) {
  if constexpr (G < 144) { constexpr int off = ((G >> 4) & 3) * 16384 + q_s(G) * 4096 + q_t(G) * 256; s.wf[G] = cat8(ldstr(lp(s.wq_rd) + off), ldstr(lp(s.wq_rd) + off + 1024)); }
}
template <int I> DI void kv_piece(St& s) {
  constexpr unsigned t = I >> 2, j = I & 3;
  dma16((t == 2 ? VR : KR + t * 32768u) + s.w * 4096u + j * 1024u, s.vl16, t == 2 ? s.rV : s.rK, (t == 1 ? s.kso : s.sKV) + j * 1024u);
}
template <int T> DI void qpack(St& s) {
#pragma unroll
  for (int r = 0; r < 16; ++r) s.o[T][r] *= QSCALE;
  s.qf[2 * T] = __builtin_bit_cast(bf16x8, pack8(s.o[T], 0)); s.qf[2 * T + 1] = __builtin_bit_cast(bf16x8, pack8(s.o[T], 1));
  asm volatile("" : "+v"(s.qf[2 * T]), "+v"(s.qf[2 * T + 1]));
}
template <int G> DI void qslot(St& s) {
  constexpr int cn = (G >> 4) + 1;
  if constexpr (cn < 9 && G == qb(cn)) { qbar<cn>(s); SBAR(); if constexpr (cn == 3 || cn == 6) TSTAMP(s); qread<G + 2>(s); qread<G + 3>(s); }
  else if constexpr (!(cn < 9 && G + QLA >= 16 * cn && G < qb(cn))) qread<G + QLA>(s);
  if constexpr (qop_chunk(G) >= 0) qload<(qop_chunk(G) >> 3), (qop_chunk(G) & 7)>(s);
  if constexpr (qkv_at(G) >= 0) kv_piece<qkv_at(G)>(s);
  constexpr int ks = 4 * (G >> 4) + q_s(G), t = q_t(G);
  const f32x16 z = {};
  s.o[t] = MFMA32(s.wf[G], s.xf[ks], ks == 0 ? z : s.o[t]);
  if constexpr (G >= 134 && ((G - 134) & 3) == 0) qpack<((G - 134) >> 2)>(s);
  SBAR();
}
template <int... Gs> DI void qrun(St& s, iseq<Gs...>) { (qslot<Gs>(s), ...); }
template <int C, int... Js> DI void qload_all(St& s, iseq<Js...>) { (qload<C, Js>(s), ...); }

template <int I> DI void frag(St& s) {
  if constexpr (I < 16) s.fr[I] = lds128(lp(s.kb) + (I & 1) * 8192 + (I >> 1) * 1024);
  else if constexpr (I < 32) s.fr[I] = lds128(lp(s.vb) + (I - 16) * 1024);
  else s.fr[I] = lds128(lp(s.kb) + ((I - 32) & 1) * 8192 + ((I - 32) >> 1) * 1024);
}
template <int J> DI void kvdma(St& s) {
  asm volatile("s_mov_b32 m0, %0\n\ts_nop 0\n\tbuffer_load_dwordx4 %1, %2, %3 offen offset:%4 lds" ::"s"(J < 4 ? s.m0k : s.m0v), "v"(s.vl16), "s"(J < 4 ? s.rK : s.rV), "s"(J < 4 ? s.kso : s.vso), "i"((J & 3) * 1024) : "memory");
}
DI unsigned opaque_lane() { unsigned l; asm volatile("v_mbcnt_lo_u32_b32 %0, -1, 0\n\tv_mbcnt_hi_u32_b32 %0, -1, %0" : "=v"(l)); return l; }
template <int X> DI void mask_diag(St& s) {
  const unsigned ln = opaque_lane();
  const int dq = 32 * ((int)(s.w & 3) - 2 * (int)(s.w >> 2)) + (int)(ln & 31) - 4 * (int)(ln >> 5);
#pragma unroll
  for (int kb = 0; kb < 2; ++kb)
#pragma unroll
    for (int r = 0; r < 16; ++r) { const int c = 32 * kb + (r & 3) + 8 * (r >> 2); s.sc[X][kb][r] = c > dq ? -__builtin_inff() : s.sc[X][kb][r]; }
}
DI void sub_ip(f32x16& v, float d) {
#pragma unroll
  for (int r = 0; r < 16; ++r) { float e = v[r]; asm("v_sub_f32_e32 %0, %0, %1" : "+v"(e) : "v"(d)); v[r] = e; }
}
DI void mul_ip(f32x16& v, float d) {
#pragma unroll
  for (int r = 0; r < 16; ++r) { float e = v[r]; asm("v_mul_f32_e32 %0, %0, %1" : "+v"(e) : "v"(d)); v[r] = e; }
}
template <int X, int E> DI void sexp(St& s) { float e = EXP2(s.sc[X][E >> 4][E & 15]); asm volatile("" : "+v"(e)); s.sc[X][E >> 4][E & 15] = e; }
template <int X, int E> DI void sadd(St& s) { if constexpr (E == 0) s.ls[X] = s.sc[X][0][0]; else { s.ls[X] += s.sc[X][E >> 4][E & 15]; asm volatile("" : "+v"(s.ls[X])); } }
template <int X, int P> DI void scvt(St& s) { unsigned e = cvtpk(s.sc[X][P >> 3][(2 * P) & 15], s.sc[X][P >> 3][(2 * P + 1) & 15]); asm volatile("" : "+v"(e)); s.pf[P >> 2][P & 3] = e; }
template <int X, bool QK, bool PREV, bool LAST, int G> DI void aslot(St& s, unsigned par, unsigned ksoff, unsigned vsoff) {
  constexpr int Y = X ^ 1;
  if constexpr (QK && G == 30) {
    wait_bar<0>();
    TSTAMP(s);
    s.kb ^= 32768u; s.vb ^= 32768u; asm volatile("" : "+v"(s.kb), "+v"(s.vb)); SBAR();
    if constexpr (!LAST) { frag<32>(s); frag<33>(s); if constexpr (ALA > 3) frag<34>(s); if constexpr (ALA > 4) frag<35>(s); }
  } else if constexpr (G + ALA < 16) { if constexpr (QK) frag<G + ALA>(s); }
  else if constexpr (G + ALA < 32) { if constexpr (PREV) frag<G + ALA>(s); }
  else if constexpr (QK && !LAST && G == 31) frag<31 + ALA>(s);
  if constexpr (G < 16) { if constexpr (QK) { const f32x16 z = {}; s.sc[X][G & 1] = MFMA32(s.fr[G], s.qf[G >> 1], G < 2 ? (PREV ? s.negm : z) : s.sc[X][G & 1]); } }
  else if constexpr (PREV) s.o[G & 3] = MFMA32(s.fr[G], __builtin_bit_cast(bf16x8, s.pf[(G - 16) >> 2]), s.o[G & 3]);
  if constexpr (QK && PREV && G < 12 && (G & 1)) kvdma<(G >> 1) + 2>(s);
  if constexpr (PREV) {
    if constexpr (G < 26) sexp<Y, G + 6>(s);
    if constexpr (G < 27) sadd<Y, G + 5>(s);
    if constexpr (G < 27 && !(G & 1)) scvt<Y, (G >> 1) + 2>(s);
    if constexpr (G == 27) { s.l += s.ls[Y]; asm volatile("" : "+v"(s.l)); }
  }
  if constexpr (QK) {
    if constexpr (LAST && G == 16) {
      mask_diag<X>(s);
      {
        unsigned qa = SP + (s.w & 3) * 8192u + opaque_lane() * 16u; asm volatile("" : "+v"(qa));
#pragma unroll
        for (int i = 0; i < 8; ++i) s.qf[i] = lds128(lp(qa) + i * 1024);
      }
    }
    if constexpr (G >= 17 && G < 25) {
      constexpr int i = G - 17, kb = i >> 2, r = 4 * (i & 3);
      if constexpr (i == 0) { s.mx0 = max3f(s.sc[X][0][0], s.sc[X][0][1], s.sc[X][0][1]); s.mx1 = max3f(s.sc[X][0][2], s.sc[X][0][3], s.sc[X][0][3]); }
      else { s.mx0 = max3f(s.mx0, s.sc[X][kb][r], s.sc[X][kb][r + 1]); s.mx1 = max3f(s.mx1, s.sc[X][kb][r + 2], s.sc[X][kb][r + 3]); }
      asm volatile("" : "+v"(s.mx0), "+v"(s.mx1));
    }
    if constexpr (G == 25) {
      const float mx = max3f(s.mx0, s.mx1, s.mx1);
      if constexpr (!PREV) {
        const float rm = __builtin_fmaxf(swap32_max(mx), -1e30f);
        s.m = rm; s.l = 0.f; sub_ip(s.sc[X][0], rm); sub_ip(s.sc[X][1], rm);
#pragma unroll
        for (int r = 0; r < 16; ++r) s.negm[r] = -rm;
      } else if (__builtin_expect(__any(mx > THR), 0)) {
        const float dl = __builtin_fmaxf(swap32_max(mx), 0.f);
        s.m += dl; s.f = __builtin_amdgcn_exp2f(-dl); s.pend = true;
        sub_ip(s.sc[X][0], dl); sub_ip(s.sc[X][1], dl);
#pragma unroll
        for (int r = 0; r < 16; ++r) { float e = s.negm[r]; asm("v_sub_f32_e32 %0, 0, %1" : "+v"(e) : "v"(s.m)); s.negm[r] = e; }
      }
    }
    if constexpr (G >= 26) sexp<X, G - 26>(s);
    if constexpr (G >= 27) sadd<X, G - 27>(s);
    if constexpr (G == 28 || G == 30) scvt<X, ((G - 28) >> 1)>(s);
    if constexpr (!LAST && G == 30) { s.m0k = KR + par + s.w * 4096u; s.m0v = VR + (par ^ 32768u) + s.w * 4096u; s.kso = ksoff; s.vso = vsoff; }
    if constexpr (!LAST && G >= 30) kvdma<G - 30>(s);
  }
  SBAR();
}
template <int X, bool QK, bool PREV, bool LAST, int... Gs> DI void astep_(St& s, unsigned par, unsigned ksoff, unsigned vsoff, iseq<Gs...>) {
  if constexpr (QK && !PREV) { frag<32>(s); frag<33>(s); frag<34>(s); if constexpr (ALA > 3) frag<35>(s); if constexpr (ALA > 4) frag<36>(s); SBAR(); }
  if constexpr (QK) { s.fr[0] = s.fr[32]; s.fr[1] = s.fr[33]; s.fr[2] = s.fr[34]; if constexpr (ALA > 3) s.fr[3] = s.fr[35]; if constexpr (ALA > 4) s.fr[4] = s.fr[36]; }
  (aslot<X, QK, PREV, LAST, Gs>(s, par, ksoff, vsoff), ...);
  if constexpr (QK && PREV) {
    if (__builtin_expect(s.pend, 0)) {
      mul_ip(s.o[0], s.f); mul_ip(s.o[1], s.f); mul_ip(s.o[2], s.f); mul_ip(s.o[3], s.f); s.l *= s.f;
      s.pend = false;
    }
  }
}
template <int X, bool QK, bool PREV, bool LAST> DI void astep(St& s, int t, int n, int c0) {
  const int kn = t + 2 < n ? t + 2 : 0;
  astep_<X, QK, PREV, LAST>(s, (unsigned)((c0 + t) & 1) << 15, s.sKV + kn * 32768u, s.sKV + (t + 1) * 32768u, mkseq<32>{});
}
template <int T> DI void tslot(St& s) {
  if constexpr (T == 0) { frag<16>(s); frag<17>(s); frag<18>(s); }
  else if constexpr (T + 17 < 32 && T >= 2) frag<T + 17>(s);
  if constexpr (T >= 2) s.o[(T - 2) & 3] = MFMA32(s.fr[T + 14], __builtin_bit_cast(bf16x8, s.pf[(T - 2) >> 2]), s.o[(T - 2) & 3]);
  if constexpr (T < 13) { sexp<1, 6 + 2 * T>(s); sexp<1, 7 + 2 * T>(s); }
  if constexpr (T > 0 && T < 14) sadd<1, 4 + 2 * T>(s);
  if constexpr (T < 14) { sadd<1, 5 + 2 * T>(s); scvt<1, T + 2>(s); }
  if constexpr (T == 14) s.l += s.ls[1];
  SBAR();
}
template <int... Ts> DI void atail(St& s, iseq<Ts...>) { (tslot<Ts>(s), ...); }
}

__global__ __launch_bounds__(512, 2) void k2_attn(const u16* __restrict__ wq, char* __restrict__ ws) {
  using namespace k2;
  const unsigned tid = threadIdx.x, lane = tid & 63, w = rfl(tid >> 6), r32 = lane & 31, hi = lane >> 5, rq = w & 3, g = w >> 2;
  const unsigned b = blockIdx.x, xcd = b & 7, ix = b >> 3, h = xcd * 4 + (ix & 3), x = ix >> 2;
  St s;
  s.w = w; s.lane = lane; s.vl16 = lane * 16u;
  TSTAMP(s);
  const unsigned q2 = (lane >> 2) & 3, p2 = lane & 3, g4 = lane >> 4;
  s.rW = mksrd(wq, DM * HDM * 2); s.rX = mksrd(ws + WS_XN, SEQ * DM * 2); s.rK = mksrd(ws + WS_KF, SEQ * HDM * 2); s.rV = mksrd(ws + WS_VF, SEQ * HDM * 2);
  s.rC = mksrd(ws + WS_CTX, SEQ * HDM * 2);
  s.vW = q2 * 8192u + g4 * 64u + p2 * 16u; s.sW = h * 256u + w * 65536u;
  s.wq_rd = QR + hi * 2048u + q2 * 64u + (g4 & 1) * 32u + p2 * 8u; asm volatile("" : "+v"(s.wq_rd));
  const unsigned qrow0 = (g == 0 ? x : 15u - x) * 128u + rq * 32u;
  s.vX = s.vl16; s.sX = (qrow0 >> 5) * 36864u;
  s.sKV = h * 524288u + w * 4096u;
  s.kso = s.sKV + (x > 0 ? 32768u : 0u);
  qload_all<0>(s, mkseq<6>{});
  __builtin_amdgcn_s_barrier();
  qload_all<1>(s, mkseq<6>{});
  qbar<0>(s); SBAR(); TSTAMP(s);
  qread<0>(s); qread<1>(s); qread<2>(s); SBAR();
  qrun(s, mkseq<144>{});
  qpack<3>(s);
  TSTAMP(s);
  wait_bar<12>();
  {
    const unsigned l16 = opaque_lane() * 16u; unsigned wa = (g == 0 ? VR + 32768u : SP) + rq * 8192u + l16; asm volatile("" : "+v"(wa));
#pragma unroll
    for (int i = 0; i < 8; ++i) *(LAS bf16x8*)((LAS char*)(size_t)wa + i * 1024) = s.qf[i];
    wait_bar<8>();
    if (g == 1) {
      unsigned ra = VR + 32768u + rq * 8192u + l16; asm volatile("" : "+v"(ra));
#pragma unroll
      for (int i = 0; i < 8; ++i) s.qf[i] = lds128(lp(ra) + i * 1024);
    }
  }
  s.kb = KR + g * 16384u + s.vl16; s.vb = VR + 32768u + g * 16384u + s.vl16; s.pend = false;
  asm volatile("" : "+v"(s.kb), "+v"(s.vb));
  TSTAMP(s);
  for (int pass = 0; pass < 2; ++pass) {
    const int j = pass == 0 ? (int)x : 15 - (int)x, n = j + 1;
    const int c0 = pass == 0 ? 0 : (int)x + 1;
    const unsigned parn = (unsigned)((c0 + n) & 1) << 15;
#pragma unroll
    for (int d = 0; d < 4; ++d) s.o[d] = f32x16{};
    if (n == 1) astep<1, true, false, true>(s, 0, n, c0);
    else {
      int t;
      if (n & 1) { astep<1, true, false, false>(s, 0, n, c0); astep<0, true, true, false>(s, 1, n, c0); t = 2; }
      else { astep<0, true, false, false>(s, 0, n, c0); t = 1; }
      for (; t < n - 1; t += 2) { astep<1, true, true, false>(s, t, n, c0); astep<0, true, true, false>(s, t + 1, n, c0); }
      astep<1, true, true, true>(s, n - 1, n, c0);
    }
    TSTAMP(s);
    atail(s, mkseq<18>{});
    TSTAMP(s);
    const float lt = swap32_add(s.l);
    const unsigned ln = opaque_lane(), lr = ln & 31, lh = ln >> 5;
    const unsigned kfree = KR + (parn ^ 32768u), ob = rq * 8192u + ln * 16u;
    unsigned mine = (g == 0 ? SP : kfree) + ob, theirs = (g == 0 ? kfree : SP) + ob; asm volatile("" : "+v"(mine), "+v"(theirs));
#pragma unroll
    for (int d = 0; d < 2; ++d)
#pragma unroll
      for (int q = 0; q < 4; ++q) {
        const f32x4 a = {s.o[d][4 * q], s.o[d][4 * q + 1], s.o[d][4 * q + 2], s.o[d][4 * q + 3]}, c = {s.o[2 + d][4 * q], s.o[2 + d][4 * q + 1], s.o[2 + d][4 * q + 2], s.o[2 + d][4 * q + 3]};
        *(LAS f32x4*)((LAS char*)(size_t)mine + (d * 4 + q) * 1024) = g == 0 ? c : a;
      }
    *(LAS f32x2*)(size_t)(VR + parn + w * 256u + lr * 8u) = f32x2{s.m, lt};
    asm volatile("s_waitcnt lgkmcnt(0)\n\ts_barrier" ::: "memory");
    const f32x2 st = *(const LAS f32x2*)(size_t)(VR + parn + (w ^ 4u) * 256u + lr * 8u);
    f32x4 p[8];
#pragma unroll
    for (int i = 0; i < 8; ++i) p[i] = *(const LAS f32x4*)(lp(theirs) + i * 1024);
    asm volatile("s_waitcnt lgkmcnt(0)\n\ts_barrier" : "+v"(p[0]), "+v"(p[1]), "+v"(p[2]), "+v"(p[3]), "+v"(p[4]), "+v"(p[5]), "+v"(p[6]), "+v"(p[7])::"memory");
    TSTAMP(s);
    s.m0k = kfree + w * 4096u; s.m0v = VR + parn + w * 4096u; s.kso = s.sKV + 32768u; s.vso = s.sKV;
    kvdma<0>(s); kvdma<1>(s); kvdma<2>(s); kvdma<3>(s); kvdma<4>(s); kvdma<5>(s); kvdma<6>(s); kvdma<7>(s);
    const float mm = __builtin_fmaxf(s.m, st[0]), fa = __builtin_amdgcn_exp2f(s.m - mm), fb = __builtin_amdgcn_exp2f(st[0] - mm);
    const float il = __builtin_amdgcn_rcpf(lt * fa + st[1] * fb), ca = fa * il, cb = fb * il;
    const unsigned vc = (lr >> 4) * 131072u + (lr & 15) * 16u + lh * 256u, sc0 = ((8u * j + 2u * rq) * 128u + 4u * h + 2u * g) * 1024u;
#pragma unroll
    for (int d = 0; d < 2; ++d)
#pragma unroll
      for (int sh = 0; sh < 2; ++sh) {
        float v[8];
#pragma unroll
        for (int e = 0; e < 8; ++e) { const float own = g == 0 ? s.o[d][8 * sh + e] : s.o[2 + d][8 * sh + e]; v[e] = own * ca + p[d * 4 + sh * 2 + (e >> 2)][e & 3] * cb; }
        st128(u32x4{cvtpk(v[0], v[1]), cvtpk(v[2], v[3]), cvtpk(v[4], v[5]), cvtpk(v[6], v[7])}, s.rC, vc, sc0 + d * 1024u + sh * 512u);
      }
  }
  TSTAMP(s);
  wait_vm<0>();
}

#define K3_OPS 9
#define K3_AAUX ""
namespace k3 {
#define K3_P 4
constexpr int P = K3_P, DIST = P - 1, WSLOT = 4096, LDS_BYTES = 163840, GRID = 240;
DI const LAS char* lp(unsigned a) { return (const LAS char*)(size_t)a; }
struct St {
  bf16x8 af[P][5], wf[4]; f32x4 acc[4][5];
  unsigned vl16, vW, rd[4], m0b, sA[5], sW;
  srd_t rA, rW;
  unsigned tsv, nts;
};
template <int I, int J> DI void load(St& s) {
  constexpr int sl = I % P;
  if constexpr (J < 4) dma16(s.m0b + sl * WSLOT + J * 1024u, s.vW, s.rW, s.sW + (I * 32u + J * 8u) * (DM * 2u));
  else asm volatile("buffer_load_dwordx4 %0, %1, %2, %3 offen offset:%4" K3_AAUX : "=v"(s.af[sl][J - 4]) : "v"(s.vl16), "s"(s.rA),
     "s"(s.sA[J - 4] + (I >> 2) * 4096u), "i"((I & 3) * 1024) : "memory");
}
template <int I, int... Js> DI void load_all(St& s, iseq<Js...>) { (load<I, Js>(s), ...); }
template <int NS, int E1, int I> DI void step(St& s) {
  constexpr int sl = I % P, ahead = (NS - 1 - I) < (DIST - 1) ? (NS - 1 - I) : (DIST - 1);
  asm volatile("s_waitcnt vmcnt(%5)" : "+v"(s.af[sl][0]), "+v"(s.af[sl][1]), "+v"(s.af[sl][2]), "+v"(s.af[sl][3]), "+v"(s.af[sl][4]) : "i"(K3_OPS * ahead) : "memory");
  if constexpr ((I & 1) == 0) TSTAMP(s);
#pragma unroll
  for (int t = 0; t < 4; ++t) s.wf[t] = cat8(ldstr(lp(s.rd[t]) + sl * WSLOT), ldstr(lp(s.rd[t]) + sl * WSLOT + E1 * 128));
  SBAR();
  const f32x4 z = {};
#define K3_MM(t, r) s.acc[t][r] = MFMA16(s.wf[t], s.af[sl][r], I == 0 ? z : s.acc[t][r])
#define K3_LD(j) if constexpr (I + DIST < NS) load<I + DIST, j>(s)
  K3_LD(0); K3_MM(0, 0); K3_MM(0, 1); SBAR(); K3_LD(1); K3_MM(0, 2); K3_MM(0, 3); SBAR();
  K3_LD(2); K3_MM(0, 4); K3_MM(1, 0); SBAR(); K3_LD(3); K3_MM(1, 1); K3_MM(1, 2); SBAR();
  K3_LD(4); K3_MM(1, 3); K3_MM(1, 4); SBAR(); K3_LD(5); K3_MM(2, 0); K3_MM(2, 1); SBAR();
  K3_LD(6); K3_MM(2, 2); K3_MM(2, 3); SBAR(); K3_LD(7); K3_MM(2, 4); K3_MM(3, 0); SBAR();
  K3_LD(8); K3_MM(3, 1); K3_MM(3, 2); K3_MM(3, 3); K3_MM(3, 4); SBAR();
#undef K3_MM
#undef K3_LD
}
template <int NS, int E1, int... Is> DI void run(St& s, iseq<Is...>) { (step<NS, E1, Is>(s), ...); }
template <int... Is> DI void prologue(St& s, iseq<Is...>) { (load_all<Is>(s, mkseq<9>{}), ...); }

template <int NK32, bool FIRST> DI void body(const char* A, const u16* W, const u16* x, char* ws, u16* out) {
  constexpr int NS = NK32 / 8;
  const unsigned tid = threadIdx.x, lane = tid & 63, w = rfl(tid >> 6), g = lane >> 4, q2 = (lane >> 2) & 3, p2 = lane & 3;
  const unsigned b = blockIdx.x, tile = (b & 7) * 30u + (b >> 3);
  if (tile >= 234u) return;
  const unsigned rbk = tile / 9u, cb = tile - rbk * 9u;
  St s;
  TSTAMP(s);
  s.rA = mksrd(A, SEQ * NK32 * 64); s.rW = mksrd(W, NK32 * 32 * DM * 2);
  s.vl16 = lane * 16u;
  s.vW = (lane >> 3) * (DM * 2u) + (((lane & 7) ^ (((lane >> 4) & 3) << 1)) << 4);
  s.m0b = w * (P * WSLOT); s.sW = w * NS * 32u * (DM * 2u) + cb * 128u;
#pragma unroll
  for (int r = 0; r < 5; ++r) s.sA[r] = ((rbk * 5u + r) * NK32 + w * NS) * 1024u;
  const unsigned krow = FIRST ? 16u * (g >> 1) + 4u * (g & 1) + q2 : 4u * g + q2;
#pragma unroll
  for (int t = 0; t < 4; ++t) { s.rd[t] = s.m0b + krow * 128u + ((((krow >> 1) & 3) ^ t) << 5) + p2 * 8u; asm volatile("" : "+v"(s.rd[t])); }
  const unsigned pc = tid & 7, m0 = tid >> 3;
  unsigned vrm[2], vfr[2];
#pragma unroll
  for (int i = 0; i < 2; ++i) {
    const unsigned m = m0 + 64u * i, row = rbk * 80u + m; const bool ok = m < 80u && row < (unsigned)SEQ;
    vrm[i] = ok ? row * (DM * 2u) + cb * 128u + pc * 16u : 0x10000000u;
    vfr[i] = ok ? (row >> 4) * 18432u + (cb * 8u + pc) * 256u + (row & 15) * 16u : 0x10000000u;
  }
  u32x4 res[2];
  {
    const srd_t rR = FIRST ? mksrd(x, SEQ * DM * 2) : mksrd(ws + WS_H, SEQ * DM * 2);
    asm volatile("buffer_load_dwordx4 %0, %2, %4, 0 offen\n\tbuffer_load_dwordx4 %1, %3, %4, 0 offen"
                 : "=&v"(res[0]), "=&v"(res[1]) : "v"(FIRST ? vrm[0] : vfr[0]), "v"(FIRST ? vrm[1] : vfr[1]), "s"(rR) : "memory");
  }
  prologue(s, mkseq<DIST>{});
  TSTAMP(s);
  run<NS, FIRST ? 8 : 16>(s, mkseq<NS>{});
  TSTAMP(s);
  asm volatile("s_waitcnt lgkmcnt(0)\n\ts_barrier" : "+v"(res[0]), "+v"(res[1])::"memory");
#pragma unroll
  for (int t = 0; t < 4; ++t) {
    unsigned wa = w * 20480u + (lane & 15) * 256u + (((4u * t + g) ^ (lane & 15)) << 4); asm volatile("" : "+v"(wa));
#pragma unroll
    for (int r = 0; r < 5; ++r) *(LAS f32x4*)((LAS char*)(size_t)wa + r * 4096) = s.acc[t][r];
  }
  asm volatile("s_waitcnt lgkmcnt(0)\n\ts_barrier" ::: "memory");
  TSTAMP(s);
  const srd_t rO = FIRST ? mksrd(ws + WS_H, SEQ * DM * 2) : mksrd(out, SEQ * DM * 2);
#pragma unroll
  for (int i = 0; i < 2; ++i) {
    if (i == 1 && w >= 2) break;
    unsigned ra = (m0 + 64u * i) * 256u + (((2u * pc) ^ (m0 & 15)) << 4), rb = ra + 81920u; asm volatile("" : "+v"(ra), "+v"(rb));
    f32x4 a = *(const LAS f32x4*)(lp(ra)), c = *(const LAS f32x4*)(lp(ra ^ 16u));
#pragma unroll
    for (int ww = 1; ww < 8; ++ww) { const unsigned base = ww < 4 ? ra : rb; a += *(const LAS f32x4*)(lp(base) + (ww & 3) * 20480); c += *(const LAS f32x4*)(lp(base ^ 16u) + (ww & 3) * 20480); }
    const unsigned y[4] = {cvtpk(a[0], a[1]), cvtpk(a[2], a[3]), cvtpk(c[0], c[1]), cvtpk(c[2], c[3])};
    u32x4 o;
#pragma unroll
    for (int e = 0; e < 4; ++e) o[e] = cvtpk(bflo(res[i][e]) + bflo(y[e]), bfhi(res[i][e]) + bfhi(y[e]));
    st128(o, rO, FIRST ? vfr[i] : vrm[i], 0);
    if constexpr (FIRST) {
      float sm = 0.f;
#pragma unroll
      for (int e = 0; e < 4; ++e) sm += bflo(o[e]) + bfhi(o[e]);
      const float mean = dpp_add<0x141>(dpp_add<0x4E>(dpp_add<0xB1>(sm))) * (1.f / 64.f);
      float q = 0.f;
#pragma unroll
      for (int e = 0; e < 4; ++e) { const float d0 = bflo(o[e]) - mean, d1 = bfhi(o[e]) - mean; q += d0 * d0 + d1 * d1; }
      q = dpp_add<0x141>(dpp_add<0x4E>(dpp_add<0xB1>(q)));
      const unsigned m = m0 + 64u * i, row = rbk * 80u + m;
      if (pc == 0 && m < 80u && row < (unsigned)SEQ) *(f32x2*)(ws + WS_ST + (row * 9u + cb) * 8u) = f32x2{mean, q};
    }
  }
}
}
__global__ __launch_bounds__(512, 2) void k3_oproj(const u16* __restrict__ wo, const u16* __restrict__ x, char* __restrict__ ws) { k3::body<128, true>(ws + WS_CTX, wo, x, ws, nullptr); }
__global__ __launch_bounds__(512, 2) void k5_down(const u16* __restrict__ wd, char* __restrict__ ws, u16* __restrict__ out) { k3::body<144, false>(ws + WS_ACT, wd, nullptr, ws, out); }

#define K4_LA 3
namespace k4 {
constexpr int SLOT = 36864, CSOFF = 4 * SLOT, LDS_BYTES = CSOFF + 2048, LA = K4_LA, BB = 2, NU = 324;
DI const LAS char* lp(unsigned a) { return (const LAS char*)(size_t)a; }
constexpr int u_pair(int g) { return g < 112 ? (g & 7) >> 1 : g < 144 ? (g - 112) >> 3 : 4 + (g - 144) / 36; }
constexpr int u_kstep(int g) { return g < 112 ? g >> 3 : g < 144 ? 14 + ((g & 7) >> 1) : ((g - 144) % 36) >> 1; }
constexpr int pair_end(int p) { return p < 4 ? 119 + 8 * p : 144 + 36 * (p - 4) + 35; }
constexpr int NE = 10;
constexpr int eks(int e) { constexpr int t[6] = {0, 2, 5, 9, 13, 18}; return t[e]; }
constexpr int estart(int e) { return e < 5 ? 8 * eks(e) : 144 + 72 * ((e - 5) >> 1) + 18 * ((e - 5) & 1); }
constexpr int bslot(int e) { return estart(e) - BB; }
constexpr int epoch_of(int g) { int e = 0; while (e + 1 < NE && estart(e + 1) <= g) ++e; return e; }
constexpr int NPRO = 9;
constexpr int cfirst(int c) { return c == 4 ? 50 : c == 5 ? bslot(3) + 2 : c == 6 ? bslot(5) + 1 : c == 7 ? bslot(5) + 11 : bslot(7) + 1; }
constexpr int op_at(int g) {
  if (g < 54 - NPRO) return NPRO + g;
  for (int c = 4; c < 9; ++c) { const int j = g - cfirst(c); if (j >= 0 && j < 10 && !(j & 1)) return 100 + 8 * (c - 4) + (j >> 1); }
  if (g == 92) return 204;
  return -1;
}
constexpr int NEP = 25;
constexpr int ep_start(int p) { int s = 0; for (int q = 0; q <= p; ++q) { const int a = pair_end(q) + 3; s = (q == 0 || a > s + NEP) ? a : s + NEP; } return s; }
constexpr int ep_pair(int g) { for (int p = 0; p < 8; ++p) { const int j = g - ep_start(p); if (j >= 0 && j < NEP) return p; } return -1; }
constexpr int ep_stage(int g) { return ep_pair(g) < 0 ? -1 : g - ep_start(ep_pair(g)) + 1; }
constexpr int ep0_pair(int g) { for (int p = 0; p < 8; ++p) if (g == ep_start(p) - 1) return p; return -1; }
static_assert(ep_start(7) + NEP <= NU, "epilogues must fit");
constexpr int st_ops(int g) { return (ep_stage(g) == NEP && (ep_pair(g) & 1)) ? 2 : 0; }
constexpr bool sure(int op) { return op >= 0 && !(op >= 100 && op < 200 && ((op - 100) & 7) == 4) && op != 204; }
constexpr int last_slot(int e) {
  const int want = e == 4 ? 204 : e < 5 ? 3 * eks(e + 1) - 1 : 100 + 8 * (e - 5) + 4;
  for (int g = 0; g < NU; ++g) if (op_at(g) == want) return g;
  return -1;
}
constexpr int vm_wait(int e) {
  const int from = last_slot(e); int n = from < 0 ? NPRO - 3 * eks(e + 1) : st_ops(from);
  for (int g = from + 1; g < (e ? bslot(e) : 0); ++g) n += (sure(op_at(g)) ? 1 : 0) + st_ops(g);
  return n;
}
constexpr int ST0 = 2;
DI unsigned olane() { unsigned l; asm volatile("v_mbcnt_lo_u32_b32 %0, -1, 0\n\tv_mbcnt_hi_u32_b32 %0, -1, %0" : "=v"(l)); return l; }
struct St {
  bf16x8 hf[18][2], wf[NU]; f32x4 acc[9][2][2];
  f32x4 sp[2]; f32x2 cs[2], sq[2]; float e0, e1, oa, ob, t[2]; u32x4 pk[2];
  float nmean[2], c1[2], c2[2];
  unsigned w, wp[2], wbA, vA, vF, sA, sF, sH, sHh, csb, vst, sAct, vsp, vsq, f0, fh;
  srd_t rG, rU, rW, rH, rS, rCS, rAct;
  unsigned tsv, nts;
};
template <int OP> DI void vop(St& s) {
  if constexpr (OP < 54) {
    constexpr int ks = OP / 3, j = OP % 3;
    if constexpr (j < 2) asm volatile("buffer_load_dwordx4 %0, %1, %2, %3 offen offset:%4" : "=v"(s.hf[ks][j]) : "v"(s.vst), "s"(s.rH), "s"(s.sHh + j * 18432u + (ks >> 2) * 4096u), "i"((ks & 3) * 1024) : "memory");
    else dma16(ks * 8192u + s.w * 1024u, s.vA, s.rW, s.sA + ks * 32u * (FF * 2u));
  } else if constexpr (OP < 200) {
    constexpr int C = 4 + ((OP - 100) >> 3), J = (OP - 100) & 7; constexpr unsigned slot = (C & 3) * SLOT;
    const unsigned idx = s.w + 8u * J; const bool up = idx >= 18u; const unsigned r = up ? idx - 18u : idx;
    if constexpr (J == 0 && (C == 4 || C == 8)) {
      const unsigned l = olane();
      if constexpr (C == 4) s.vF = (l >> 2) * (FF * 2u) + (((l & 3) ^ ((l >> 5) << 1)) << 4);
      else { const unsigned s8 = l >> 3, kq = (s8 & 4) | ((s8 & 1) << 1) | ((s8 >> 1) & 1); s.vF = (kq * 4u + ((l >> 1) & 3)) * (FF * 2u) + (l & 1) * 16u; }
    }
    if (J < 4 || s.w < 4u) {
      if constexpr (C < 8) dma16(slot + idx * 1024u, s.vF, up ? s.rU : s.rG, s.sF + (C >> 1) * 64u + ((C & 1) * 288u + r * 16u) * (FF * 2u));
      else dma16(slot + idx * 1024u, s.vF, up ? s.rU : s.rG, s.sH + r * 32u * (FF * 2u));
    }
  } else if constexpr (OP < 202) asm volatile("buffer_load_dwordx4 %0, %1, %2, 0 offen offset:%3" : "=v"(s.sp[OP - 200]) : "v"(s.vsp), "s"(s.rS), "i"((OP - 200) * 1152) : "memory");
  else if constexpr (OP < 204) asm volatile("buffer_load_dwordx2 %0, %1, %2, 0 offen offset:%3" : "=v"(s.sq[OP - 202]) : "v"(s.vsq), "s"(s.rS), "i"((OP - 202) * 1152 + 64) : "memory");
  else { const unsigned l = olane(), pr = l >> 2; s.csb = CSOFF + (l >> 4) * 16u; asm volatile("" : "+v"(s.csb));
    if (s.w < 2u) dma16(CSOFF + s.w * 1024u, ((pr < 8 ? s.f0 + pr * 16u : s.fh) + (l & 3) * 4u) * 4u, s.rCS, s.w * (FF * 4u)); }
}
template <int G> DI void reads(St& s) {
  if constexpr (G < NU) {
    constexpr int mat = G & 1, p = u_pair(G), ks = u_kstep(G);
    if constexpr (G < 144) { constexpr int off = (ks - eks(epoch_of(G))) * 8192 + mat * 4096 + (p >> 1) * 256; s.wf[G] = cat8(ldstr(lp(s.wp[p & 1]) + off), ldstr(lp(s.wp[p & 1]) + off + 512)); }
    else {
      if constexpr ((G - 144) % 18 == 0 && G <= 288) {
        const unsigned l = olane(), g = l >> 4, q = (l >> 2) & 3;
        unsigned a = (l & 3) * 8u + (G < 288 ? g * 512u + q * 64u + (((g ^ p) & 1) << 5) + ((p & 2) + ks / 9) * SLOT : (g >> 1) * 512u + (g & 1) * 128u + q * 32u);
        asm volatile("" : "+v"(a)); s.wp[0] = a;
      }
      constexpr int off = mat * 18432 + (G < 288 ? (ks % 9) * 2048 : ks * 1024);
      s.wf[G] = cat8(ldstr(lp(s.wp[0]) + off), ldstr(lp(s.wp[0]) + off + 256));
    }
  }
}
template <int K> DI void pin1(St& s) { asm volatile("" : "+v"(s.hf[K][0]), "+v"(s.hf[K][1])); }
template <int E, int... Ks> DI void pin_h(St& s, iseq<Ks...>) { (pin1<eks(E) + Ks>(s), ...); }
template <int E> DI void barrier(St& s) {
  wait_bar<vm_wait(E)>();
  if constexpr (E < 5) {
    pin_h<E>(s, mkseq<eks(E + 1) - eks(E)>{});
    unsigned a = s.wbA + eks(E) * 8192u, b = (s.wbA ^ 32u) + eks(E) * 8192u;
    asm volatile("" : "+v"(a), "+v"(b)); s.wp[0] = a; s.wp[1] = b;
  }
  SBAR(); TSTAMP(s);
}
template <int ST> DI void stats(St& s) {
  constexpr int rt = ST / 8, k = ST % 8;
  if constexpr (k == 0) { asm volatile("" : "+v"(s.sp[rt]), "+v"(s.sq[rt])); s.t[0] = swap16_add(s.sp[rt][0] + s.sp[rt][2]); }
  else if constexpr (k == 1) s.t[0] = (swap32_add(s.t[0]) + s.sq[rt][0]) * (1.f / 9.f);
  else if constexpr (k == 2) { const float d0 = s.sp[rt][0] - s.t[0], d1 = s.sp[rt][2] - s.t[0]; s.t[1] = __builtin_fmaf(64.f, d0 * d0 + d1 * d1, s.sp[rt][1] + s.sp[rt][3]); }
  else if constexpr (k == 3) s.t[1] = swap16_add(s.t[1]);
  else if constexpr (k == 4) { const float d = s.sq[rt][0] - s.t[0]; s.t[1] = swap32_add(s.t[1]) + __builtin_fmaf(64.f * d, d, s.sq[rt][1]); }
  else if constexpr (k == 5) { s.t[1] = rbf(rbf(s.t[1] * (1.f / DM)) + 1e-5f); }
  else if constexpr (k == 6) { s.t[1] = rbf(__builtin_amdgcn_sqrtf(s.t[1])); s.nmean[rt] = -rbf(s.t[0]); }
  else { const float rs = __builtin_amdgcn_rcpf(s.t[1]); s.c1[rt] = -1.4426950408889634f * rs; s.c2[rt] = s.t[1] * s.t[1]; asm volatile("" : "+v"(s.c1[rt]), "+v"(s.c2[rt]), "+v"(s.nmean[rt])); }
  if constexpr (k < 7) asm volatile("" : "+v"(s.t[0]), "+v"(s.t[1]));
}
template <int P, int I> DI void csread(St& s) { s.cs[I & 1][0] = *(const LAS float*)(lp(s.csb) + P * 64 + I * 4); s.cs[I & 1][1] = *(const LAS float*)(lp(s.csb) + 1024 + P * 64 + I * 4); }
template <int P, int ST> DI void ep(St& s) {
  if constexpr (ST == 0) csread<P, 0>(s);
  else if constexpr (ST < 25) {
    constexpr int v = (ST - 1) / 3, k = (ST - 1) % 3, rt = v & 1, i = v >> 1;
    if constexpr (k == 0) {
      const float ag = __builtin_fmaf(s.nmean[rt], s.cs[i & 1][0], s.acc[P][0][rt][i]), au = __builtin_fmaf(s.nmean[rt], s.cs[i & 1][1], s.acc[P][1][rt][i]);
      s.e0 = ag * s.c1[rt]; s.e1 = ag * au;
      if constexpr (rt == 0 && i < 3) csread<P, i + 1>(s);
    } else if constexpr (k == 1) s.e0 = __builtin_amdgcn_exp2f(s.e0);
    else {
      float o = s.e1 * __builtin_amdgcn_rcpf(__builtin_fmaf(s.e0, s.c2[rt], s.c2[rt]));
      if constexpr (i & 1) { unsigned pkd; asm volatile("v_cvt_pk_bf16_f32 %0, %1, %2" : "=v"(pkd) : "v"(rt ? s.ob : s.oa), "v"(o)); s.pk[rt][(P & 1) * 2 + (i >> 1)] = pkd; }
      else { asm volatile("" : "+v"(o)); if constexpr (rt) s.ob = o; else s.oa = o; }
    }
    if constexpr (k < 2) asm volatile("" : "+v"(s.e0), "+v"(s.e1));
  } else if constexpr (P < 8 && (P & 1)) {
#pragma unroll
    for (int r2 = 0; r2 < 2; ++r2) st128(s.pk[r2], s.rAct, s.vst, s.sAct + (P >> 1) * 1024u + r2 * 147456u);
  }
}
template <int G> DI void slot(St& s) {
  constexpr int en = epoch_of(G) + 1;
  if constexpr (en < NE && G == bslot(en)) { barrier<en>(s); reads<G + 2>(s); if constexpr (LA > 2) reads<G + 3>(s); }
  else if constexpr (!(en < NE && G + LA >= estart(en) && G < bslot(en))) reads<G + LA>(s);
  if constexpr (op_at(G) >= 0) vop<op_at(G)>(s);
  constexpr int p = u_pair(G), mat = G & 1, ks = u_kstep(G);
#pragma unroll
  for (int rt = 0; rt < 2; ++rt) { if constexpr (ks == 0) MFMA16_NEW(s.acc[p][mat][rt], s.wf[G], s.hf[ks][rt]); else MFMA16_ACC(s.acc[p][mat][rt], s.wf[G], s.hf[ks][rt]); }
  if constexpr (G >= ST0 && G < ST0 + 16) stats<G - ST0>(s);
  if constexpr (ep_pair(G) >= 0) ep<ep_pair(G), ep_stage(G)>(s);
  if constexpr (ep0_pair(G) >= 0) ep<ep0_pair(G), 0>(s);
  SBAR();
}
template <int... Gs> DI void run(St& s, iseq<Gs...>) { (slot<Gs>(s), ...); }
template <int O0, int... Os> DI void vops(St& s, iseq<Os...>) { (vop<O0 + Os>(s), ...); }
template <int... Ss> DI void ep_all(St& s, iseq<Ss...>) { (ep<8, Ss>(s), ...); }
}

__global__ __launch_bounds__(512, 2) void k4_up(const u16* __restrict__ wg, const u16* __restrict__ wu, char* __restrict__ ws) {
  using namespace k4;
  const unsigned tid = threadIdx.x, lane = tid & 63, w = rfl(tid >> 6), r = lane & 15, g = lane >> 4, q2 = (lane >> 2) & 3, p2 = lane & 3;
  const unsigned b = blockIdx.x, xcd = b & 7, ix = b >> 3, rb = ix >> 2, cb = xcd * 4 + (ix & 3), odd = cb & 1;
  St s;
  TSTAMP(s);
  s.w = w; s.rG = mksrd(wg, DM * FF * 2); s.rU = mksrd(wu, DM * FF * 2); s.rW = mksrd(w < 4 ? wg : wu, DM * FF * 2); s.rAct = mksrd(ws + WS_ACT, SEQ * FF * 2);
  s.rH = mksrd(ws + WS_H, SEQ * DM * 2); s.rS = mksrd(ws + WS_ST, SEQ * 72); s.rCS = mksrd(ws + WS_CS, FF * 8);
  const unsigned f0 = cb * 144u + odd * 16u, fh = cb * 144u + (odd ? 0u : 128u);
  const unsigned row0 = rb * 256u + w * 32u;
  s.vA = (4u * (lane >> 5) + q2) * (FF * 2u) + (g & 1) * 64u + ((p2 ^ ((w & 1) << 1)) << 4); s.sA = f0 * 2u + (w & 3) * 8u * (FF * 2u);
  s.sF = f0 * 2u; s.sH = fh * 2u; s.f0 = f0; s.fh = fh;
  s.wbA = g * 1024u + q2 * 64u + (g & 1) * 32u + p2 * 8u;
  s.vst = lane * 16u; s.sAct = ((rb * 16u + w * 2u) * 144u + (f0 >> 5)) * 1024u; s.sHh = (row0 >> 4) * 18432u;
  s.vsq = (row0 + r) * 72u; s.vsp = s.vsq + g * 16u;
  vops<200>(s, mkseq<4>{});
  vops<0>(s, mkseq<6>{});
  __builtin_amdgcn_s_barrier();
  vops<6>(s, mkseq<NPRO - 6>{});
  barrier<0>(s);
  reads<0>(s); reads<1>(s); if constexpr (LA > 2) reads<2>(s); SBAR();
  run(s, mkseq<NU>{});
  TSTAMP(s);
  asm volatile("s_nop 7\n\ts_nop 3" ::: "memory"); SBAR();
  ep_all(s, mkseq<NEP + 1>{});
  {
    const unsigned so = ((rb * 16u + w * 2u) * 144u + (fh >> 5)) * 1024u + odd * 8u;
#pragma unroll
    for (int rt = 0; rt < 2; ++rt) asm volatile("buffer_store_dwordx2 %0, %1, %2, %3 offen" ST_AUX ::"v"(u32x2{s.pk[rt][0], s.pk[rt][1]}), "v"(s.vst), "s"(s.rAct), "s"(so + rt * 147456u) : "memory");
  }
}

extern "C" void kernel_launch(void* const* d_in, const int* in_sizes, int n_in, void* d_out, int out_size, void* d_ws, size_t ws_size, hipStream_t stream) {
  const u16* x = (const u16*)d_in[0]; const u16* ckv = (const u16*)d_in[1]; const u16* wq = (const u16*)d_in[2]; const u16* wuk = (const u16*)d_in[3];
  const u16* wuv = (const u16*)d_in[4]; const u16* wo = (const u16*)d_in[5]; const u16* wg = (const u16*)d_in[7]; const u16* wu = (const u16*)d_in[8]; const u16* wd = (const u16*)d_in[9];
  char* ws = (char*)d_ws;
  (void)hipFuncSetAttribute((const void*)k1_kvproj, hipFuncAttributeMaxDynamicSharedMemorySize, k1::LDS_BYTES);
  k1_kvproj<<<256, 512, k1::LDS_BYTES, stream>>>(ckv, wuk, wuv, x, wq, wo, wg, wu, wd, ws);
  (void)hipFuncSetAttribute((const void*)k2_attn, hipFuncAttributeMaxDynamicSharedMemorySize, k2::LDS_BYTES);
  k2_attn<<<256, 512, k2::LDS_BYTES, stream>>>(wq, ws);
  (void)hipFuncSetAttribute((const void*)k3_oproj, hipFuncAttributeMaxDynamicSharedMemorySize, k3::LDS_BYTES);
  k3_oproj<<<k3::GRID, 512, k3::LDS_BYTES, stream>>>(wo, x, ws);
  (void)hipFuncSetAttribute((const void*)k4_up, hipFuncAttributeMaxDynamicSharedMemorySize, k4::LDS_BYTES);
  k4_up<<<256, 512, k4::LDS_BYTES, stream>>>(wg, wu, ws);
  (void)hipFuncSetAttribute((const void*)k5_down, hipFuncAttributeMaxDynamicSharedMemorySize, k3::LDS_BYTES);
  k5_down<<<k3::GRID, 512, k3::LDS_BYTES, stream>>>(wd, ws, (u16*)d_out);
}
```

```cpp
#include <hip/hip_runtime.h>
#include <utility>
typedef unsigned short u16;
typedef short bf16x8 __attribute__((ext_vector_type(8)));
typedef short s16x4 __attribute__((ext_vector_type(4)));
typedef float f32x16 __attribute__((ext_vector_type(16)));
typedef float f32x4 __attribute__((ext_vector_type(4)));
typedef float f32x2 __attribute__((ext_vector_type(2)));
typedef unsigned u32x4 __attribute__((ext_vector_type(4)));
typedef unsigned u32x2 __attribute__((ext_vector_type(2)));
typedef __bf16 bf16x2_t __attribute__((ext_vector_type(2)));
#define LAS __attribute__((address_space(3)))
#define DI __device__ __forceinline__
#define SBAR() __builtin_amdgcn_sched_barrier(0)
#define MFMA32(a, b, c) __builtin_amdgcn_mfma_f32_32x32x16_bf16((a), (b), (c), 0, 0, 0)
#define MFMA16(a, b, c) __builtin_amdgcn_mfma_f32_16x16x32_bf16((a), (b), (c), 0, 0, 0)
#define MFMA16_ACC(c, a, b) asm volatile("v_mfma_f32_16x16x32_bf16 %0, %1, %2, %0" : "+v"(c) : "v"(a), "v"(b))
#define MFMA16_NEW(c, a, b) asm volatile("v_mfma_f32_16x16x32_bf16 %0, %1, %2, 0" : "=v"(c) : "v"(a), "v"(b))
#define MFMA32_ACC(c, a, b) asm volatile("v_mfma_f32_32x32x16_bf16 %0, %1, %2, %0" : "+v"(c) : "v"(a), "v"(b))
#define MFMA32_NEW(c, a, b) asm volatile("v_mfma_f32_32x32x16_bf16 %0, %1, %2, 0" : "=v"(c) : "v"(a), "v"(b))
template <int... Is> using iseq = std::integer_sequence<int, Is...>;
template <int N> using mkseq = std::make_integer_sequence<int, N>;
typedef __amdgpu_buffer_rsrc_t srd_t;

DI unsigned rfl(unsigned x) { return __builtin_amdgcn_readfirstlane(x); }
DI float bf2f(unsigned v) { return __uint_as_float(v << 16); }
DI float bflo(unsigned w) { return __uint_as_float(w << 16); }
DI float bfhi(unsigned w) { return __uint_as_float(w & 0xffff0000u); }
DI unsigned cvtpk(float lo, float hi) { f32x2 v = {lo, hi}; bf16x2_t b = __builtin_convertvector(v, bf16x2_t); return __builtin_bit_cast(unsigned, b); }
DI float rbf(float f) { return bflo(cvtpk(f, f)); }
DI srd_t mksrd(const void* p, unsigned bytes) { return __builtin_amdgcn_make_buffer_rsrc((void*)p, (short)0, (int)bytes, 0x00020000); }
DI void dma16(unsigned m0v, unsigned voff, srd_t srd, unsigned soff) {
  asm volatile("s_mov_b32 m0, %0\n\ts_nop 0\n\tbuffer_load_dwordx4 %1, %2, %3 offen lds" ::"s"(m0v), "v"(voff), "s"(srd), "s"(soff) : "memory");
}
template <int N> DI void wait_vm() { asm volatile("s_waitcnt vmcnt(%0)" ::"i"(N) : "memory"); }
template <int N> DI void wait_bar() { asm volatile("s_waitcnt vmcnt(%0) lgkmcnt(0)\n\ts_barrier" ::"i"(N) : "memory"); }
DI bf16x8 lds128(const LAS char* p) { return *(const LAS bf16x8*)p; }
typedef short v4i16_t __attribute__((ext_vector_type(4)));
DI s16x4 ldstr(const LAS char* p) { return __builtin_bit_cast(s16x4, __builtin_amdgcn_ds_read_tr16_b64_v4i16((LAS v4i16_t*)p)); }
DI bf16x8 cat8(s16x4 a, s16x4 b) { return __builtin_shufflevector(a, b, 0, 1, 2, 3, 4, 5, 6, 7); }
DI u32x4 pack8(const f32x16& x, int s) {
  u32x4 w = {cvtpk(x[8 * s], x[8 * s + 1]), cvtpk(x[8 * s + 2], x[8 * s + 3]), cvtpk(x[8 * s + 4], x[8 * s + 5]), cvtpk(x[8 * s + 6], x[8 * s + 7])};
  return w;
}
template <int MODE> DI void st128m(u32x4 v, srd_t srd, unsigned voff, unsigned soff) {
  if constexpr (MODE == 0) asm volatile("buffer_store_dwordx4 %0, %1, %2, %3 offen\n\ts_nop 1" ::"v"(v), "v"(voff), "s"(srd), "s"(soff) : "memory");
  else if constexpr (MODE == 1) asm volatile("buffer_store_dwordx4 %0, %1, %2, %3 offen sc1\n\ts_nop 1" ::"v"(v), "v"(voff), "s"(srd), "s"(soff) : "memory");
  else if constexpr (MODE == 2) asm volatile("buffer_store_dwordx4 %0, %1, %2, %3 offen nt\n\ts_nop 1" ::"v"(v), "v"(voff), "s"(srd), "s"(soff) : "memory");
  else asm volatile("buffer_store_dwordx4 %0, %1, %2, %3 offen sc0 sc1\n\ts_nop 1" ::"v"(v), "v"(voff), "s"(srd), "s"(soff) : "memory");
}
#define ST1 1
#define ST2 1
#define ST3 1
#define ST4 1
#define ST5 1
template <int CTRL> DI float dpp_add(float x) { return x + __builtin_bit_cast(float, __builtin_amdgcn_update_dpp(0, __builtin_bit_cast(int, x), CTRL, 0xf, 0xf, true)); }
DI float swap32_add(float x) { auto r = __builtin_amdgcn_permlane32_swap(__float_as_uint(x), __float_as_uint(x), false, false); return __uint_as_float(r[0]) + __uint_as_float(r[1]); }
DI float swap16_add(float x) { auto r = __builtin_amdgcn_permlane16_swap(__float_as_uint(x), __float_as_uint(x), false, false); return __uint_as_float(r[0]) + __uint_as_float(r[1]); }
DI float swap32_max(float x) { auto r = __builtin_amdgcn_permlane32_swap(__float_as_uint(x), __float_as_uint(x), false, false); return __builtin_fmaxf(__uint_as_float(r[0]), __uint_as_float(r[1])); }
DI float wave_sum(float x) {
  x = dpp_add<0xB1>(x); x = dpp_add<0x4E>(x); x = dpp_add<0x141>(x); x = dpp_add<0x140>(x);
  x = swap16_add(x); return swap32_add(x);
}

constexpr int SEQ = 2048, DM = 576, DC = 512, NH = 32, HD = 128, FF = 4608, HDM = NH * HD;
constexpr size_t WS_XN = 0;
constexpr size_t WS_KF = WS_XN + (size_t)SEQ * DM * 2;
constexpr size_t WS_VF = WS_KF + (size_t)SEQ * HDM * 2;
constexpr size_t WS_CTX = WS_VF + (size_t)SEQ * HDM * 2;
constexpr size_t WS_QS = WS_CTX + (size_t)SEQ * HDM * 2;
constexpr size_t WS_WO = WS_QS + (size_t)256 * 32768;
constexpr size_t WS_WGU = WS_WO + (size_t)HDM * DM * 2;
constexpr size_t WS_WD = WS_WGU + (size_t)DM * FF * 4;
constexpr size_t WS_H = WS_WD + (size_t)FF * DM * 2;
constexpr size_t WS_ST = WS_H + (size_t)SEQ * DM * 2;
constexpr size_t WS_CS = WS_ST + (size_t)SEQ * 128;
constexpr size_t WS_ACT = WS_CS + (size_t)288 * 128;
constexpr size_t WS_END = WS_ACT + (size_t)SEQ * FF * 2;

#define TSTAMP(s) do {} while (0)

namespace k1 {
constexpr int SLOT = 49152, XSCR = 3 * SLOT, LDS_BYTES = XSCR + 9216, LA = 3  , BB = 2  ;
constexpr int cstart(int c) { return c <= 8 ? 16 * c : 128 + 32 * (c - 8); }
constexpr int chunk_of(int g) { return g < 128 ? g >> 4 : 8 + ((g - 128) >> 5); }
constexpr int bslot(int c) { return cstart(c) - BB; }
constexpr int dcnt(int c) { return c < 8 ? 6 : (c < 12 ? 4 : 0); }
constexpr int kt_of(int g) { const int c = chunk_of(g), i = g - cstart(c); return c < 7 ? (i & 7) : c == 7 ? (i >> 1) : (i & 1); }
constexpr int sloc_of(int g) { const int c = chunk_of(g), i = g - cstart(c); return c < 7 ? (i >> 3) : c == 7 ? (i & 1) : (i >> 1); }
constexpr int kstep_of(int g) { const int c = chunk_of(g); return c < 8 ? 2 * c + sloc_of(g) : sloc_of(g); }
constexpr bool w_first(int g) { const int c = chunk_of(g), i = g - cstart(c); return c < 7 ? (i & 7) == 0 : (c == 7 && i < 2); }
constexpr int acc_of(int g) { const int c = chunk_of(g); return c < 8 ? kt_of(g) : 8 + 2 * (c - 8) + kt_of(g); }
constexpr int dma_at(int g) { for (int c = 0; c < 10; ++c) { const int j = g - (c ? bslot(c) : 0); if (j >= 0 && j < dcnt(c + 2)) return (c + 2) * 8 + j; } return -1; }
constexpr int pk_at(int g) {
  for (int u = 0; u < 4; ++u) { if (g == 112 + 4 * u + 6) return 2 * u; if (g == 112 + 4 * u + 7) return 2 * u + 1; }
  for (int u = 4; u < 7; ++u) { if (g == cstart(u + 5) + 3) return 2 * u; if (g == cstart(u + 5) + 4) return 2 * u + 1; }
  return -1;
}
constexpr int st_at(int g) {
  if (g < 128 || (g & 7) != 5) return -1;
  constexpr int order[16] = {0, 1, 2, 3, 8, 4, 9, 5, 10, 6, 11, 7, 12, 13, -1, -1};
  return order[(g - 128) >> 3];
}
constexpr int LN0 = 6, NLN = 48, LNRD = 72, LNST = 76;
constexpr int ln_at(int g) { return (g >= LN0 && g < LN0 + NLN) ? g - LN0 : -1; }
constexpr int WARM = 163;
constexpr int late_ops(int g) { return (st_at(g) >= 0 ? 1 : 0) + (g == LNST ? 2 : 0) + (g == WARM ? 1 : 0); }
constexpr int ops_between(int from, int to) { int n = 0; for (int g = from + 1; g < to; ++g) n += (dma_at(g) >= 0 ? 1 : 0) + late_ops(g); return n; }
constexpr int vm_wait(int c) {
  if (c == 0) return 6;
  if (c == 1) return ops_between(-1, bslot(1));
  const int from = (c == 2 ? 0 : bslot(c - 2)) + dcnt(c) - 1;
  return late_ops(from) + ops_between(from, bslot(c));
}
struct St {
  bf16x8 wf[16][2]; bf16x8 af[256]; f32x4 acc[16][2]; u32x4 pk[16];
  unsigned wp, ap[4];
  unsigned wb, ab0, ab1, voffA0, voffA1[4], voffW, m0w, sA, sW, vst, sO;
  srd_t rA, rK, rV, rO, rXN;
  u32x4 x0, x1; float v[16], t0, t1, t2; unsigned lane, sXN; bool two;
  srd_t rWm; unsigned sWm;
};
template <int C, int J> DI void dma(St& s) {
  constexpr unsigned slot = (C % 3) * SLOT;
  if constexpr (C < 8) {
    if constexpr (J < 2) dma16(s.m0w * 2048u + slot + 32768u + J * 1024u, s.voffA0, s.rA, s.sA + s.m0w * 16384u + J * 8192u + C * 128u);
    else {
      constexpr unsigned mat = (J >> 1) & 1, jj = J & 1;
      dma16(s.m0w * 2048u + slot + mat * 16384u + jj * 1024u, s.voffW, mat ? s.rV : s.rK, s.sW + s.m0w * 65536u + jj * 32768u + C * 524288u);
    }
  } else dma16(s.m0w * 4096u + slot + J * 1024u, s.voffA1[J], s.rA, s.sA + (128u + (C - 8) * 32u + J) * 1024u + s.m0w * 4096u);
}
DI const LAS char* lp(unsigned a) { return (const LAS char*)(size_t)a; }
template <int G> DI void reads(St& s) {
  if constexpr (G < 256) {
    constexpr int c = chunk_of(G), kt = kt_of(G), sl = sloc_of(G), ks = kstep_of(G);
    if constexpr (w_first(G)) {
      s.wf[ks][0] = cat8(ldstr(lp(s.wp) + sl * 8192), ldstr(lp(s.wp) + sl * 8192 + 1024));
      s.wf[ks][1] = cat8(ldstr(lp(s.wp ^ 16u) + sl * 8192), ldstr(lp(s.wp ^ 16u) + sl * 8192 + 1024));
    }
    if constexpr (c < 8) s.af[G] = lds128(lp(s.ap[sl]) + (kt >> 1) * 4096 + (kt & 1) * 1024);
    else s.af[G] = lds128(lp(s.ap[sl & 3]) + (sl >> 2) * 256 + kt * 8192);
  }
}
template <int C> DI void set_bases(St& s) {
  constexpr unsigned slot = (C % 3) * SLOT;
  if constexpr (C < 8) {
    unsigned w = s.wb + slot, a0 = s.ab0 + slot, a1 = (s.ab0 ^ 64u) + slot;
    asm volatile("" : "+v"(w), "+v"(a0), "+v"(a1));
    s.wp = w; s.ap[0] = a0; s.ap[1] = a1;
  } else {
    unsigned a0 = s.ab1 + slot, a1 = (s.ab1 ^ 64u) + slot, a2 = (s.ab1 ^ 128u) + slot, a3 = (s.ab1 ^ 192u) + slot;
    asm volatile("" : "+v"(a0), "+v"(a1), "+v"(a2), "+v"(a3));
    s.ap[0] = a0; s.ap[1] = a1; s.ap[2] = a2; s.ap[3] = a3;
  }
}
template <bool ISV, int ID> DI void pack(St& s) {
  constexpr int u = ID >> 1, j = ID & 1;
  const f32x4 a = ISV ? s.acc[2 * u][j] : s.acc[2 * u + j][0], b = ISV ? s.acc[2 * u + 1][j] : s.acc[2 * u + j][1];
  s.pk[ID] = u32x4{cvtpk(a[0], a[1]), cvtpk(a[2], a[3]), cvtpk(b[0], b[1]), cvtpk(b[2], b[3])};
  asm volatile("" : "+v"(s.pk[ID]));
}
template <int ID> DI void store(St& s) { st128m<ST1>(s.pk[ID], s.rO, s.vst, s.sO + (ID >> 1) * 8192u + (ID & 1) * 128u); }
template <int ST> DI void ln_stage(St& s) {
  if constexpr (ST < 4) {
    if constexpr (ST == 0) { asm volatile("" : "+v"(s.x0), "+v"(s.x1)); s.t0 = 0.f; s.t2 = 0.f; }
    s.v[2 * ST] = bflo(s.x0[ST]); s.v[2 * ST + 1] = bfhi(s.x0[ST]); s.t0 += s.v[2 * ST] + s.v[2 * ST + 1];
  } else if constexpr (ST < 8) { constexpr int i = ST - 4; s.v[8 + 2 * i] = bflo(s.x1[i]); s.v[9 + 2 * i] = bfhi(s.x1[i]); s.t2 += s.v[8 + 2 * i] + s.v[9 + 2 * i]; }
  else if constexpr (ST == 8) s.t0 = dpp_add<0xB1>(s.t0 + (s.two ? s.t2 : 0.f));
  else if constexpr (ST == 9) s.t0 = dpp_add<0x141>(dpp_add<0x4E>(s.t0));
  else if constexpr (ST == 10) s.t0 = dpp_add<0x140>(s.t0);
  else if constexpr (ST == 11) s.t0 = swap16_add(s.t0);
  else if constexpr (ST == 12) { s.t0 = swap32_add(s.t0) * (1.f / DM); s.t1 = 0.f; s.t2 = 0.f; }
  else if constexpr (ST < 21) { constexpr int i = 2 * (ST - 13); float& q = i < 8 ? s.t1 : s.t2; const float d = s.v[i] - s.t0, e = s.v[i + 1] - s.t0; q += d * d; q += e * e; }
  else if constexpr (ST == 21) s.t1 = dpp_add<0xB1>(s.t1 + (s.two ? s.t2 : 0.f));
  else if constexpr (ST == 22) s.t1 = dpp_add<0x141>(dpp_add<0x4E>(s.t1));
  else if constexpr (ST == 23) s.t1 = dpp_add<0x140>(s.t1);
  else if constexpr (ST == 24) s.t1 = swap16_add(s.t1);
  else if constexpr (ST == 25) s.t1 = swap32_add(s.t1) * (1.f / DM);
  else if constexpr (ST == 26) { s.t0 = rbf(s.t0); s.t1 = rbf(s.t1); }
  else if constexpr (ST == 27) s.t1 = rbf(s.t1 + 1e-5f);
  else if constexpr (ST == 28) s.t1 = rbf(__builtin_amdgcn_sqrtf(s.t1));
  else if constexpr (ST == 29) s.t1 = __builtin_amdgcn_rcpf(s.t1);
  else if constexpr (ST < 46) {
    constexpr int i = (ST - 30) >> 1; u32x4& o = i < 4 ? s.x0 : s.x1;
    if constexpr (((ST - 30) & 1) == 0) o[i & 3] = cvtpk(s.v[2 * i] - s.t0, s.v[2 * i + 1] - s.t0);
    else o[i & 3] = cvtpk(bflo(o[i & 3]) * s.t1, bfhi(o[i & 3]) * s.t1);
    asm volatile("" : "+v"(o));
  } else if constexpr (ST == 46) *(LAS u32x4*)(size_t)(XSCR + s.lane * 128u + ((s.m0w ^ (s.lane & 7)) << 4)) = s.x0;
  else { if (s.two) *(LAS u32x4*)(size_t)(XSCR + 8192u + s.lane * 128u + ((s.m0w ^ (s.lane & 7)) << 4)) = s.x1; }
  if constexpr (ST < 30) asm volatile("" : "+v"(s.t0), "+v"(s.t1), "+v"(s.t2));
}
template <bool ISV, int G> DI void slot(St& s) {
  constexpr int cn = chunk_of(G) + 1;
  constexpr bool isbar = cn < 12 && G == bslot(cn);
  if constexpr (isbar) {
    wait_bar<vm_wait(cn)>(); set_bases<cn>(s); SBAR();
    reads<G + BB>(s); reads<G + BB + 1>(s);
  } else if constexpr (!(cn < 12 && G + LA >= cstart(cn) && G < bslot(cn))) reads<G + LA>(s);
  if constexpr (dma_at(G) >= 0) dma<(dma_at(G) >> 3), (dma_at(G) & 7)>(s);
  constexpr int ai = acc_of(G), ks = kstep_of(G);
#pragma unroll
  for (int ct = 0; ct < 2; ++ct) {
    const bf16x8 a = ISV ? s.af[G] : s.wf[ks][ct], bq = ISV ? s.wf[ks][ct] : s.af[G];
    if constexpr (ks == 0) MFMA16_NEW(s.acc[ai][ct], a, bq); else MFMA16_ACC(s.acc[ai][ct], a, bq);
  }
  if constexpr (pk_at(G) >= 0) pack<ISV, pk_at(G)>(s);
  if constexpr (st_at(G) >= 0) store<st_at(G)>(s);
  if constexpr (ln_at(G) >= 0) ln_stage<ln_at(G)>(s);
  if constexpr (G == LNRD) {
    s.x0 = *(const LAS u32x4*)(size_t)(XSCR + s.m0w * 1024u + s.lane * 16u); s.x1 = *(const LAS u32x4*)(size_t)(XSCR + 8192u + s.lane * 16u);
  }
  if constexpr (G == LNST) {
    const unsigned vo = (s.lane >> 3) * 512u + (((s.lane ^ (s.lane >> 3)) & 7) << 4);
    st128m<ST1>(s.x0, s.rXN, vo, s.sXN + s.m0w * 4096u); st128m<ST1>(s.x1, s.rXN, vo, s.m0w == 0 ? s.sXN + 32768u : 0x10000000u);
  }
  if constexpr (G == WARM)
    asm volatile("s_mov_b32 m0, %0\n\ts_nop 0\n\tbuffer_load_dword %1, %2, %3 offen lds" ::"s"(XSCR + 4096u + s.m0w * 256u), "v"(s.lane < 18u ? s.lane * 128u : 0x10000000u), "s"(s.rWm), "s"(s.sWm) : "memory");
  SBAR();
}
template <bool ISV, int... Gs> DI void run(St& s, iseq<Gs...>) {
  reads<0>(s); reads<1>(s); reads<2>(s); SBAR();
  (slot<ISV, Gs>(s), ...);
  asm volatile("s_nop 7\n\ts_nop 3" ::: "memory"); SBAR();
  pack<ISV, 14>(s); store<14>(s); pack<ISV, 15>(s); store<15>(s);
}
template <int C, int... Js> DI void dma_all(St& s, iseq<Js...>) { (dma<C, Js>(s), ...); }
}

__global__ __launch_bounds__(512, 2) void k1_kvproj(const u16* __restrict__ ckv, const u16* __restrict__ wuk, const u16* __restrict__ wuv, const u16* __restrict__ x,
    const u16* __restrict__ wq, char* __restrict__ ws) {
  using namespace k1;
  const unsigned tid = threadIdx.x, lane = tid & 63, w = rfl(tid >> 6), r = lane & 15, g = lane >> 4;
  const unsigned b = blockIdx.x, xcd = b & 7, ix = b >> 3, h = xcd * 4 + (ix & 3), rb = ix >> 2;
  St s;
  s.rA = mksrd(ckv, SEQ * DC * 2); s.rK = mksrd(wuk, DC * HDM * 2); s.rV = mksrd(wuv, DC * HDM * 2);
  s.rO = mksrd(ws + (w < 4 ? WS_KF : WS_VF), SEQ * HDM * 2); s.rXN = mksrd(ws + WS_XN, SEQ * DM * 2);
  s.m0w = w; s.two = lane < 8; s.lane = lane; s.sXN = (b >> 2) * 36864u + (b & 3) * 128u;
  const unsigned q2 = (lane >> 2) & 3, p2 = lane & 3, rrow = 16u * (r >> 3) + (r & 7);
  s.voffA0 = (lane >> 3) * 1024u + (((lane & 7) ^ (4u * (w & 1) + g)) << 4);
#pragma unroll
  for (int j = 0; j < 4; ++j) s.voffA1[j] = (lane ^ (8u * (w >> 2) + ((4u * w + j) & 7u))) << 4;
  s.voffW = q2 * 8192u + g * 64u + ((p2 ^ (w & 1u)) << 4);
  s.sA = rb * 256u * 1024u; s.sW = h * 256u;
  s.wb = (w >> 2) * 16384u + (w & 3) * 256u + g * 2048u + q2 * 64u + (p2 >> 1) * 32u + (g & 1) * 16u + (p2 & 1) * 8u;
  s.ab0 = 32768u + rrow * 128u + ((g ^ (r >> 1)) << 4);
  s.ab1 = rrow * 1024u + ((g ^ r) << 4);
  s.vst = (g >> 1) * (w < 4 ? 1024u : 4096u) + (rrow + 32u * (g & 1)) * 16u;
  s.sO = (h * 64u + rb * 8u) * 8192u + (w < 4 ? w * 2048u : (w - 4) * 1024u);
  s.rWm = mksrd(wq, DM * HDM * 2); s.sWm = (b * 8u + w) * 2304u;
  {
    const srd_t rX = mksrd(x, SEQ * DM * 2);
    asm volatile("buffer_load_dwordx4 %0, %2, %4, %5 offen\n\tbuffer_load_dwordx4 %1, %3, %4, %5 offen offset:1024"
                 : "=&v"(s.x0), "=&v"(s.x1) : "v"(lane * 16u), "v"((lane & 7) * 16u), "s"(rX), "s"((b * 8u + w) * (DM * 2u)) : "memory");
  }
  dma_all<0>(s, mkseq<6>{});
  __builtin_amdgcn_s_barrier();
  dma_all<1>(s, mkseq<6>{});
  wait_bar<vm_wait(0)>(); set_bases<0>(s); SBAR();
  if (w < 4) run<false>(s, mkseq<256>{}); else run<true>(s, mkseq<256>{});
}

namespace k2 {
constexpr unsigned KR = 0, VR = 65536, SP = 131072, QR = VR  , LDS_BYTES = 163840;
constexpr float THR = 8.f;
constexpr float QSCALE = 0.08838834764831845f * 1.4426950408889634f;
DI const LAS char* lp(unsigned a) { return (const LAS char*)(size_t)a; }
DI float max3f(float a, float b, float c) { float r; asm("v_max3_f32 %0, %1, %2, %3" : "=v"(r) : "v"(a), "v"(b), "v"(c)); return r; }
DI unsigned opaque_lane() { unsigned l; asm volatile("v_mbcnt_lo_u32_b32 %0, -1, 0\n\tv_mbcnt_hi_u32_b32 %0, -1, %0" : "=v"(l)); return l; }

constexpr int QLA = 3, QBB = 2;
constexpr int qb(int c) { return 16 * c - QBB; }
constexpr int qfirst(int c) { return c == 2 ? 0 : c == 3 ? 6 : qb(c - 3); }
constexpr int qop_chunk(int g) { for (int c = 2; c < 9; ++c) { const int j = g - qfirst(c); if (j >= 0 && j < 6) return c * 8 + j; } return -1; }
constexpr int qkv_at(int g) { return (g >= 96 && g < 112 && ((g - 96) & 3) == 0) ? (g - 96) >> 2 : -1; }
constexpr int qops(int g) { return (qop_chunk(g) >= 0) + (qkv_at(g) >= 0); }
constexpr int qwait(int c) {
  if (c == 0) return 6;
  int n = 0; const int from = c == 1 ? 0 : qfirst(c) + 6;
  for (int g = from; g < qb(c); ++g) n += qops(g);
  return n;
}
struct St {
  bf16x8 xf[36], wf[144];
  bf16x8 qf[8], fr[35]; f32x16 o[4], sc[2][2], negm, oo[2]; u32x4 pf[4], out[4]; f32x4 p[8]; f32x2 st;
  float m, mo, l, ls0, ls1, mx0, mx1, f, ca, cb; bool pend;
  unsigned w, vl16, wq_rd, vW, sX, sW, qw, kb, va, vb, sKV, n0, sC0;
  srd_t rW, rX, rK, rV, rC, rWm; unsigned vWm, sWm, dummy;
  unsigned tsv, nts;
};
struct Dm { unsigned m0k, m0v, kso, vso; };
DI unsigned tile_off(const St& s, unsigned c) { return s.sKV + (c < s.n0 ? c : c - s.n0) * 32768u; }
DI Dm mkdm(const St& s, int c) {
  const unsigned par = (unsigned)(c & 1) << 15;
  return Dm{KR + par + s.w * 4096u, VR + (par ^ 32768u) + s.w * 4096u, tile_off(s, c + 2), tile_off(s, c + 1)};
}
template <int J> DI void kvdma(const St& s, const Dm& d) {
  asm volatile("s_mov_b32 m0, %0\n\ts_nop 0\n\tbuffer_load_dwordx4 %1, %2, %3 offen offset:%4 lds" ::"s"(J < 4 ? d.m0k : d.m0v), "v"(s.vl16), "s"(J < 4 ? s.rK : s.rV), "s"(J < 4 ? d.kso : d.vso), "i"((J & 3) * 1024) : "memory");
}
template <int C, int J> DI void qload(St& s) {
  if constexpr (J < 2) dma16(QR + (C & 3) * 16384u + s.w * 2048u + J * 1024u, s.vW, s.rW, s.sW + (C * 64u + J * 4u) * 8192u);
  else asm volatile("buffer_load_dwordx4 %0, %1, %2, %3 offen offset:%4" : "=v"(s.xf[4 * C + J - 2]) : "v"(s.vl16), "s"(s.rX), "s"(s.sX + C * 4096u), "i"((J - 2) * 1024) : "memory");
}
template <int C> DI void qbar(St& s) {
  asm volatile("s_waitcnt vmcnt(%4) lgkmcnt(0)\n\ts_barrier" : "+v"(s.xf[4 * C]), "+v"(s.xf[4 * C + 1]), "+v"(s.xf[4 * C + 2]), "+v"(s.xf[4 * C + 3]) : "i"(qwait(C)) : "memory");
}
constexpr int q_t(int g) { return g < 128 ? (g & 3) : ((g - 128) >> 2); }
constexpr int q_s(int g) { return g < 128 ? ((g >> 2) & 3) : (g & 3); }
template <int G> DI void qread(St& s) {
  if constexpr (G < 144) { constexpr int off = ((G >> 4) & 3) * 16384 + q_s(G) * 4096 + q_t(G) * 256; s.wf[G] = cat8(ldstr(lp(s.wq_rd) + off), ldstr(lp(s.wq_rd) + off + 1024)); }
}
template <int F> DI void qpack(St& s) {
  constexpr int T = F >> 1, o8 = (F & 1) * 8;
  const u32x4 v = {cvtpk(s.o[T][o8] * QSCALE, s.o[T][o8 + 1] * QSCALE), cvtpk(s.o[T][o8 + 2] * QSCALE, s.o[T][o8 + 3] * QSCALE),
                   cvtpk(s.o[T][o8 + 4] * QSCALE, s.o[T][o8 + 5] * QSCALE), cvtpk(s.o[T][o8 + 6] * QSCALE, s.o[T][o8 + 7] * QSCALE)};
  s.qf[F] = __builtin_bit_cast(bf16x8, v);
  *(LAS bf16x8*)((LAS char*)(size_t)s.qw + F * 1024) = s.qf[F];
}
template <int G> DI void qslot(St& s) {
  constexpr int cn = (G >> 4) + 1;
  if constexpr (cn < 9 && G == qb(cn)) { qbar<cn>(s); SBAR(); if constexpr (cn == 3 || cn == 6) TSTAMP(s); qread<G + 2>(s); qread<G + 3>(s); }
  else if constexpr (!(cn < 9 && G + QLA >= 16 * cn && G < qb(cn))) qread<G + QLA>(s);
  if constexpr (qop_chunk(G) >= 0) qload<(qop_chunk(G) >> 3), (qop_chunk(G) & 7)>(s);
  if constexpr (qkv_at(G) >= 0) kvdma<qkv_at(G)>(s, mkdm(s, -2));
  constexpr int ks = 4 * (G >> 4) + q_s(G), t = q_t(G);
  const f32x16 z = {};
  s.o[t] = MFMA32(s.wf[G], s.xf[ks], ks == 0 ? z : s.o[t]);
  if constexpr (G >= 134 && ((G - 134) & 1) == 0) qpack<((G - 134) >> 1)>(s);
  SBAR();
}
template <int... Gs> DI void qrun(St& s, iseq<Gs...>) { (qslot<Gs>(s), ...); }
template <int C, int... Js> DI void qload_all(St& s, iseq<Js...>) { (qload<C, Js>(s), ...); }

template <int I> DI void frag(St& s) {
  if constexpr (I < 16) s.fr[I] = lds128(lp(s.kb) + (I & 1) * 8192 + (I >> 1) * 1024);
  else if constexpr (I < 32) s.fr[I] = lds128(lp((I & 2) ? s.vb : s.va) + (I - 16) * 1024);
  else s.fr[I] = lds128(lp(s.kb) + ((I - 32) & 1) * 8192 + ((I - 32) >> 1) * 1024);
}
template <int X> DI void mask_diag(St& s) {
  const unsigned ln = opaque_lane();
  const int dq = 32 * ((int)(s.w & 3) - 2 * (int)(s.w >> 2)) + (int)(ln & 31) - 4 * (int)(ln >> 5);
#pragma unroll
  for (int kb = 0; kb < 2; ++kb)
#pragma unroll
    for (int r = 0; r < 16; ++r) { const int c = 32 * kb + (r & 3) + 8 * (r >> 2); s.sc[X][kb][r] = c > dq ? -__builtin_inff() : s.sc[X][kb][r]; }
}
DI void sub_ip(f32x16& v, float d) {
#pragma unroll
  for (int r = 0; r < 16; ++r) { float e = v[r]; asm("v_sub_f32_e32 %0, %0, %1" : "+v"(e) : "v"(d)); v[r] = e; }
}
DI void mul_ip(f32x16& v, float d) {
#pragma unroll
  for (int r = 0; r < 16; ++r) { float e = v[r]; asm("v_mul_f32_e32 %0, %0, %1" : "+v"(e) : "v"(d)); v[r] = e; }
}
template <int X, int E> DI void sexp(St& s) { float e = __builtin_amdgcn_exp2f(s.sc[X][E >> 4][E & 15]); asm volatile("" : "+v"(e)); s.sc[X][E >> 4][E & 15] = e; }
template <int X, int E> DI void sadd(St& s) { float& ls = X ? s.ls1 : s.ls0; if constexpr (E == 0) ls = s.sc[X][0][0]; else { ls += s.sc[X][E >> 4][E & 15]; asm volatile("" : "+v"(ls)); } }
template <int X, int P> DI void scvt(St& s) { unsigned e = cvtpk(s.sc[X][P >> 3][(2 * P) & 15], s.sc[X][P >> 3][(2 * P + 1) & 15]); asm volatile("" : "+v"(e)); s.pf[P >> 2][P & 3] = e; }
DI void mg_stats(St& s, unsigned area, float m) { s.l = swap32_add(s.l); asm volatile("" : "+v"(m)); *(LAS f32x2*)(size_t)(area + s.w * 256u + (opaque_lane() & 31) * 8u) = f32x2{m, s.l}; }
template <int I> DI void mg_write(St& s, unsigned a) { const f32x16& v = s.o[2 + (I >> 2)]; *(LAS f32x4*)((LAS char*)(size_t)a + I * 1024) = f32x4{v[4 * (I & 3)], v[4 * (I & 3) + 1], v[4 * (I & 3) + 2], v[4 * (I & 3) + 3]}; }
DI void mg_read(St& s, unsigned a, unsigned area) {
  s.st = *(const LAS f32x2*)(size_t)(area + (s.w ^ 4u) * 256u + (opaque_lane() & 31) * 8u);
#pragma unroll
  for (int i = 0; i < 8; ++i) s.p[i] = *(const LAS f32x4*)(lp(a) + i * 1024);
}
DI void mg_coef(St& s, float m) {
  const float mm = __builtin_fmaxf(m, s.st[0]), fa = __builtin_amdgcn_exp2f(m - mm), fb = __builtin_amdgcn_exp2f(s.st[0] - mm);
  const float il = __builtin_amdgcn_rcpf(s.l * fa + s.st[1] * fb); s.ca = fa * il; s.cb = fb * il;
}
template <int I> DI void mg_final(St& s) {
  float v[8];
#pragma unroll
  for (int e = 0; e < 8; ++e) v[e] = s.oo[I >> 1][8 * (I & 1) + e] * s.ca + s.p[2 * I + (e >> 2)][e & 3] * s.cb;
  s.out[I] = u32x4{cvtpk(v[0], v[1]), cvtpk(v[2], v[3]), cvtpk(v[4], v[5]), cvtpk(v[6], v[7])};
  asm volatile("" : "+v"(s.out[I]));
}
template <int I> DI void mg_store(St& s, unsigned blk) {
  const unsigned ln = opaque_lane();
  st128m<ST2>(s.out[I], s.rC, ((ln >> 4) & 1) * 131072u + (ln & 15) * 16u + (ln >> 5) * 256u, s.sC0 + blk * 1048576u + I * 512u);
}
template <int X, int KIND, bool DIAG, int G> DI void aslot(St& s, int c) {
  constexpr int Y = X ^ 1; constexpr bool PREV = KIND != 0, FRESH = KIND == 0 || KIND == 2;
  if constexpr (G == 30) {
    wait_bar<KIND == 3 ? 5 : 1>();
    TSTAMP(s);
    s.kb ^= 32768u; s.va ^= 32768u; s.vb ^= 32768u; asm volatile("" : "+v"(s.kb), "+v"(s.va), "+v"(s.vb)); SBAR();
    frag<32>(s); frag<33>(s);
  } else if constexpr (G + 3 < 16) frag<G + 3>(s);
  else if constexpr (G + 3 < 32) { if constexpr (PREV) frag<G + 3>(s); }
  else if constexpr (G == 31) frag<34>(s);
  const f32x16 z = {};
  if constexpr (G < 16) s.sc[X][G & 1] = MFMA32(s.fr[G], s.qf[G >> 1], G < 2 ? (FRESH ? z : s.negm) : s.sc[X][G & 1]);
  else if constexpr (PREV) s.o[G & 3] = MFMA32(s.fr[G], __builtin_bit_cast(bf16x8, s.pf[(G - 16) >> 2]), (KIND == 3 && G < 20) ? z : s.o[G & 3]);
  if constexpr (KIND == 0) {
    if constexpr (G == 1) asm volatile("s_waitcnt lgkmcnt(0)\n\ts_barrier" ::: "memory");
    if constexpr (G >= 1 && G < 5) kvdma<G - 1>(s, mkdm(s, -1));
  } else if constexpr (KIND != 3) {
    if constexpr (G < 12 && (G & 1)) kvdma<(G >> 1) + 2>(s, mkdm(s, c - 1));
  }
  if constexpr (KIND == 3) {
    const Dm d = mkdm(s, c - 1);
    if constexpr (G == 0) { mg_stats(s, SP, s.mo); s.qw = (s.w < 4u ? d.m0k : d.m0v - 32768u) + s.w * 4096u + s.vl16; asm volatile("" : "+v"(s.qw)); }
    if constexpr (G >= 2 && G < 10) mg_write<G - 2>(s, s.qw);
    if constexpr (G == 10) {
      asm volatile("s_waitcnt lgkmcnt(0)\n\ts_barrier" ::: "memory");
      unsigned a = (s.w < 4u ? d.m0v : d.m0k - 16384u) + (s.w & 3) * 4096u + s.vl16; asm volatile("" : "+v"(a));
      mg_read(s, a, SP); s.oo[0] = s.o[0]; s.oo[1] = s.o[1];
    }
    if constexpr (G == 12) mg_coef(s, s.mo);
    if constexpr (G == 13) asm volatile("s_waitcnt lgkmcnt(0)\n\ts_barrier" ::: "memory");
    if constexpr (G >= 13 && G < 21) kvdma<G - 13>(s, d);
    if constexpr (G >= 13 && G < 25 && (G - 13) % 3 == 0) mg_final<(G - 13) / 3>(s);
    if constexpr (G >= 24 && G < 28) mg_store<G - 24>(s, s.n0 - 1u);
  }
  if constexpr (G == 28)
    asm volatile("buffer_load_dword %0, %1, %2, %3 offen" : "+v"(s.dummy) : "v"(s.vWm), "s"(s.rWm), "s"(
      s.sWm + c * 640u
      ) : "memory");
  if constexpr (PREV) {
    if constexpr (G < 26) sexp<Y, G + 6>(s);
    if constexpr (G < 27) sadd<Y, G + 5>(s);
    if constexpr (G < 27 && !(G & 1)) scvt<Y, (G >> 1) + 2>(s);
    if constexpr (G == 27) { const float ls = Y ? s.ls1 : s.ls0; if constexpr (KIND == 3) s.l = ls; else s.l += ls; asm volatile("" : "+v"(s.l)); }
  }
  if constexpr (DIAG && G == 16) {
    mask_diag<X>(s);
    unsigned qa = SP + (s.w & 3) * 8192u + s.vl16; asm volatile("" : "+v"(qa));
#pragma unroll
    for (int i = 0; i < 8; ++i) s.qf[i] = lds128(lp(qa) + i * 1024);
  }
  if constexpr (G >= 17 && G < 25) {
    constexpr int i = G - 17, kb = i >> 2, r = 4 * (i & 3);
    if constexpr (i == 0) { s.mx0 = max3f(s.sc[X][0][0], s.sc[X][0][1], s.sc[X][0][1]); s.mx1 = max3f(s.sc[X][0][2], s.sc[X][0][3], s.sc[X][0][3]); }
    else { s.mx0 = max3f(s.mx0, s.sc[X][kb][r], s.sc[X][kb][r + 1]); s.mx1 = max3f(s.mx1, s.sc[X][kb][r + 2], s.sc[X][kb][r + 3]); }
    asm volatile("" : "+v"(s.mx0), "+v"(s.mx1));
  }
  if constexpr (G == 25) {
    const float mx = max3f(s.mx0, s.mx1, s.mx1);
    if constexpr (FRESH) {
      const float rm = __builtin_fmaxf(swap32_max(mx), -1e30f);
      if constexpr (KIND == 2) s.mo = s.m; else s.l = 0.f;
      s.m = rm; sub_ip(s.sc[X][0], rm); sub_ip(s.sc[X][1], rm);
#pragma unroll
      for (int r = 0; r < 16; ++r) s.negm[r] = -rm;
    } else if (__builtin_expect(__any(mx > THR), 0)) {
      const float dl = __builtin_fmaxf(swap32_max(mx), 0.f);
      s.m += dl; s.f = __builtin_amdgcn_exp2f(-dl); s.pend = true;
      sub_ip(s.sc[X][0], dl); sub_ip(s.sc[X][1], dl);
#pragma unroll
      for (int r = 0; r < 16; ++r) { float e = s.negm[r]; asm("v_sub_f32_e32 %0, 0, %1" : "+v"(e) : "v"(s.m)); s.negm[r] = e; }
    }
  }
  if constexpr (G >= 26) sexp<X, G - 26>(s);
  if constexpr (G >= 27) sadd<X, G - 27>(s);
  if constexpr (G == 28 || G == 30) scvt<X, ((G - 28) >> 1)>(s);
  if constexpr (KIND != 2 && G >= 30) kvdma<G - 30>(s, mkdm(s, c));
  SBAR();
}
template <int X, int KIND, bool DIAG, int... Gs> DI void astep_(St& s, int c, iseq<Gs...>) {
  if constexpr (KIND == 0) { frag<32>(s); frag<33>(s); frag<34>(s); SBAR(); }
  s.fr[0] = s.fr[32]; s.fr[1] = s.fr[33]; s.fr[2] = s.fr[34];
  (aslot<X, KIND, DIAG, Gs>(s, c), ...);
  if constexpr (KIND == 1 || KIND == 3) {
    if (__builtin_expect(s.pend, 0)) {
      mul_ip(s.o[0], s.f); mul_ip(s.o[1], s.f); mul_ip(s.o[2], s.f); mul_ip(s.o[3], s.f); s.l *= s.f;
      s.pend = false;
    }
  }
}
template <int X, int KIND, bool DIAG = false> DI void astep(St& s, int c) { astep_<X, KIND, DIAG>(s, c, mkseq<32>{}); }
template <int T> DI void tslot(St& s) {
  if constexpr (T == 0) { frag<16>(s); frag<17>(s); frag<18>(s); }
  else if constexpr (T + 17 < 32 && T >= 2) frag<T + 17>(s);
  if constexpr (T >= 2) s.o[(T - 2) & 3] = MFMA32(s.fr[T + 14], __builtin_bit_cast(bf16x8, s.pf[(T - 2) >> 2]), s.o[(T - 2) & 3]);
  if constexpr (T < 13) { sexp<1, 6 + 2 * T>(s); sexp<1, 7 + 2 * T>(s); }
  if constexpr (T > 0 && T < 14) sadd<1, 4 + 2 * T>(s);
  if constexpr (T < 14) { sadd<1, 5 + 2 * T>(s); scvt<1, T + 2>(s); }
  if constexpr (T == 14) { s.l += s.ls1; mg_stats(s, VR + 32768u, s.m); }
  SBAR();
}
template <int... Ts> DI void atail(St& s, iseq<Ts...>) { (tslot<Ts>(s), ...); }
template <int... Is> DI void mg_write_all(St& s, unsigned a, iseq<Is...>) { (mg_write<Is>(s, a), ...); }
}

__global__ __launch_bounds__(512, 2) void k2_attn(const u16* __restrict__ wq, const u16* __restrict__ wo, const u16* __restrict__ wg, const u16* __restrict__ wu, const u16* __restrict__ wd, char* __restrict__ ws) {
  using namespace k2;
  const unsigned tid = threadIdx.x, lane = tid & 63, w = rfl(tid >> 6), hi = lane >> 5, rq = w & 3, g = w >> 2;
  const unsigned b = blockIdx.x, xcd = b & 7, ix = b >> 3, h = xcd * 4 + (ix & 3), x = ix >> 2;
  St s;
  s.w = w; s.vl16 = lane * 16u;
  TSTAMP(s);
  const unsigned q2 = (lane >> 2) & 3, p2 = lane & 3, g4 = lane >> 4;
  s.rW = mksrd(wq, DM * HDM * 2); s.rX = mksrd(ws + WS_XN, SEQ * DM * 2); s.rK = mksrd(ws + WS_KF, SEQ * HDM * 2); s.rV = mksrd(ws + WS_VF, SEQ * HDM * 2);
  s.rC = mksrd(ws + WS_CTX, SEQ * HDM * 2);
  s.vW = q2 * 8192u + g4 * 64u + p2 * 16u; s.sW = h * 256u + w * 65536u;
  s.wq_rd = QR + hi * 2048u + q2 * 64u + (g4 & 1) * 32u + p2 * 8u; asm volatile("" : "+v"(s.wq_rd));
  s.sX = ((g == 0 ? x : 15u - x) * 4u + rq) * 36864u;
  s.sKV = h * 524288u + w * 4096u; s.n0 = x + 1u; s.sC0 = (2u * rq * 128u + 4u * h + 2u * g) * 1024u;
  s.qw = (g == 0 ? KR + 32768u : SP) + rq * 8192u + s.vl16; asm volatile("" : "+v"(s.qw));
  qload_all<0>(s, mkseq<6>{});
  __builtin_amdgcn_s_barrier();
  qload_all<1>(s, mkseq<6>{});
  qbar<0>(s); SBAR(); TSTAMP(s);
  qread<0>(s); qread<1>(s); qread<2>(s); SBAR();
  qrun(s, mkseq<144>{});
  qpack<5>(s); qpack<6>(s); qpack<7>(s);
  TSTAMP(s);
  wait_bar<0>();
  if (g == 1) {
    unsigned ra = KR + 32768u + rq * 8192u + s.vl16; asm volatile("" : "+v"(ra));
#pragma unroll
    for (int i = 0; i < 8; ++i) s.qf[i] = lds128(lp(ra) + i * 1024);
  }
  { const Dm d = mkdm(s, -1); kvdma<4>(s, d); kvdma<5>(s, d); kvdma<6>(s, d); kvdma<7>(s, d); }
  {
    const unsigned W = b * 8u + w;
    s.rWm = mksrd(W < 434u ? wo : W < 922u ? wg : W < 1410u ? wu : wd, W < 434u ? HDM * DM * 2 : DM * FF * 2);
    s.sWm = (W < 434u ? W : W < 922u ? W - 434u : W < 1410u ? W - 922u : W - 1410u) * 10880u;
    const unsigned l = opaque_lane();
    s.vWm = l < 5u ? l * 128u : 0x10000000u;
    s.dummy = 0;
  }
  s.kb = KR + g * 16384u + s.vl16; s.va = VR + 32768u + g * 18432u + s.vl16; s.vb = VR + 32768u + g * 14336u + s.vl16; s.pend = false;
  asm volatile("" : "+v"(s.kb), "+v"(s.va), "+v"(s.vb));
#pragma unroll
  for (int d = 0; d < 4; ++d) s.o[d] = f32x16{};
  TSTAMP(s);
  if (x == 0) astep<1, 0, true>(s, 0); else astep<0, 0>(s, 0);
  int c = 1;
  for (int pass = 0; pass < 2; ++pass) {
    if (pass == 1 || x != 0) {
      const int cend = pass == 0 ? (int)x : 16;
      for (; c + 1 < cend; c += 2) { astep<1, 1>(s, c); astep<0, 1>(s, c + 1); }
      if (c < cend) { astep<1, 1>(s, c); ++c; s.sc[0][0] = s.sc[1][0]; s.sc[0][1] = s.sc[1][1]; s.ls0 = s.ls1; }
      astep<1, 1, true>(s, c); ++c;
    }
    if (pass == 0) { astep<0, 2>(s, c); astep<1, 3>(s, c + 1); astep<0, 1>(s, c + 2); c += 3; }
  }
  TSTAMP(s);
  atail(s, mkseq<18>{});
  TSTAMP(s);
  {
    unsigned mine = (g == 0 ? SP : KR + 32768u) + rq * 8192u + s.vl16, theirs = (g == 0 ? KR + 32768u : SP) + rq * 8192u + s.vl16; asm volatile("" : "+v"(mine), "+v"(theirs));
    mg_write_all(s, mine, mkseq<8>{});
    asm volatile("s_waitcnt lgkmcnt(0)\n\ts_barrier" ::: "memory");
    TSTAMP(s);
    mg_read(s, theirs, VR + 32768u); s.oo[0] = s.o[0]; s.oo[1] = s.o[1];
    mg_coef(s, s.m);
    mg_final<0>(s); mg_store<0>(s, 15u - x); mg_final<1>(s); mg_store<1>(s, 15u - x); mg_final<2>(s); mg_store<2>(s, 15u - x); mg_final<3>(s); mg_store<3>(s, 15u - x);
  }
  TSTAMP(s);
  wait_vm<4>();
  asm volatile("" ::"v"(s.dummy));
}

#define K3_OPS 9
#define K3_AAUX ""
#define K3_RA 0u
#define K3_RW 0u
namespace k3 {
#define K3_P 2
constexpr int P = K3_P, DIST = P - 1, WSLOT = 4096, LDS_BYTES = 163840, GRID = 256;
DI const LAS char* lp(unsigned a) { return (const LAS char*)(size_t)a; }
struct St {
  bf16x8 af[P][5], wf[4]; f32x4 acc[4][5];
  unsigned vl16, vW, rd[4], m0b, sA[5], sW, rot, kcur;
  srd_t rA, rW;
  unsigned tsv, nts;
};
template <int NS, int I, int J> DI void load(St& s) {
  constexpr int sl = I % P;
  if constexpr (J == 0) { const unsigned k = s.rot + I; s.kcur = k >= (unsigned)NS ? k - NS : k; }
  if constexpr (J < 4) dma16(s.m0b + sl * WSLOT + J * 1024u, s.vW, s.rW, s.sW + (s.kcur * 32u + J * 8u) * (DM * 2u));
  else asm volatile("buffer_load_dwordx4 %0, %1, %2, %3 offen" : "=v"(s.af[sl][J - 4]) : "v"(s.vl16), "s"(s.rA), "s"(s.sA[J - 4] + s.kcur * 1024u) : "memory");
}
template <int NS, int I, int... Js> DI void load_all(St& s, iseq<Js...>) { (load<NS, I, Js>(s), ...); }
template <int NS, int E1, int I> DI void step(St& s) {
  constexpr int sl = I % P, ahead = (NS - 1 - I) < (DIST - 1) ? (NS - 1 - I) : (DIST - 1);
  asm volatile("s_waitcnt vmcnt(%5)" : "+v"(s.af[sl][0]), "+v"(s.af[sl][1]), "+v"(s.af[sl][2]), "+v"(s.af[sl][3]), "+v"(s.af[sl][4]) : "i"(9 * ahead) : "memory");
  if constexpr ((I & 1) == 0) TSTAMP(s);
#pragma unroll
  for (int t = 0; t < 4; ++t) s.wf[t] = cat8(ldstr(lp(s.rd[t]) + sl * WSLOT), ldstr(lp(s.rd[t]) + sl * WSLOT + E1 * 128));
  SBAR();
  const f32x4 z = {};
#define K3_MM(t, r) s.acc[t][r] = MFMA16(s.wf[t], s.af[sl][r], I == 0 ? z : s.acc[t][r])
#define K3_LD(j) if constexpr (I + DIST < NS) load<NS, I + DIST, j>(s)
  K3_LD(0); K3_MM(0, 0); K3_MM(0, 1); SBAR(); K3_LD(1); K3_MM(0, 2); K3_MM(0, 3); SBAR();
  K3_LD(2); K3_MM(0, 4); K3_MM(1, 0); SBAR(); K3_LD(3); K3_MM(1, 1); K3_MM(1, 2); SBAR();
  K3_LD(4); K3_MM(1, 3); K3_MM(1, 4); SBAR(); K3_LD(5); K3_MM(2, 0); K3_MM(2, 1); SBAR();
  K3_LD(6); K3_MM(2, 2); K3_MM(2, 3); SBAR(); K3_LD(7); K3_MM(2, 4); K3_MM(3, 0); SBAR();
  K3_LD(8); K3_MM(3, 1); K3_MM(3, 2); K3_MM(3, 3); K3_MM(3, 4); SBAR();
#undef K3_MM
#undef K3_LD
}
template <int NS, int E1, int... Is> DI void run(St& s, iseq<Is...>) { (step<NS, E1, Is>(s), ...); }
template <int NS, int... Is> DI void prologue(St& s, iseq<Is...>) { (load_all<NS, Is>(s, mkseq<9>{}), ...); }

DI void repack(const u16* wg, const u16* wu, char* ws, unsigned wgi, unsigned w, unsigned lane) {
  const unsigned task = wgi * 8u + w, blk = task >> 1, half = task & 1;
  const bool on = blk < 72u;
  const srd_t rG = mksrd(wg, DM * FF * 2), rU = mksrd(wu, DM * FF * 2), rP = mksrd(ws + WS_WGU, DM * FF * 4);
  const unsigned base = w * 20480u, row = lane >> 3;
  const unsigned vg = row * (FF * 2u) + (((lane & 7) ^ (((row >> 1) & 1) << 2)) << 4), vu = vg ^ 32u;
  const unsigned ssrc = on ? blk * 128u + half * 288u * (FF * 2u) : 0x10000000u;
  unsigned sdst[4];
#pragma unroll
  for (int p = 0; p < 4; ++p) sdst[p] = (on && (blk * 4u + p) % 9u >= 4u) ? (blk * 4u + p) * 36864u + half * 18432u : 0x10000000u;
  const unsigned hi = lane >> 5, mat = (lane >> 4) & 1, q = (lane >> 2) & 3;
  const unsigned rd = base + mat * 2048u + (8u * hi + q) * 128u + (((q >> 1) << 6) ^ (mat << 5)) + (lane & 3) * 8u;
  const bf16x8 ones = {0x3f80, 0x3f80, 0x3f80, 0x3f80, 0x3f80, 0x3f80, 0x3f80, 0x3f80};
  f32x16 cs[4] = {};
  auto fetch = [&](unsigned st, unsigned slot) {
#pragma unroll
    for (int j = 0; j < 4; ++j) dma16(base + slot * 4096u + j * 1024u, j < 2 ? vg : vu, j < 2 ? rG : rU, ssrc + (st * 16u + (j & 1) * 8u) * (FF * 2u));
  };
#pragma unroll
  for (int i = 0; i < 4; ++i) fetch(i, i);
  for (unsigned st = 0; st < 18u; ++st) {
    const unsigned slot = st % 5u;
    if (st < 4u) wait_vm<12>(); else wait_vm<24>();
    bf16x8 f[4];
#pragma unroll
    for (int p = 0; p < 4; ++p) { const unsigned a = (rd + slot * 4096u) ^ (p << 5); f[p] = cat8(ldstr(lp(a)), ldstr(lp(a) + 512)); }
    fetch(st + 4u < 18u ? st + 4u : st, (st + 4u) % 5u);
#pragma unroll
    for (int p = 0; p < 4; ++p) { cs[p] = MFMA32(f[p], ones, cs[p]); st128m<0>(__builtin_bit_cast(u32x4, f[p]), rP, lane * 16u, sdst[p] + st * 1024u); }
  }
  wait_vm<0>();
  if ((lane & 31) == 0) {
#pragma unroll
    for (int p = 0; p < 4; ++p)
#pragma unroll
      for (int r4 = 0; r4 < 4; ++r4) *(LAS f32x4*)(size_t)(base + p * 128u + r4 * 32u + hi * 16u) = f32x4{cs[p][4 * r4], cs[p][4 * r4 + 1], cs[p][4 * r4 + 2], cs[p][4 * r4 + 3]};
  }
  asm volatile("s_waitcnt lgkmcnt(0)\n\ts_barrier" ::: "memory");
  if (on && half == 0) {
    const f32x2 a = *(const LAS f32x2*)(size_t)(base + lane * 8u), c = *(const LAS f32x2*)(size_t)(base + 20480u + lane * 8u);
    *(f32x2*)(ws + WS_CS + blk * 512u + lane * 8u) = a + c;
  }
}
template <int NK32, bool FIRST> DI void body(const char* A, const u16* W, const u16* x, const u16* wg, const u16* wu, char* ws, u16* out) {
  constexpr int NS = NK32 / 8;
  const unsigned tid = threadIdx.x, lane = tid & 63, w = rfl(tid >> 6), g = lane >> 4, q2 = (lane >> 2) & 3, p2 = lane & 3;
  const unsigned b = blockIdx.x, tile = (b & 7) * 30u + (b >> 3);
  if (b >= 240u || tile >= 234u) {
    if constexpr (FIRST) repack(wg, wu, ws, b >= 240u ? b - 234u : tile - 234u, w, lane);
    return;
  }
  const unsigned rbk = tile / 9u, cb = tile - rbk * 9u;
  St s;
  TSTAMP(s);
  s.rA = mksrd(A, SEQ * NK32 * 64); s.rW = mksrd(W, NK32 * 32 * DM * 2);
  s.vl16 = lane * 16u;
  s.vW = (lane >> 3) * (DM * 2u) + (((lane & 7) ^ (((lane >> 4) & 3) << 1)) << 4);
  s.m0b = 81920u + w * (P * WSLOT);        s.sW = w * NS * 32u * (DM * 2u) + cb * 128u;
#pragma unroll
  for (int r = 0; r < 5; ++r) s.sA[r] = ((rbk * 5u + r) * NK32 + w * NS) * 1024u;
  s.rot = (K3_RA * cb + K3_RW * (rbk & 3)) % NS;
  const unsigned krow = 16u * (g >> 1) + 4u * (g & 1) + q2;
#pragma unroll
  for (int t = 0; t < 4; ++t) { s.rd[t] = s.m0b + krow * 128u + ((((krow >> 1) & 3) ^ t) << 5) + p2 * 8u; asm volatile("" : "+v"(s.rd[t])); }
  const unsigned pc = tid & 7, m0 = tid >> 3;
  unsigned vrm[2], vfr[2];
#pragma unroll
  for (int i = 0; i < 2; ++i) {
    const unsigned m = m0 + 64u * i, row = rbk * 80u + m; const bool ok = m < 80u && row < (unsigned)SEQ;
    vrm[i] = ok ? row * (DM * 2u) + cb * 128u + pc * 16u : 0x10000000u;
    vfr[i] = ok ? (row >> 5) * 36864u + (cb * 8u + pc) * 512u + (row & 31) * 16u : 0x10000000u;
  }
  u32x4 res[2];
  {
    const srd_t rR = FIRST ? mksrd(x, SEQ * DM * 2) : mksrd(ws + WS_H, SEQ * DM * 2);
    asm volatile("buffer_load_dwordx4 %0, %2, %4, 0 offen\n\tbuffer_load_dwordx4 %1, %3, %4, 0 offen"
                 : "=&v"(res[0]), "=&v"(res[1]) : "v"(FIRST ? vrm[0] : vfr[0]), "v"(FIRST ? vrm[1] : vfr[1]), "s"(rR) : "memory");
  }
  prologue<NS>(s, mkseq<DIST>{});
  TSTAMP(s);
  run<NS, 8>(s, mkseq<NS>{});
  TSTAMP(s);
  asm volatile("" : "+v"(res[0]), "+v"(res[1]));
  {
    unsigned pa = (w & 3) * 20480u + s.vl16; asm volatile("" : "+v"(pa));
    if (w < 4u) {
#pragma unroll
      for (int i = 0; i < 20; ++i) *(LAS f32x4*)((LAS char*)(size_t)pa + i * 1024) = s.acc[i / 5][i % 5];
    }
    asm volatile("s_waitcnt lgkmcnt(0)\n\ts_barrier" ::: "memory");
    if (w >= 4u) {
#pragma unroll
      for (int i = 0; i < 20; ++i) s.acc[i / 5][i % 5] += *(const LAS f32x4*)(lp(pa) + i * 1024);
#pragma unroll
      for (int t = 0; t < 4; ++t) {
        unsigned wa = (w & 3) * 20480u + (lane & 15) * 256u + (((4u * t + g) ^ (lane & 15)) << 4); asm volatile("" : "+v"(wa), "+v"(s.acc[3][4]));
#pragma unroll
        for (int r = 0; r < 5; ++r) *(LAS f32x4*)((LAS char*)(size_t)wa + r * 4096) = s.acc[t][r];
      }
    }
  }
  asm volatile("s_waitcnt lgkmcnt(0)\n\ts_barrier" ::: "memory");
  TSTAMP(s);
  const srd_t rO = FIRST ? mksrd(ws + WS_H, SEQ * DM * 2) : mksrd(out, SEQ * DM * 2);
#pragma unroll
  for (int i = 0; i < 2; ++i) {
    if (i == 1 && w >= 2) break;
    unsigned ra = (m0 + 64u * i) * 256u + (((2u * pc) ^ (m0 & 15)) << 4); asm volatile("" : "+v"(ra));
    f32x4 a = *(const LAS f32x4*)(lp(ra)), c = *(const LAS f32x4*)(lp(ra ^ 16u));
#pragma unroll
    for (int ww = 1; ww < 4; ++ww) { a += *(const LAS f32x4*)(lp(ra) + ww * 20480); c += *(const LAS f32x4*)(lp(ra ^ 16u) + ww * 20480); }
    const unsigned y[4] = {cvtpk(a[0], a[1]), cvtpk(a[2], a[3]), cvtpk(c[0], c[1]), cvtpk(c[2], c[3])};
    u32x4 o;
#pragma unroll
    for (int e = 0; e < 4; ++e) o[e] = cvtpk(bflo(res[i][e]) + bflo(y[e]), bfhi(res[i][e]) + bfhi(y[e]));
    st128m<FIRST ? ST3 : ST5>(o, rO, FIRST ? vfr[i] : vrm[i], 0);
    if constexpr (FIRST) {
      float sm = 0.f;
#pragma unroll
      for (int e = 0; e < 4; ++e) sm += bflo(o[e]) + bfhi(o[e]);
      const float mean = dpp_add<0x141>(dpp_add<0x4E>(dpp_add<0xB1>(sm))) * (1.f / 64.f);
      float q = 0.f;
#pragma unroll
      for (int e = 0; e < 4; ++e) { const float d0 = bflo(o[e]) - mean, d1 = bfhi(o[e]) - mean; q += d0 * d0 + d1 * d1; }
      q = dpp_add<0x141>(dpp_add<0x4E>(dpp_add<0xB1>(q)));
      const unsigned m = m0 + 64u * i, row = rbk * 80u + m;
      if (pc == 0 && m < 80u && row < (unsigned)SEQ) *(f32x2*)(ws + WS_ST + (row * 9u + cb) * 8u) = f32x2{mean, q};
    }
  }
}
}
__global__ __launch_bounds__(512, 2) void k3_oproj(const u16* __restrict__ wo, const u16* __restrict__ x, const u16* __restrict__ wg, const u16* __restrict__ wu, char* __restrict__ ws) { k3::body<128, true>(ws + WS_CTX, wo, x, wg, wu, ws, nullptr); }
__global__ __launch_bounds__(512, 2) void k5_down(const u16* __restrict__ wd, char* __restrict__ ws, u16* __restrict__ out) { k3::body<144, false>(ws + WS_ACT, wd, nullptr, nullptr, nullptr, ws, out); }

namespace k4 {
constexpr int SLOT = 36864, CSOFF = 4 * SLOT, LDS_BYTES = CSOFF + 2048, LA = 3, BB = 2, NU = 324;
DI const LAS char* lp(unsigned a) { return (const LAS char*)(size_t)a; }
constexpr int u_pair(int g) { return g < 112 ? g & 3 : g < 144 ? (g - 112) >> 3 : 4 + (g - 144) / 36; }
constexpr int u_kstep(int g) { return g < 112 ? g >> 2 : g < 144 ? 28 + (g & 7) : (g - 144) % 36; }
constexpr int pair_end(int p) { return p < 4 ? 119 + 8 * p : 144 + 36 * (p - 4) + 35; }
constexpr int NE = 13;
constexpr int eks(int e) { return e < 8 ? 2 * e : 18; }
constexpr int estart(int e) { return e < 8 ? 16 * e : 144 + 36 * (e - 8); }
constexpr int bslot(int e) { return estart(e) - BB; }
constexpr int epoch_of(int g) { int e = 0; while (e + 1 < NE && estart(e + 1) <= g) ++e; return e; }
#define K4_NPRO 12
#define K4_RATE 2
constexpr int NPRO = K4_NPRO;
constexpr int s_lo(int g) { const int n = NPRO + (g + K4_RATE - 1) / K4_RATE; return n < 54 ? n : 54; }
constexpr int SEND = (54 - NPRO) * K4_RATE;
constexpr int cfirst(int p) { return p == 4 ? SEND + 1 : p == 5 ? SEND + 16 : p == 6 ? bslot(7) + 3 : p == 7 ? bslot(8) + 1 : bslot(9) + 1; }
static_assert(cfirst(4) > bslot(3) && cfirst(5) > bslot(5), "slot reuse");
constexpr int op_at(int g) {
  for (int p = 4; p < 9; ++p) { const int j = g - cfirst(p); if (j >= 0 && j < 15 && j % 3 == 0) return 100 + 8 * (p - 4) + j / 3; }
  if (g == SEND) return 204;
  return -1;
}
constexpr int NEP = 30, SP = 24;
constexpr int ep_start(int p) { int s = 0; for (int q = 0; q <= p; ++q) { const int a = pair_end(q) + 3; s = (q == 0 || a > s + SP) ? a : s + SP; } return s; }
static_assert(ep_start(7) + NEP <= NU, "epilogues must fit");
constexpr int st_ops(int g) { for (int p = 0; p < 8; ++p) if (g == ep_start(p) + NEP - 2) return 1; return 0; }
constexpr bool sure(int op) { return op >= 0 && !(op >= 100 && op < 200 && ((op - 100) & 7) == 4) && op != 204; }
constexpr int vm_wait(int e) {
  const int to = e ? bslot(e) : 0;
  if (e < 7) { int n = s_lo(to) - 3 * eks(e + 1); for (int g = 0; g < to; ++g) n += sure(op_at(g)) ? 1 : 0; return n; }
  const int want = e == 7 ? 204 : 100 + 8 * (e - 8) + 4;
  int from = 0; while (op_at(from) != want) ++from;
  int n = st_ops(from);
  for (int g = from + 1; g < to; ++g) n += (sure(op_at(g)) ? 1 : 0) + st_ops(g);
  return n;
}
constexpr bool sched_ok() { for (int e = 1; e < 7; ++e) if (s_lo(bslot(e)) < 3 * eks(e + 1)) return false; return SEND < bslot(7); }
static_assert(sched_ok(), "stream ops must precede their barriers");
constexpr int ST0 = 2;
DI unsigned olane() { unsigned l; asm volatile("v_mbcnt_lo_u32_b32 %0, -1, 0\n\tv_mbcnt_hi_u32_b32 %0, -1, %0" : "=v"(l)); return l; }
struct St {
  bf16x8 hf[36], wf[NU]; f32x16 acc[9];
  f32x4 sp0, sp1; f32x2 sq, csg, csu, csg2, csu2; float ag, au, x0, x1, t0, t1, d0, o0, o1; u32x4 pk;
  float nmean, c1, c2;
  unsigned w, wp, wp1, wbA, vA, sA, sW, sHh, csb, vl16, vst, sAct, vsp, p0;
  srd_t rR, rW, rH, rS, rCS, rAct;
  unsigned tsv, nts;
};
template <int OP> DI void vop(St& s) {
  if constexpr (OP < 54) {
    constexpr int ks = OP / 3, j = OP % 3;
    if constexpr (j < 2) asm volatile("buffer_load_dwordx4 %0, %1, %2, %3 offen offset:%4" : "=v"(s.hf[2 * ks + j]) : "v"(s.vl16), "s"(s.rH), "s"(s.sHh + (ks >> 1) * 4096u), "i"(((ks & 1) * 2 + j) * 1024) : "memory");
    else dma16(ks * 8192u + s.w * 1024u, s.vA, s.rR, s.sA + ks * 32u * (FF * 2u));
  } else if constexpr (OP < 200) {
    constexpr int P = 4 + ((OP - 100) >> 3), J = (OP - 100) & 7;
    if (J < 4 || s.w < 4u) dma16((P & 3) * SLOT + J * 8192u + s.w * 1024u, s.vl16, s.rW, s.sW + P * 36864u + J * 8192u + s.w * 1024u);
  } else if constexpr (OP == 200) asm volatile("buffer_load_dwordx4 %0, %1, %2, 0 offen" : "=v"(s.sp0) : "v"(s.vsp), "s"(s.rS) : "memory");
  else if constexpr (OP == 201) asm volatile("buffer_load_dwordx4 %0, %1, %2, 0 offen offset:16" : "=v"(s.sp1) : "v"(s.vsp), "s"(s.rS) : "memory");
  else if constexpr (OP == 202) asm volatile("buffer_load_dwordx2 %0, %1, %2, 0 offen offset:64" : "=v"(s.sq) : "v"(s.csb), "s"(s.rS) : "memory");
  else {
    s.csb = CSOFF + (s.vl16 >> 9) * 16u; asm volatile("" : "+v"(s.csb));
    if (s.w < 2u) dma16(CSOFF + s.w * 1024u, s.vl16, s.rCS, s.p0 * 128u + s.w * 1024u);
  }
}
template <int O0, int... Os> DI void vops(St& s, iseq<Os...>) { (vop<O0 + Os>(s), ...); }
template <int G> DI void reads(St& s) {
  if constexpr (G < NU) {
    constexpr int p = u_pair(G), ks = u_kstep(G);
    if constexpr (G < 144) {
      constexpr int off = ((ks >> 1) - eks(epoch_of(G))) * 8192 + (ks & 1) * 2048 + (p >> 1) * 256; const unsigned a = (p & 1) ? s.wp1 : s.wp;
      s.wf[G] = cat8(ldstr(lp(a) + off), ldstr(lp(a) + off + 512));
    } else s.wf[G] = lds128(lp(s.wp) + ks * 1024);
  }
}
template <int K> DI void pin1(St& s) { asm volatile("" : "+v"(s.hf[2 * K]), "+v"(s.hf[2 * K + 1])); }
template <int E, int... Ks> DI void pin_h(St& s, iseq<Ks...>) { (pin1<eks(E) + Ks>(s), ...); }
template <int E> DI void barrier(St& s) {
  wait_bar<vm_wait(E)>();
  if constexpr (E < 8) {
    pin_h<E>(s, mkseq<eks(E + 1) - eks(E)>{});
    unsigned a = s.wbA + eks(E) * 8192u, b = (s.wbA ^ 32u) + eks(E) * 8192u; asm volatile("" : "+v"(a), "+v"(b)); s.wp = a; s.wp1 = b;
  } else { unsigned a = s.vl16 + ((E - 8) & 3) * SLOT; asm volatile("" : "+v"(a)); s.wp = a; }
  SBAR(); if constexpr (E == 0 || E == 4 || E >= 7) TSTAMP(s);
}
template <int K> DI void stats(St& s) {
  if constexpr (K == 0) { asm volatile("" : "+v"(s.sp0), "+v"(s.sp1), "+v"(s.sq)); s.t0 = (s.sp0[0] + s.sp0[2]) + (s.sp1[0] + s.sp1[2]); s.t1 = (s.sp0[1] + s.sp0[3]) + (s.sp1[1] + s.sp1[3]); }
  else if constexpr (K == 1) s.t0 = (swap32_add(s.t0) + s.sq[0]) * (1.f / 9.f);
  else if constexpr (K == 2) { const float d0 = s.sp0[0] - s.t0, d1 = s.sp0[2] - s.t0; s.t1 = __builtin_fmaf(64.f, d0 * d0 + d1 * d1, s.t1); }
  else if constexpr (K == 3) { const float d0 = s.sp1[0] - s.t0, d1 = s.sp1[2] - s.t0; s.t1 = __builtin_fmaf(64.f, d0 * d0 + d1 * d1, s.t1); }
  else if constexpr (K == 4) { const float d = s.sq[0] - s.t0; s.t1 = swap32_add(s.t1) + __builtin_fmaf(64.f * d, d, s.sq[1]); }
  else if constexpr (K == 5) s.t1 = rbf(rbf(s.t1 * (1.f / DM)) + 1e-5f);
  else if constexpr (K == 6) { s.t1 = rbf(__builtin_amdgcn_sqrtf(s.t1)); s.nmean = -rbf(s.t0); }
  else { s.c1 = -1.4426950408889634f * __builtin_amdgcn_rcpf(s.t1); s.c2 = s.t1 * s.t1; asm volatile("" : "+v"(s.c1), "+v"(s.c2), "+v"(s.nmean)); }
  if constexpr (K < 7) asm volatile("" : "+v"(s.t0), "+v"(s.t1));
}
template <int P, int H> DI void csread(St& s, f32x2& g, f32x2& u) { g = *(const LAS f32x2*)(lp(s.csb) + P * 128 + (H >> 1) * 32 + (H & 1) * 8); u = *(const LAS f32x2*)(lp(s.csb) + P * 128 + 64 + (H >> 1) * 32 + (H & 1) * 8); }
template <int P, int ST> DI void ep(St& s) {
  if constexpr (ST == 0) csread<P, 0>(s, s.csg, s.csu);
  constexpr int k = (ST + 2) % 3, v = (ST - 1) / 3;
  if constexpr (ST >= 1 && ST < 28) {
    if constexpr (k == 0) {
      if constexpr (v > 0) { s.d0 = __builtin_fmaf(s.x1, s.c2, s.c2); asm volatile("" : "+v"(s.d0)); }
      if constexpr (v < 8) {
        const f32x2& cg = (v & 2) ? s.csg2 : s.csg; const f32x2& cu = (v & 2) ? s.csu2 : s.csu;
        s.ag = __builtin_fmaf(s.nmean, cg[v & 1], s.acc[P][v]); s.au = __builtin_fmaf(s.nmean, cu[v & 1], s.acc[P][8 + v]); asm volatile("" : "+v"(s.ag), "+v"(s.au));
        if constexpr ((v & 1) == 0 && v < 6) { if constexpr (v & 2) csread<P, (v >> 1) + 1>(s, s.csg, s.csu); else csread<P, (v >> 1) + 1>(s, s.csg2, s.csu2); }
      }
    } else if constexpr (k == 1) {
      if constexpr (v > 0) { s.d0 = __builtin_amdgcn_rcpf(s.d0); asm volatile("" : "+v"(s.d0)); }
      if constexpr (v < 8) { s.x0 = s.ag * s.c1; s.t0 = s.ag * s.au; asm volatile("" : "+v"(s.x0), "+v"(s.t0)); }
    } else {
      if constexpr (v > 0) { float o = s.t1 * s.d0; asm volatile("" : "+v"(o)); if constexpr (v & 1) s.o0 = o; else s.o1 = o; }
      if constexpr (v < 8) { s.x1 = __builtin_amdgcn_exp2f(s.x0); s.t1 = s.t0; asm volatile("" : "+v"(s.x1)); }
    }
  }
  if constexpr (ST >= 10 && ST <= 28 && (ST - 10) % 6 == 0) { unsigned pkd; asm volatile("v_cvt_pk_bf16_f32 %0, %1, %2" : "=v"(pkd) : "v"(s.o0), "v"(s.o1)); s.pk[(ST - 10) / 6] = pkd; }
  if constexpr (ST == NEP - 1) st128m<ST4>(s.pk, s.rAct, s.vst, s.sAct + P * 512u);
}
template <int G, int P> DI void ep_try(St& s) {
  if constexpr (G >= ep_start(P) - 1 && G < ep_start(P) - 1 + NEP) ep<P, G - ep_start(P) + 1>(s); }
template <int G> DI void slot(St& s) {
  constexpr int en = epoch_of(G) + 1;
  if constexpr (en < NE && G == bslot(en)) { barrier<en>(s); reads<G + 2>(s); reads<G + 3>(s); }
  else if constexpr (!(en < NE && G + LA >= estart(en) && G < bslot(en))) reads<G + LA>(s);
  vops<s_lo(G)>(s, mkseq<s_lo(G + 1) - s_lo(G)>{});
  if constexpr (op_at(G) >= 0) vop<op_at(G)>(s);
  constexpr int p = u_pair(G), ks = u_kstep(G);
  if constexpr (ks == 0) MFMA32_NEW(s.acc[p], s.wf[G], s.hf[ks]); else MFMA32_ACC(s.acc[p], s.wf[G], s.hf[ks]);
  if constexpr (G >= ST0 && G < ST0 + 8) stats<G - ST0>(s);
  ep_try<G, 0>(s); ep_try<G, 1>(s); ep_try<G, 2>(s); ep_try<G, 3>(s); ep_try<G, 4>(s); ep_try<G, 5>(s); ep_try<G, 6>(s); ep_try<G, 7>(s);
  SBAR();
}
template <int... Gs> DI void run(St& s, iseq<Gs...>) { (slot<Gs>(s), ...); }
template <int... Ss> DI void ep_all(St& s, iseq<Ss...>) { (ep<8, Ss>(s), ...); }
}

__global__ __launch_bounds__(512, 2) void k4_up(const u16* __restrict__ wg, const u16* __restrict__ wu, char* __restrict__ ws) {
  using namespace k4;
  const unsigned tid = threadIdx.x, lane = tid & 63, w = rfl(tid >> 6), r = lane & 31, hi = lane >> 5, blk = (lane >> 4) & 1, q2 = (lane >> 2) & 3, p2 = lane & 3;
  const unsigned b = blockIdx.x, xcd = b & 7, ix = b >> 3, rb = ix >> 2, cb = xcd * 4 + (ix & 3);
  St s;
  TSTAMP(s);
  s.w = w; s.rW = mksrd(ws + WS_WGU, DM * FF * 4); s.rAct = mksrd(ws + WS_ACT, SEQ * FF * 2);
  s.rH = mksrd(ws + WS_H, SEQ * DM * 2); s.rS = mksrd(ws + WS_ST, SEQ * 72); s.rCS = mksrd(ws + WS_CS, FF * 8);
  const unsigned row0 = rb * 256u + w * 32u;
  s.p0 = cb * 9u; s.sW = cb * 9u * 36864u; s.rR = mksrd(w < 4 ? wg : wu, DM * FF * 2);
  s.vA = (4u * hi + q2) * (FF * 2u) + blk * 64u + ((p2 ^ ((w >> 2) << 1)) << 4); s.sA = cb * 288u + (w & 3) * 8u * (FF * 2u);
  s.wbA = blk * 4096u + hi * 1024u + q2 * 64u + blk * 32u + p2 * 8u;
  s.vl16 = lane * 16u; s.sHh = (row0 >> 5) * 36864u;
  s.vst = (r >> 4) * 147456u + hi * 256u + (r & 15) * 16u;
  s.sAct = (rb * 16u + w * 2u) * 147456u + cb * 9u * 512u;
  s.csb = (row0 + r) * 72u; s.vsp = s.csb + hi * 32u;
  vops<200>(s, mkseq<3>{});
  vops<0>(s, mkseq<6>{});
  __builtin_amdgcn_s_barrier();
  vops<6>(s, mkseq<NPRO - 6>{});
  barrier<0>(s);
  reads<0>(s); reads<1>(s); reads<2>(s); SBAR();
  run(s, mkseq<NU>{});
  TSTAMP(s);
  asm volatile("s_nop 7\n\ts_nop 7" ::: "memory"); SBAR();
  ep_all(s, mkseq<NEP>{});
}

extern "C" void kernel_launch(void* const* d_in, const int* in_sizes, int n_in, void* d_out, int out_size, void* d_ws, size_t ws_size, hipStream_t stream) {
  const u16* x = (const u16*)d_in[0]; const u16* ckv = (const u16*)d_in[1]; const u16* wq = (const u16*)d_in[2]; const u16* wuk = (const u16*)d_in[3];
  const u16* wuv = (const u16*)d_in[4]; const u16* wo = (const u16*)d_in[5]; const u16* wg = (const u16*)d_in[7]; const u16* wu = (const u16*)d_in[8]; const u16* wd = (const u16*)d_in[9];
  char* ws = (char*)d_ws;
  (void)hipFuncSetAttribute((const void*)k1_kvproj, hipFuncAttributeMaxDynamicSharedMemorySize, k1::LDS_BYTES);
  k1_kvproj<<<256, 512, k1::LDS_BYTES, stream>>>(ckv, wuk, wuv, x, wq, ws);
  (void)hipFuncSetAttribute((const void*)k2_attn, hipFuncAttributeMaxDynamicSharedMemorySize, k2::LDS_BYTES);
  k2_attn<<<256, 512, k2::LDS_BYTES, stream>>>(wq, wo, wg, wu, wd, ws);
  (void)hipFuncSetAttribute((const void*)k3_oproj, hipFuncAttributeMaxDynamicSharedMemorySize, k3::LDS_BYTES);
  k3_oproj<<<k3::GRID, 512, k3::LDS_BYTES, stream>>>(wo, x, wg, wu, ws);
  (void)hipFuncSetAttribute((const void*)k4_up, hipFuncAttributeMaxDynamicSharedMemorySize, k4::LDS_BYTES);
  k4_up<<<256, 512, k4::LDS_BYTES, stream>>>(wg, wu, ws);
  (void)hipFuncSetAttribute((const void*)k5_down, hipFuncAttributeMaxDynamicSharedMemorySize, k3::LDS_BYTES);
  k5_down<<<k3::GRID, 512, k3::LDS_BYTES, stream>>>(wd, ws, (u16*)d_out);
}
```

```cpp
#include <hip/hip_runtime.h>
#include <utility>
typedef unsigned short u16;
typedef short bf16x8 __attribute__((ext_vector_type(8)));
typedef short s16x4 __attribute__((ext_vector_type(4)));
typedef float f32x16 __attribute__((ext_vector_type(16)));
typedef float f32x4 __attribute__((ext_vector_type(4)));
typedef float f32x2 __attribute__((ext_vector_type(2)));
typedef unsigned u32x4 __attribute__((ext_vector_type(4)));
typedef unsigned u32x2 __attribute__((ext_vector_type(2)));
typedef __bf16 bf16x2_t __attribute__((ext_vector_type(2)));
#define LAS __attribute__((address_space(3)))
#define DI __device__ __forceinline__
#define SBAR() __builtin_amdgcn_sched_barrier(0)
#define MFMA32(a, b, c) __builtin_amdgcn_mfma_f32_32x32x16_bf16((a), (b), (c), 0, 0, 0)
#define MFMA16(a, b, c) __builtin_amdgcn_mfma_f32_16x16x32_bf16((a), (b), (c), 0, 0, 0)
#define MFMA16_ACC(c, a, b) asm volatile("v_mfma_f32_16x16x32_bf16 %0, %1, %2, %0" : "+v"(c) : "v"(a), "v"(b))
#define MFMA16_NEW(c, a, b) asm volatile("v_mfma_f32_16x16x32_bf16 %0, %1, %2, 0" : "=v"(c) : "v"(a), "v"(b))
#define MFMA32_ACC(c, a, b) asm volatile("v_mfma_f32_32x32x16_bf16 %0, %1, %2, %0" : "+v"(c) : "v"(a), "v"(b))
#define MFMA32_NEW(c, a, b) asm volatile("v_mfma_f32_32x32x16_bf16 %0, %1, %2, 0" : "=v"(c) : "v"(a), "v"(b))
template <int... Is> using iseq = std::integer_sequence<int, Is...>;
template <int N> using mkseq = std::make_integer_sequence<int, N>;
typedef __amdgpu_buffer_rsrc_t srd_t;

DI unsigned rfl(unsigned x) { return __builtin_amdgcn_readfirstlane(x); }
DI float bf2f(unsigned v) { return __uint_as_float(v << 16); }
DI float bflo(unsigned w) { return __uint_as_float(w << 16); }
DI float bfhi(unsigned w) { return __uint_as_float(w & 0xffff0000u); }
DI unsigned cvtpk(float lo, float hi) { f32x2 v = {lo, hi}; bf16x2_t b = __builtin_convertvector(v, bf16x2_t); return __builtin_bit_cast(unsigned, b); }
DI float rbf(float f) { return bflo(cvtpk(f, f)); }
DI srd_t mksrd(const void* p, unsigned bytes) { return __builtin_amdgcn_make_buffer_rsrc((void*)p, (short)0, (int)bytes, 0x00020000); }
DI void dma16(unsigned m0v, unsigned voff, srd_t srd, unsigned soff) {
  asm volatile("s_mov_b32 m0, %0\n\ts_nop 0\n\tbuffer_load_dwordx4 %1, %2, %3 offen lds" ::"s"(m0v), "v"(voff), "s"(srd), "s"(soff) : "memory");
}
template <int N> DI void wait_vm() { asm volatile("s_waitcnt vmcnt(%0)" ::"i"(N) : "memory"); }
template <int N> DI void wait_bar() { asm volatile("s_waitcnt vmcnt(%0) lgkmcnt(0)\n\ts_barrier" ::"i"(N) : "memory"); }
DI bf16x8 lds128(const LAS char* p) { return *(const LAS bf16x8*)p; }
typedef short v4i16_t __attribute__((ext_vector_type(4)));
DI s16x4 ldstr(const LAS char* p) { return __builtin_bit_cast(s16x4, __builtin_amdgcn_ds_read_tr16_b64_v4i16((LAS v4i16_t*)p)); }
DI bf16x8 cat8(s16x4 a, s16x4 b) { return __builtin_shufflevector(a, b, 0, 1, 2, 3, 4, 5, 6, 7); }
DI u32x4 pack8(const f32x16& x, int s) {
  u32x4 w = {cvtpk(x[8 * s], x[8 * s + 1]), cvtpk(x[8 * s + 2], x[8 * s + 3]), cvtpk(x[8 * s + 4], x[8 * s + 5]), cvtpk(x[8 * s + 6], x[8 * s + 7])};
  return w;
}
template <int MODE> DI void st128m(u32x4 v, srd_t srd, unsigned voff, unsigned soff) {
  if constexpr (MODE == 0) asm volatile("buffer_store_dwordx4 %0, %1, %2, %3 offen\n\ts_nop 1" ::"v"(v), "v"(voff), "s"(srd), "s"(soff) : "memory");
  else if constexpr (MODE == 1) asm volatile("buffer_store_dwordx4 %0, %1, %2, %3 offen sc1\n\ts_nop 1" ::"v"(v), "v"(voff), "s"(srd), "s"(soff) : "memory");
  else if constexpr (MODE == 2) asm volatile("buffer_store_dwordx4 %0, %1, %2, %3 offen nt\n\ts_nop 1" ::"v"(v), "v"(voff), "s"(srd), "s"(soff) : "memory");
  else asm volatile("buffer_store_dwordx4 %0, %1, %2, %3 offen sc0 sc1\n\ts_nop 1" ::"v"(v), "v"(voff), "s"(srd), "s"(soff) : "memory");
}
#define ST1 1
#define ST2 1
#define ST3 1
#define ST4 1
#define ST5 1
template <int CTRL> DI float dpp_add(float x) { return x + __builtin_bit_cast(float, __builtin_amdgcn_update_dpp(0, __builtin_bit_cast(int, x), CTRL, 0xf, 0xf, true)); }
DI float swap32_add(float x) { auto r = __builtin_amdgcn_permlane32_swap(__float_as_uint(x), __float_as_uint(x), false, false); return __uint_as_float(r[0]) + __uint_as_float(r[1]); }
DI float swap16_add(float x) { auto r = __builtin_amdgcn_permlane16_swap(__float_as_uint(x), __float_as_uint(x), false, false); return __uint_as_float(r[0]) + __uint_as_float(r[1]); }
DI float swap32_max(float x) { auto r = __builtin_amdgcn_permlane32_swap(__float_as_uint(x), __float_as_uint(x), false, false); return __builtin_fmaxf(__uint_as_float(r[0]), __uint_as_float(r[1])); }
DI float wave_sum(float x) {
  x = dpp_add<0xB1>(x); x = dpp_add<0x4E>(x); x = dpp_add<0x141>(x); x = dpp_add<0x140>(x);
  x = swap16_add(x); return swap32_add(x);
}

constexpr int SEQ = 2048, DM = 576, DC = 512, NH = 32, HD = 128, FF = 4608, HDM = NH * HD;
constexpr size_t WS_XN = 0;
constexpr size_t WS_KF = WS_XN + (size_t)SEQ * DM * 2;
constexpr size_t WS_VF = WS_KF + (size_t)SEQ * HDM * 2;
constexpr size_t WS_CTX = WS_VF + (size_t)SEQ * HDM * 2;
constexpr size_t WS_QS = WS_CTX + (size_t)SEQ * HDM * 2;
constexpr size_t WS_WO = WS_QS + (size_t)256 * 32768;
constexpr size_t WS_WGU = WS_WO + (size_t)HDM * DM * 2;
constexpr size_t WS_WD = WS_WGU + (size_t)DM * FF * 4;
constexpr size_t WS_H = WS_WD + (size_t)FF * DM * 2;
constexpr size_t WS_ST = WS_H + (size_t)SEQ * DM * 2;
constexpr size_t WS_CS = WS_ST + (size_t)SEQ * 128;
constexpr size_t WS_ACT = WS_CS + (size_t)288 * 128;
constexpr size_t WS_END = WS_ACT + (size_t)SEQ * FF * 2;

#define TSTAMP(s) do {} while (0)

namespace k1 {
constexpr int SLOT = 53248, XSCR = 32768  , DUMP = 3 * SLOT  , LDS_BYTES = DUMP + 2048, LA = 3  , BB = 2  ;
constexpr int NC = 11, P1 = 160;
constexpr int cstart(int c) { return c <= 8 ? 20 * c : P1 + 32 * (c - 8); }
constexpr int chunk_of(int g) { return g < P1 ? g / 20 : 8 + ((g - P1) >> 5); }
constexpr int bslot(int c) { return cstart(c) - BB; }
constexpr int dcnt(int c) { return c < 8 ? 7 : (c < NC ? 4 : 0); }
constexpr int kt_of(int g) { const int c = chunk_of(g), i = g - cstart(c); return c < 7 ? (i % 10) : c == 7 ? (i >> 1) : (i & 1); }
constexpr int sloc_of(int g) { const int c = chunk_of(g), i = g - cstart(c); return c < 7 ? (i / 10) : c == 7 ? (i & 1) : (i >> 1); }
constexpr int kstep_of(int g) { const int c = chunk_of(g); return c < 8 ? 2 * c + sloc_of(g) : sloc_of(g); }
constexpr bool w_first(int g) { const int c = chunk_of(g), i = g - cstart(c); return c < 7 ? (i % 10) == 0 : (c == 7 && i < 2); }
constexpr int acc_of(int g) { const int c = chunk_of(g); return c < 8 ? kt_of(g) : 10 + 2 * (c - 8) + kt_of(g); }
constexpr int dma_at(int g) { for (int c = 0; c < NC - 2; ++c) { const int j = g - (c ? bslot(c) : 0); if (j >= 0 && j < 2 * dcnt(c + 2) && !(j & 1)) return (c + 2) * 8 + (j >> 1); } return -1; }
constexpr bool sure(int d) { return d >= 0 && (d & 7) != 6; }
constexpr int pk_at(int g) {
  for (int u = 0; u < 5; ++u) { if (g == 140 + 4 * u + 6) return 2 * u; if (g == 140 + 4 * u + 7) return 2 * u + 1; }
  for (int u = 5; u < 7; ++u) { if (g == cstart(u + 4) + 3) return 2 * u; if (g == cstart(u + 4) + 4) return 2 * u + 1; }
  return -1;
}
constexpr int st_at(int g) {
  if (g < 155 || (g - 155) % 7 != 0) return -1;
  constexpr int order[15] = {0, 1, 2, 3, 4, 5, 6, 10, 7, 11, 8, 9, 12, 13, -1};
  return order[(g - 155) / 7];
}
constexpr int XWARM = 15, XLD = 161, LN0 = 172, NLN = 48, LNRD = 226, LNST = 230, WARM = 236;
constexpr int ln_at(int g) { return (g >= LN0 && g < LN0 + NLN) ? g - LN0 : -1; }
constexpr int late_ops(int g) { return (st_at(g) >= 0 ? 1 : 0) + (g == LNST ? 2 : 0) + (g == WARM || g == XWARM ? 1 : 0) + (g == XLD ? 2 : 0); }
constexpr int ops_between(int from, int to) { int n = 0; for (int g = from + 1; g < to; ++g) n += (sure(dma_at(g)) ? 1 : 0) + late_ops(g); return n; }
constexpr int vm_wait(int c) {
  if (c == 0) return 6;
  if (c == 1) return ops_between(-1, bslot(1));
  const int from = (c == 2 ? 0 : bslot(c - 2)) + 2 * (dcnt(c) - 1);
  return late_ops(from) + ops_between(from, bslot(c));
}
DI unsigned olane() { unsigned l; asm volatile("v_mbcnt_lo_u32_b32 %0, -1, 0\n\tv_mbcnt_hi_u32_b32 %0, -1, %0" : "=v"(l)); return l; }
struct St {
  bf16x8 wf[16][2]; bf16x8 af[256]; f32x4 acc[16][2]; u32x4 pk[16];
  unsigned wp, ap[4];
  unsigned wb, ab0, ab1, voffA0, voffW, m0w, sA, sW, vst, sO;
  srd_t rA, rK, rV, rO, rXN, rX; unsigned sX;
  u32x4 x0, x1; float v[16], t0, t1, t2; unsigned lane, sXN; bool two;
  srd_t rWm; unsigned sWm;
};
template <int C, int J> DI void dma(St& s) {
  constexpr unsigned slot = (C % 3) * SLOT;
  if constexpr (C < 8) {
    if constexpr (J < 4) {
      constexpr unsigned mat = J >> 1, jj = J & 1;
      dma16(s.m0w * 2048u + slot + mat * 16384u + jj * 1024u, s.voffW, mat ? s.rV : s.rK, s.sW + s.m0w * 65536u + jj * 32768u + C * 524288u);
    } else if constexpr (J < 6) dma16(s.m0w * 2048u + slot + 32768u + (J - 4) * 1024u, s.voffA0, s.rA, s.sA + s.m0w * 16384u + (J - 4) * 8192u + C * 128u);
    else { if (s.m0w < 4u) dma16(s.m0w * 1024u + slot + 49152u, s.voffA0 ^ (((s.m0w ^ (s.m0w >> 1)) & 1u) << 6), s.rA, s.sA + 131072u + s.m0w * 8192u + C * 128u); }
  } else dma16(s.m0w * 4096u + slot + J * 1024u, (olane() ^ (8u * (s.m0w >> 2) + ((4u * s.m0w + J) & 7u))) << 4, s.rA, s.sA + (160u + (C - 8) * 32u + J) * 1024u + s.m0w * 4096u);
}
DI const LAS char* lp(unsigned a) { return (const LAS char*)(size_t)a; }
template <int G> DI void reads(St& s) {
  if constexpr (G < 256) {
    constexpr int c = chunk_of(G), kt = kt_of(G), sl = sloc_of(G), ks = kstep_of(G);
    if constexpr (w_first(G)) {
      s.wf[ks][0] = cat8(ldstr(lp(s.wp) + sl * 8192), ldstr(lp(s.wp) + sl * 8192 + 1024));
      s.wf[ks][1] = cat8(ldstr(lp(s.wp ^ 16u) + sl * 8192), ldstr(lp(s.wp ^ 16u) + sl * 8192 + 1024));
    }
    if constexpr (c < 8) s.af[G] = lds128(lp(s.ap[sl]) + (kt >> 1) * 4096 + (kt & 1) * 1024);
    else s.af[G] = lds128(lp(s.ap[sl & 3]) + (sl >> 2) * 256 + kt * 8192);
  }
}
template <int C> DI void set_bases(St& s) {
  constexpr unsigned slot = (C % 3) * SLOT;
  if constexpr (C < 8) {
    unsigned w = s.wb + slot, a0 = s.ab0 + slot, a1 = (s.ab0 ^ 64u) + slot;
    asm volatile("" : "+v"(w), "+v"(a0), "+v"(a1));
    s.wp = w; s.ap[0] = a0; s.ap[1] = a1;
  } else {
    if constexpr (C == 8) { const unsigned l = olane(), r = l & 15; s.ab1 = (16u * (r >> 3) + (r & 7)) * 1024u + (((l >> 4) ^ r) << 4); s.lane = l; s.two = l < 8u; }
    unsigned a0 = s.ab1 + slot, a1 = (s.ab1 ^ 64u) + slot, a2 = (s.ab1 ^ 128u) + slot, a3 = (s.ab1 ^ 192u) + slot;
    asm volatile("" : "+v"(a0), "+v"(a1), "+v"(a2), "+v"(a3));
    s.ap[0] = a0; s.ap[1] = a1; s.ap[2] = a2; s.ap[3] = a3;
  }
}
template <bool ISV, int ID> DI void pack(St& s) {
  constexpr int u = ID >> 1, j = ID & 1;
  const f32x4 a = ISV ? s.acc[2 * u][j] : s.acc[2 * u + j][0], b = ISV ? s.acc[2 * u + 1][j] : s.acc[2 * u + j][1];
  s.pk[ID] = u32x4{cvtpk(a[0], a[1]), cvtpk(a[2], a[3]), cvtpk(b[0], b[1]), cvtpk(b[2], b[3])};
  asm volatile("" : "+v"(s.pk[ID]));
}
template <int ID> DI void store(St& s) { st128m<ST1>(s.pk[ID], s.rO, s.vst, s.sO + (ID >> 1) * 8192u + (ID & 1) * 128u); }
template <int ST> DI void ln_stage(St& s) {
  if constexpr (ST < 4) {
    if constexpr (ST == 0) { asm volatile("s_waitcnt vmcnt(%2)" : "+v"(s.x0), "+v"(s.x1) : "i"(ops_between(XLD, LN0 + 1)) : "memory"); s.t0 = 0.f; s.t2 = 0.f; }
    s.v[2 * ST] = bflo(s.x0[ST]); s.v[2 * ST + 1] = bfhi(s.x0[ST]); s.t0 += s.v[2 * ST] + s.v[2 * ST + 1];
  } else if constexpr (ST < 8) { constexpr int i = ST - 4; s.v[8 + 2 * i] = bflo(s.x1[i]); s.v[9 + 2 * i] = bfhi(s.x1[i]); s.t2 += s.v[8 + 2 * i] + s.v[9 + 2 * i]; }
  else if constexpr (ST == 8) s.t0 = dpp_add<0xB1>(s.t0 + (s.two ? s.t2 : 0.f));
  else if constexpr (ST == 9) s.t0 = dpp_add<0x141>(dpp_add<0x4E>(s.t0));
  else if constexpr (ST == 10) s.t0 = dpp_add<0x140>(s.t0);
  else if constexpr (ST == 11) s.t0 = swap16_add(s.t0);
  else if constexpr (ST == 12) { s.t0 = swap32_add(s.t0) * (1.f / DM); s.t1 = 0.f; s.t2 = 0.f; }
  else if constexpr (ST < 21) { constexpr int i = 2 * (ST - 13); float& q = i < 8 ? s.t1 : s.t2; const float d = s.v[i] - s.t0, e = s.v[i + 1] - s.t0; q += d * d; q += e * e; }
  else if constexpr (ST == 21) s.t1 = dpp_add<0xB1>(s.t1 + (s.two ? s.t2 : 0.f));
  else if constexpr (ST == 22) s.t1 = dpp_add<0x141>(dpp_add<0x4E>(s.t1));
  else if constexpr (ST == 23) s.t1 = dpp_add<0x140>(s.t1);
  else if constexpr (ST == 24) s.t1 = swap16_add(s.t1);
  else if constexpr (ST == 25) s.t1 = swap32_add(s.t1) * (1.f / DM);
  else if constexpr (ST == 26) { s.t0 = rbf(s.t0); s.t1 = rbf(s.t1); }
  else if constexpr (ST == 27) s.t1 = rbf(s.t1 + 1e-5f);
  else if constexpr (ST == 28) s.t1 = rbf(__builtin_amdgcn_sqrtf(s.t1));
  else if constexpr (ST == 29) s.t1 = __builtin_amdgcn_rcpf(s.t1);
  else if constexpr (ST < 46) {
    constexpr int i = (ST - 30) >> 1; u32x4& o = i < 4 ? s.x0 : s.x1;
    if constexpr (((ST - 30) & 1) == 0) o[i & 3] = cvtpk(s.v[2 * i] - s.t0, s.v[2 * i + 1] - s.t0);
    else o[i & 3] = cvtpk(bflo(o[i & 3]) * s.t1, bfhi(o[i & 3]) * s.t1);
    asm volatile("" : "+v"(o));
  } else if constexpr (ST == 46) *(LAS u32x4*)(size_t)(XSCR + s.lane * 128u + ((s.m0w ^ (s.lane & 7)) << 4)) = s.x0;
  else { if (s.two) *(LAS u32x4*)(size_t)(XSCR + 8192u + s.lane * 128u + ((s.m0w ^ (s.lane & 7)) << 4)) = s.x1; }
  if constexpr (ST < 30) asm volatile("" : "+v"(s.t0), "+v"(s.t1), "+v"(s.t2));
}
template <bool ISV, int G> DI void slot(St& s) {
  constexpr int cn = chunk_of(G) + 1;
  constexpr bool isbar = cn < NC && G == bslot(cn);
  if constexpr (isbar) {
    wait_bar<vm_wait(cn)>(); set_bases<cn>(s); SBAR();
    reads<G + BB>(s); reads<G + BB + 1>(s);
  } else if constexpr (!(cn < NC && G + LA >= cstart(cn) && G < bslot(cn))) reads<G + LA>(s);
  if constexpr (dma_at(G) >= 0) dma<(dma_at(G) >> 3), (dma_at(G) & 7)>(s);
  constexpr int ai = acc_of(G), ks = kstep_of(G);
#pragma unroll
  for (int ct = 0; ct < 2; ++ct) {
    const bf16x8 a = ISV ? s.af[G] : s.wf[ks][ct], bq = ISV ? s.wf[ks][ct] : s.af[G];
    if constexpr (ks == 0) MFMA16_NEW(s.acc[ai][ct], a, bq); else MFMA16_ACC(s.acc[ai][ct], a, bq);
  }
  if constexpr (pk_at(G) >= 0) pack<ISV, pk_at(G)>(s);
  if constexpr (st_at(G) >= 0) store<st_at(G)>(s);
  if constexpr (G == XLD) asm volatile("buffer_load_dwordx4 %0, %2, %4, %5 offen\n\tbuffer_load_dwordx4 %1, %3, %4, %5 offen offset:1024"
                                       : "=&v"(s.x0), "=&v"(s.x1) : "v"(s.lane * 16u), "v"((s.lane & 7) * 16u), "s"(s.rX), "s"(s.sX) : "memory");
  if constexpr (ln_at(G) >= 0) ln_stage<ln_at(G)>(s);
  if constexpr (G == XWARM) { const unsigned l = olane();
    asm volatile("s_mov_b32 m0, %0\n\ts_nop 0\n\tbuffer_load_dword %1, %2, %3 offen lds" ::"s"(DUMP + s.m0w * 256u), "v"(l < 9u ? l * 128u : 0x10000000u), "s"(s.rX), "s"(s.sX) : "memory"); }
  if constexpr (G == LNRD) {
    s.x0 = *(const LAS u32x4*)(size_t)(XSCR + s.m0w * 1024u + s.lane * 16u); s.x1 = *(const LAS u32x4*)(size_t)(XSCR + 8192u + s.lane * 16u);
  }
  if constexpr (G == LNST) {
    const unsigned vo = (s.lane >> 3) * 512u + (((s.lane ^ (s.lane >> 3)) & 7) << 4);
    st128m<ST1>(s.x0, s.rXN, vo, s.sXN + s.m0w * 4096u); st128m<ST1>(s.x1, s.rXN, vo, s.m0w == 0 ? s.sXN + 32768u : 0x10000000u);
  }
  if constexpr (G == WARM)
    asm volatile("s_mov_b32 m0, %0\n\ts_nop 0\n\tbuffer_load_dword %1, %2, %3 offen lds" ::"s"(DUMP + s.m0w * 256u), "v"(s.lane < 18u ? (s.lane >> 1) * 8192u + (s.lane & 1) * 128u : 0x10000000u), "s"(s.rWm), "s"(s.sWm) : "memory");
  SBAR();
}
template <bool ISV, int... Gs> DI void run(St& s, iseq<Gs...>) {
  reads<0>(s); reads<1>(s); reads<2>(s); SBAR();
  (slot<ISV, Gs>(s), ...);
  asm volatile("s_nop 7\n\ts_nop 3" ::: "memory"); SBAR();
  pack<ISV, 14>(s); store<14>(s); pack<ISV, 15>(s); store<15>(s);
}
template <int C, int... Js> DI void dma_all(St& s, iseq<Js...>) { (dma<C, Js>(s), ...); }
}

__global__ __launch_bounds__(512, 2) void k1_kvproj(const u16* __restrict__ ckv, const u16* __restrict__ wuk, const u16* __restrict__ wuv, const u16* __restrict__ x,
    const u16* __restrict__ wq, char* __restrict__ ws) {
  using namespace k1;
  const unsigned tid = threadIdx.x, lane = tid & 63, w = rfl(tid >> 6), r = lane & 15, g = lane >> 4;
  const unsigned b = blockIdx.x, xcd = b & 7, ix = b >> 3, h = xcd * 4 + (ix & 3), rb = ix >> 2;
  St s;
  s.rA = mksrd(ckv, SEQ * DC * 2); s.rK = mksrd(wuk, DC * HDM * 2); s.rV = mksrd(wuv, DC * HDM * 2);
  s.rO = mksrd(ws + (w < 4 ? WS_KF : WS_VF), SEQ * HDM * 2); s.rXN = mksrd(ws + WS_XN, SEQ * DM * 2);
  s.m0w = w; s.sXN = (b >> 2) * 36864u + (b & 3) * 128u;
  const unsigned q2 = (lane >> 2) & 3, p2 = lane & 3, rrow = 16u * (r >> 3) + (r & 7);
  s.voffA0 = (lane >> 3) * 1024u + (((lane & 7) ^ (4u * (w & 1) + g)) << 4);
  s.voffW = q2 * 8192u + g * 64u + ((p2 ^ (w & 1u)) << 4);
  s.sA = rb * 256u * 1024u; s.sW = h * 256u;
  s.wb = (w >> 2) * 16384u + (w & 3) * 256u + g * 2048u + q2 * 64u + (p2 >> 1) * 32u + (g & 1) * 16u + (p2 & 1) * 8u;
  s.ab0 = 32768u + rrow * 128u + ((g ^ (r >> 1)) << 4);
  s.vst = (g >> 1) * (w < 4 ? 1024u : 4096u) + (rrow + 32u * (g & 1)) * 16u;
  s.sO = (h * 64u + rb * 8u) * 8192u + (w < 4 ? w * 2048u : (w - 4) * 1024u);
  s.rWm = mksrd(wq, DM * HDM * 2); s.sWm = (rb * 72u + w * 9u) * 8192u + h * 256u;
  s.rX = mksrd(x, SEQ * DM * 2); s.sX = (b * 8u + w) * (DM * 2u);
  dma_all<0>(s, mkseq<7>{});
  __builtin_amdgcn_s_barrier();
  dma_all<1>(s, mkseq<7>{});
  wait_bar<vm_wait(0)>(); set_bases<0>(s); SBAR();
  if (w < 4) run<false>(s, mkseq<256>{}); else run<true>(s, mkseq<256>{});
}

namespace k2 {
constexpr unsigned KR = 0, VR = 65536, SP = 131072, QR = VR  , LDS_BYTES = 163840;
constexpr float THR = 8.f;
constexpr float QSCALE = 0.08838834764831845f * 1.4426950408889634f;
DI const LAS char* lp(unsigned a) { return (const LAS char*)(size_t)a; }
DI float max3f(float a, float b, float c) { float r; asm("v_max3_f32 %0, %1, %2, %3" : "=v"(r) : "v"(a), "v"(b), "v"(c)); return r; }
DI unsigned opaque_lane() { unsigned l; asm volatile("v_mbcnt_lo_u32_b32 %0, -1, 0\n\tv_mbcnt_hi_u32_b32 %0, -1, %0" : "=v"(l)); return l; }

constexpr int QLA = 3, QBB = 2;
constexpr int qb(int c) { return 16 * c - QBB; }
constexpr int qfirst(int c) { return c == 2 ? 0 : c == 3 ? 6 : qb(c - 3); }
constexpr int qop_chunk(int g) { for (int c = 2; c < 9; ++c) { const int j = g - qfirst(c); if (j >= 0 && j < 6) return c * 8 + j; } return -1; }
constexpr int qkv_at(int g) { return (g >= 96 && g < 112 && ((g - 96) & 3) == 0) ? (g - 96) >> 2 : -1; }
constexpr int qops(int g) { return (qop_chunk(g) >= 0) + (qkv_at(g) >= 0); }
constexpr int qwait(int c) {
  if (c == 0) return 6;
  int n = 0; const int from = c == 1 ? 0 : qfirst(c) + 6;
  for (int g = from; g < qb(c); ++g) n += qops(g);
  return n;
}
struct St {
  bf16x8 xf[36], wf[144];
  bf16x8 qf[8], fr[35]; f32x16 o[4], sc[2][2], negm, oo[2]; u32x4 pf[4], out[4]; f32x4 p[8]; f32x2 st; f32x4 la; bf16x8 sel;
  float m, mo, l, ls0, ls1, mx0, mx1, f, ca, cb; bool pend;
  unsigned w, vl16, wq_rd, vW, sX, sW, qw, kb, va, vb, sKV, n0, sC0;
  srd_t rW, rX, rK, rV, rC, rWm; unsigned vWm, sWm, dummy;
  unsigned tsv, nts;
};
struct Dm { unsigned m0k, m0v, kso, vso; };
DI unsigned tile_off(const St& s, unsigned c) { return s.sKV + (c < s.n0 ? c : c - s.n0) * 32768u; }
DI Dm mkdm(const St& s, int c) {
  const unsigned par = (unsigned)(c & 1) << 15;
  return Dm{KR + par + s.w * 4096u, VR + (par ^ 32768u) + s.w * 4096u, tile_off(s, c + 2), tile_off(s, c + 1)};
}
template <int J> DI void kvdma(const St& s, const Dm& d) {
  asm volatile("s_mov_b32 m0, %0\n\ts_nop 0\n\tbuffer_load_dwordx4 %1, %2, %3 offen offset:%4 lds" ::"s"(J < 4 ? d.m0k : d.m0v), "v"(s.vl16), "s"(J < 4 ? s.rK : s.rV), "s"(J < 4 ? d.kso : d.vso), "i"((J & 3) * 1024) : "memory");
}
template <int C, int J> DI void qload(St& s) {
  if constexpr (J < 2) dma16(QR + (C & 3) * 16384u + s.w * 2048u + J * 1024u, s.vW, s.rW, s.sW + (C * 64u + J * 4u) * 8192u);
  else asm volatile("buffer_load_dwordx4 %0, %1, %2, %3 offen offset:%4" : "=v"(s.xf[4 * C + J - 2]) : "v"(s.vl16), "s"(s.rX), "s"(s.sX + C * 4096u), "i"((J - 2) * 1024) : "memory");
}
template <int C> DI void qbar(St& s) {
  asm volatile("s_waitcnt vmcnt(%4) lgkmcnt(0)\n\ts_barrier" : "+v"(s.xf[4 * C]), "+v"(s.xf[4 * C + 1]), "+v"(s.xf[4 * C + 2]), "+v"(s.xf[4 * C + 3]) : "i"(qwait(C)) : "memory");
}
constexpr int q_t(int g) { return g < 128 ? (g & 3) : ((g - 128) >> 2); }
constexpr int q_s(int g) { return g < 128 ? ((g >> 2) & 3) : (g & 3); }
template <int G> DI void qread(St& s) {
  if constexpr (G < 144) { constexpr int off = ((G >> 4) & 3) * 16384 + q_s(G) * 4096 + q_t(G) * 256; s.wf[G] = cat8(ldstr(lp(s.wq_rd) + off), ldstr(lp(s.wq_rd) + off + 1024)); }
}
template <int F> DI void qpack(St& s) {
  constexpr int T = F >> 1, o8 = (F & 1) * 8;
  const u32x4 v = {cvtpk(s.o[T][o8] * QSCALE, s.o[T][o8 + 1] * QSCALE), cvtpk(s.o[T][o8 + 2] * QSCALE, s.o[T][o8 + 3] * QSCALE),
                   cvtpk(s.o[T][o8 + 4] * QSCALE, s.o[T][o8 + 5] * QSCALE), cvtpk(s.o[T][o8 + 6] * QSCALE, s.o[T][o8 + 7] * QSCALE)};
  s.qf[F] = __builtin_bit_cast(bf16x8, v);
  *(LAS bf16x8*)((LAS char*)(size_t)s.qw + F * 1024) = s.qf[F];
}
template <int G> DI void qslot(St& s) {
  constexpr int cn = (G >> 4) + 1;
  if constexpr (cn < 9 && G == qb(cn)) { qbar<cn>(s); SBAR(); if constexpr (cn == 3 || cn == 6) TSTAMP(s); qread<G + 2>(s); qread<G + 3>(s); }
  else if constexpr (!(cn < 9 && G + QLA >= 16 * cn && G < qb(cn))) qread<G + QLA>(s);
  if constexpr (qop_chunk(G) >= 0) qload<(qop_chunk(G) >> 3), (qop_chunk(G) & 7)>(s);
  if constexpr (qkv_at(G) >= 0) kvdma<qkv_at(G)>(s, mkdm(s, -2));
  constexpr int ks = 4 * (G >> 4) + q_s(G), t = q_t(G);
  const f32x16 z = {};
  s.o[t] = MFMA32(s.wf[G], s.xf[ks], ks == 0 ? z : s.o[t]);
  if constexpr (G >= 134 && ((G - 134) & 1) == 0) qpack<((G - 134) >> 1)>(s);
  SBAR();
}
template <int... Gs> DI void qrun(St& s, iseq<Gs...>) { (qslot<Gs>(s), ...); }
template <int C, int... Js> DI void qload_all(St& s, iseq<Js...>) { (qload<C, Js>(s), ...); }

template <int I> DI void frag(St& s) {
  if constexpr (I < 16) s.fr[I] = lds128(lp(s.kb) + (I & 1) * 8192 + (I >> 1) * 1024);
  else if constexpr (I < 32) s.fr[I] = lds128(lp((I & 2) ? s.vb : s.va) + (I - 16) * 1024);
  else s.fr[I] = lds128(lp(s.kb) + ((I - 32) & 1) * 8192 + ((I - 32) >> 1) * 1024);
}
template <int X> DI void mask_diag(St& s) {
  const unsigned ln = opaque_lane();
  const int dq = 32 * ((int)(s.w & 3) - 2 * (int)(s.w >> 2)) + (int)(ln & 31) - 4 * (int)(ln >> 5);
#pragma unroll
  for (int kb = 0; kb < 2; ++kb)
#pragma unroll
    for (int r = 0; r < 16; ++r) { const int c = 32 * kb + (r & 3) + 8 * (r >> 2); s.sc[X][kb][r] = c > dq ? -__builtin_inff() : s.sc[X][kb][r]; }
}
DI void sub_ip(f32x16& v, float d) {
#pragma unroll
  for (int r = 0; r < 16; ++r) { float e = v[r]; asm("v_sub_f32_e32 %0, %0, %1" : "+v"(e) : "v"(d)); v[r] = e; }
}
DI void mul_ip(f32x16& v, float d) {
#pragma unroll
  for (int r = 0; r < 16; ++r) { float e = v[r]; asm("v_mul_f32_e32 %0, %0, %1" : "+v"(e) : "v"(d)); v[r] = e; }
}
template <int X, int E> DI void sexp(St& s) { float e = __builtin_amdgcn_exp2f(s.sc[X][E >> 4][E & 15]); asm volatile("" : "+v"(e)); s.sc[X][E >> 4][E & 15] = e; }
template <int X, int E> DI void sadd(St& s) {
  float& ls = X ? s.ls1 : s.ls0; if constexpr (E == 0) ls = s.sc[X][0][0]; else { ls += s.sc[X][E >> 4][E & 15]; asm volatile("" : "+v"(ls)); } }
template <int X, int P> DI void scvt(St& s) { unsigned e = cvtpk(s.sc[X][P >> 3][(2 * P) & 15], s.sc[X][P >> 3][(2 * P + 1) & 15]); asm volatile("" : "+v"(e)); s.pf[P >> 2][P & 3] = e; }
DI void mg_stats(St& s, unsigned area, float m) {
  s.l = swap32_add(s.l);
  asm volatile("" : "+v"(m)); *(LAS f32x2*)(size_t)(area + s.w * 256u + (opaque_lane() & 31) * 8u) = f32x2{m, s.l}; }
template <int I> DI void mg_write(St& s, unsigned a) { const f32x16& v = s.o[2 + (I >> 2)]; *(LAS f32x4*)((LAS char*)(size_t)a + I * 1024) = f32x4{v[4 * (I & 3)], v[4 * (I & 3) + 1], v[4 * (I & 3) + 2], v[4 * (I & 3) + 3]}; }
DI void mg_read(St& s, unsigned a, unsigned area) {
  s.st = *(const LAS f32x2*)(size_t)(area + (s.w ^ 4u) * 256u + (opaque_lane() & 31) * 8u);
#pragma unroll
  for (int i = 0; i < 8; ++i) s.p[i] = *(const LAS f32x4*)(lp(a) + i * 1024);
}
DI void mg_coef(St& s, float m) {
  const float mm = __builtin_fmaxf(m, s.st[0]), fa = __builtin_amdgcn_exp2f(m - mm), fb = __builtin_amdgcn_exp2f(s.st[0] - mm);
  const float il = __builtin_amdgcn_rcpf(s.l * fa + s.st[1] * fb); s.ca = fa * il; s.cb = fb * il;
}
template <int I> DI void mg_final(St& s) {
  float v[8];
#pragma unroll
  for (int e = 0; e < 8; ++e) v[e] = s.oo[I >> 1][8 * (I & 1) + e] * s.ca + s.p[2 * I + (e >> 2)][e & 3] * s.cb;
  s.out[I] = u32x4{cvtpk(v[0], v[1]), cvtpk(v[2], v[3]), cvtpk(v[4], v[5]), cvtpk(v[6], v[7])};
  asm volatile("" : "+v"(s.out[I]));
}
template <int I> DI void mg_store(St& s, unsigned blk) {
  const unsigned ln = opaque_lane();
  st128m<ST2>(s.out[I], s.rC, ((ln >> 4) & 1) * 131072u + (ln & 15) * 16u + (ln >> 5) * 256u, s.sC0 + blk * 1048576u + I * 512u);
}
template <int X, int KIND, bool DIAG, int G> DI void aslot(St& s, int c) {
  constexpr int Y = X ^ 1; constexpr bool PREV = KIND != 0, FRESH = KIND == 0 || KIND == 2;
  if constexpr (G == 30) {
    wait_bar<KIND == 3 ? 5 : 1>();
    TSTAMP(s);
    s.kb ^= 32768u; s.va ^= 32768u; s.vb ^= 32768u; asm volatile("" : "+v"(s.kb), "+v"(s.va), "+v"(s.vb)); SBAR();
    frag<32>(s); frag<33>(s);
  } else if constexpr (G + 3 < 16) frag<G + 3>(s);
  else if constexpr (G + 3 < 32) { if constexpr (PREV) frag<G + 3>(s); }
  else if constexpr (G == 31) frag<34>(s);
  const f32x16 z = {};
  if constexpr (G < 16) s.sc[X][G & 1] = MFMA32(s.fr[G], s.qf[G >> 1], G < 2 ? (FRESH ? z : s.negm) : s.sc[X][G & 1]);
  else if constexpr (PREV) s.o[G & 3] = MFMA32(s.fr[G], __builtin_bit_cast(bf16x8, s.pf[(G - 16) >> 2]), (KIND == 3 && G < 20) ? z : s.o[G & 3]);
  if constexpr (KIND == 0) {
    if constexpr (G == 1) asm volatile("s_waitcnt lgkmcnt(0)\n\ts_barrier" ::: "memory");
    if constexpr (G >= 1 && G < 5) kvdma<G - 1>(s, mkdm(s, -1));
  } else if constexpr (KIND != 3) {
    if constexpr (G < 12 && (G & 1)) kvdma<(G >> 1) + 2>(s, mkdm(s, c - 1));
  }
  if constexpr (KIND == 3) {
    const Dm d = mkdm(s, c - 1);
    if constexpr (G == 0) { mg_stats(s, SP, s.mo); s.qw = (s.w < 4u ? d.m0k : d.m0v - 32768u) + s.w * 4096u + s.vl16; asm volatile("" : "+v"(s.qw)); }
    if constexpr (G >= 2 && G < 10) mg_write<G - 2>(s, s.qw);
    if constexpr (G == 10) {
      asm volatile("s_waitcnt lgkmcnt(0)\n\ts_barrier" ::: "memory");
      unsigned a = (s.w < 4u ? d.m0v : d.m0k - 16384u) + (s.w & 3) * 4096u + s.vl16; asm volatile("" : "+v"(a));
      mg_read(s, a, SP); s.oo[0] = s.o[0]; s.oo[1] = s.o[1];
    }
    if constexpr (G == 12) mg_coef(s, s.mo);
    if constexpr (G == 13) asm volatile("s_waitcnt lgkmcnt(0)\n\ts_barrier" ::: "memory");
    if constexpr (G >= 13 && G < 21) kvdma<G - 13>(s, d);
    if constexpr (G >= 13 && G < 25 && (G - 13) % 3 == 0) mg_final<(G - 13) / 3>(s);
    if constexpr (G >= 24 && G < 28) mg_store<G - 24>(s, s.n0 - 1u);
  }
  if constexpr (G == 28)
    asm volatile("buffer_load_dword %0, %1, %2, %3 offen" : "+v"(s.dummy) : "v"(s.vWm), "s"(s.rWm), "s"(s.sWm + c * 640u) : "memory");
  if constexpr (PREV) {
    if constexpr (G < 26) sexp<Y, G + 6>(s);
    if constexpr (G < 27) sadd<Y, G + 5>(s);
    if constexpr (G < 27 && !(G & 1)) scvt<Y, (G >> 1) + 2>(s);
    if constexpr (G == 27) { const float ls = Y ? s.ls1 : s.ls0; if constexpr (KIND == 3) s.l = ls; else s.l += ls; asm volatile("" : "+v"(s.l)); }
  }
  if constexpr (KIND == 0 && G == 16) asm volatile("s_nop 7\n\ts_nop 7" : "+v"(s.sc[X][0]), "+v"(s.sc[X][1]));
  if constexpr (DIAG && G == 16) {
    mask_diag<X>(s);
    unsigned qa = SP + (s.w & 3) * 8192u + s.vl16; asm volatile("" : "+v"(qa));
#pragma unroll
    for (int i = 0; i < 8; ++i) s.qf[i] = lds128(lp(qa) + i * 1024);
  }
  if constexpr (G >= 17 && G < 25) {
    constexpr int i = G - 17, kb = i >> 2, r = 4 * (i & 3);
    if constexpr (i == 0) { s.mx0 = max3f(s.sc[X][0][0], s.sc[X][0][1], s.sc[X][0][1]); s.mx1 = max3f(s.sc[X][0][2], s.sc[X][0][3], s.sc[X][0][3]); }
    else { s.mx0 = max3f(s.mx0, s.sc[X][kb][r], s.sc[X][kb][r + 1]); s.mx1 = max3f(s.mx1, s.sc[X][kb][r + 2], s.sc[X][kb][r + 3]); }
    asm volatile("" : "+v"(s.mx0), "+v"(s.mx1));
  }
  if constexpr (G == 25) {
    const float mx = max3f(s.mx0, s.mx1, s.mx1);
    if constexpr (FRESH) {
      const float rm = __builtin_fmaxf(swap32_max(mx), -1e30f);
      if constexpr (KIND == 2) s.mo = s.m; else s.l = 0.f;
      s.m = rm; sub_ip(s.sc[X][0], rm); sub_ip(s.sc[X][1], rm);
#pragma unroll
      for (int r = 0; r < 16; ++r) s.negm[r] = -rm;
    } else if (__builtin_expect(__any(mx > THR), 0)) {
      const float dl = __builtin_fmaxf(swap32_max(mx), 0.f);
      s.m += dl; s.f = __builtin_amdgcn_exp2f(-dl); s.pend = true;
      sub_ip(s.sc[X][0], dl); sub_ip(s.sc[X][1], dl);
#pragma unroll
      for (int r = 0; r < 16; ++r) { float e = s.negm[r]; asm("v_sub_f32_e32 %0, 0, %1" : "+v"(e) : "v"(s.m)); s.negm[r] = e; }
    }
  }
  if constexpr (G >= 26) sexp<X, G - 26>(s);
  if constexpr (G >= 27) sadd<X, G - 27>(s);
  if constexpr (G == 28 || G == 30) scvt<X, ((G - 28) >> 1)>(s);
  if constexpr (KIND != 2 && G >= 30) kvdma<G - 30>(s, mkdm(s, c));
  SBAR();
}
template <int X, int KIND, bool DIAG, int... Gs> DI void astep_(St& s, int c, iseq<Gs...>) {
  if constexpr (KIND == 0) { frag<32>(s); frag<33>(s); frag<34>(s); SBAR(); }
  s.fr[0] = s.fr[32]; s.fr[1] = s.fr[33]; s.fr[2] = s.fr[34];
  (aslot<X, KIND, DIAG, Gs>(s, c), ...);
  if constexpr (KIND == 1 || KIND == 3) {
    if (__builtin_expect(s.pend, 0)) {
      asm volatile("s_nop 7");
      mul_ip(s.o[0], s.f); mul_ip(s.o[1], s.f); mul_ip(s.o[2], s.f); mul_ip(s.o[3], s.f);
      s.l *= s.f;
      s.pend = false;
    }
  }
}
template <int X, int KIND, bool DIAG = false> DI void astep(St& s, int c) { astep_<X, KIND, DIAG>(s, c, mkseq<32>{}); }
template <int T> DI void tslot(St& s) {
  if constexpr (T == 0) { frag<16>(s); frag<17>(s); frag<18>(s); }
  else if constexpr (T + 17 < 32 && T >= 2) frag<T + 17>(s);
  if constexpr (T >= 2) s.o[(T - 2) & 3] = MFMA32(s.fr[T + 14], __builtin_bit_cast(bf16x8, s.pf[(T - 2) >> 2]), s.o[(T - 2) & 3]);
  if constexpr (T < 13) { sexp<1, 6 + 2 * T>(s); sexp<1, 7 + 2 * T>(s); }
  if constexpr (T > 0 && T < 14) sadd<1, 4 + 2 * T>(s);
  if constexpr (T < 14) { sadd<1, 5 + 2 * T>(s); scvt<1, T + 2>(s); }
  if constexpr (T == 14) { s.l += s.ls1; mg_stats(s, VR + 32768u, s.m); }
  SBAR();
}
template <int... Ts> DI void atail(St& s, iseq<Ts...>) { (tslot<Ts>(s), ...); }
template <int... Is> DI void mg_write_all(St& s, unsigned a, iseq<Is...>) { (mg_write<Is>(s, a), ...); }
}

__global__ __launch_bounds__(512, 2) void k2_attn(const u16* __restrict__ wq, const u16* __restrict__ wo, const u16* __restrict__ wg, const u16* __restrict__ wu, const u16* __restrict__ wd, char* __restrict__ ws) {
  using namespace k2;
  const unsigned tid = threadIdx.x, lane = tid & 63, w = rfl(tid >> 6), hi = lane >> 5, rq = w & 3, g = w >> 2;
  const unsigned b = blockIdx.x, xcd = b & 7, ix = b >> 3, h = xcd * 4 + (ix & 3), x = ix >> 2;
  St s;
  s.w = w; s.vl16 = lane * 16u;
  TSTAMP(s);
  const unsigned q2 = (lane >> 2) & 3, p2 = lane & 3, g4 = lane >> 4;
  s.rW = mksrd(wq, DM * HDM * 2); s.rX = mksrd(ws + WS_XN, SEQ * DM * 2); s.rK = mksrd(ws + WS_KF, SEQ * HDM * 2); s.rV = mksrd(ws + WS_VF, SEQ * HDM * 2);
  s.rC = mksrd(ws + WS_CTX, SEQ * HDM * 2);
  s.vW = q2 * 8192u + g4 * 64u + p2 * 16u; s.sW = h * 256u + w * 65536u;
  s.wq_rd = QR + hi * 2048u + q2 * 64u + (g4 & 1) * 32u + p2 * 8u; asm volatile("" : "+v"(s.wq_rd));
  s.sX = ((g == 0 ? x : 15u - x) * 4u + rq) * 36864u;
  s.sKV = h * 524288u + w * 4096u; s.n0 = x + 1u; s.sC0 = (2u * rq * 128u + 4u * h + 2u * g) * 1024u;
  s.qw = (g == 0 ? KR + 32768u : SP) + rq * 8192u + s.vl16; asm volatile("" : "+v"(s.qw));
  qload_all<0>(s, mkseq<6>{});
  __builtin_amdgcn_s_barrier();
  qload_all<1>(s, mkseq<6>{});
  qbar<0>(s); SBAR(); TSTAMP(s);
  qread<0>(s); qread<1>(s); qread<2>(s); SBAR();
  qrun(s, mkseq<144>{});
  qpack<5>(s); qpack<6>(s); qpack<7>(s);
  TSTAMP(s);
  wait_bar<0>();
  if (g == 1) {
    unsigned ra = KR + 32768u + rq * 8192u + s.vl16; asm volatile("" : "+v"(ra));
#pragma unroll
    for (int i = 0; i < 8; ++i) s.qf[i] = lds128(lp(ra) + i * 1024);
  }
  { const Dm d = mkdm(s, -1); kvdma<4>(s, d); kvdma<5>(s, d); kvdma<6>(s, d); kvdma<7>(s, d); }
  {
    const unsigned W = b * 8u + w;
    s.rWm = mksrd(W < 434u ? wo : W < 922u ? wg : W < 1410u ? wu : wd, W < 434u ? HDM * DM * 2 : DM * FF * 2);
    s.sWm = (W < 434u ? W : W < 922u ? W - 434u : W < 1410u ? W - 922u : W - 1410u) * 10880u;
    const unsigned l = opaque_lane(); s.vWm = l < 5u ? l * 128u : 0x10000000u;
    s.dummy = 0;
  }
  s.kb = KR + g * 16384u + s.vl16; s.va = VR + 32768u + g * 18432u + s.vl16; s.vb = VR + 32768u + g * 14336u + s.vl16; s.pend = false;
  asm volatile("" : "+v"(s.kb), "+v"(s.va), "+v"(s.vb));
#pragma unroll
  for (int d = 0; d < 4; ++d) s.o[d] = f32x16{};
  { const unsigned l = opaque_lane(), e = (((l >> 2) ^ (l >> 4)) & 1) ? 0u : 0x3f803f80u; s.sel = __builtin_bit_cast(bf16x8, u32x4{e, e, e, e}); s.la = f32x4{}; }
  TSTAMP(s);
  if (x == 0) astep<1, 0, true>(s, 0); else astep<0, 0>(s, 0);
  int c = 1;
  for (int pass = 0; pass < 2; ++pass) {
    if (pass == 1 || x != 0) {
      const int cend = pass == 0 ? (int)x : 16;
      for (; c + 1 < cend; c += 2) { astep<1, 1>(s, c); astep<0, 1>(s, c + 1); }
      if (c < cend) { astep<1, 1>(s, c); ++c; s.sc[0][0] = s.sc[1][0]; s.sc[0][1] = s.sc[1][1]; s.ls0 = s.ls1; }
      astep<1, 1, true>(s, c); ++c;
    }
    if (pass == 0) { astep<0, 2>(s, c); astep<1, 3>(s, c + 1); astep<0, 1>(s, c + 2); c += 3; }
  }
  TSTAMP(s);
  atail(s, mkseq<18>{});
  TSTAMP(s);
  {
    unsigned mine = (g == 0 ? SP : KR + 32768u) + rq * 8192u + s.vl16, theirs = (g == 0 ? KR + 32768u : SP) + rq * 8192u + s.vl16; asm volatile("" : "+v"(mine), "+v"(theirs));
    mg_write_all(s, mine, mkseq<8>{});
    asm volatile("s_waitcnt lgkmcnt(0)\n\ts_barrier" ::: "memory");
    TSTAMP(s);
    mg_read(s, theirs, VR + 32768u); s.oo[0] = s.o[0]; s.oo[1] = s.o[1];
    mg_coef(s, s.m);
    mg_final<0>(s); mg_store<0>(s, 15u - x); mg_final<1>(s); mg_store<1>(s, 15u - x); mg_final<2>(s); mg_store<2>(s, 15u - x); mg_final<3>(s); mg_store<3>(s, 15u - x);
  }
  TSTAMP(s);
  wait_vm<4>();
  asm volatile("" ::"v"(s.dummy));
}

#define K3_OPS 9
#define K3_AAUX ""
#define K3_RA 0u
#define K3_RW 0u
namespace k3 {
#define K3_P 2
constexpr int P = K3_P, DIST = P - 1, WSLOT = 4096, LDS_BYTES = 163840, GRID = 256;
DI const LAS char* lp(unsigned a) { return (const LAS char*)(size_t)a; }
struct St {
  bf16x8 af[P][5], wf[4]; f32x4 acc[4][5];
  unsigned vl16, vW, rd[4], m0b, sA[5], sW, rot, kcur;
  srd_t rA, rW;
  unsigned tsv, nts;
};
template <int NS, int I, int J> DI void load(St& s) {
  constexpr int sl = I % P;
  if constexpr (J == 0) { const unsigned k = s.rot + I; s.kcur = k >= (unsigned)NS ? k - NS : k; }
  if constexpr (J < 4) dma16(s.m0b + sl * WSLOT + J * 1024u, s.vW, s.rW, s.sW + (s.kcur * 32u + J * 8u) * (DM * 2u));
  else asm volatile("buffer_load_dwordx4 %0, %1, %2, %3 offen" : "=v"(s.af[sl][J - 4]) : "v"(s.vl16), "s"(s.rA), "s"(s.sA[J - 4] + s.kcur * 1024u) : "memory");
}
template <int NS, int I, int... Js> DI void load_all(St& s, iseq<Js...>) { (load<NS, I, Js>(s), ...); }
template <int NS, int E1, int I> DI void step(St& s) {
  constexpr int sl = I % P, ahead = (NS - 1 - I) < (DIST - 1) ? (NS - 1 - I) : (DIST - 1);
  asm volatile("s_waitcnt vmcnt(%5)" : "+v"(s.af[sl][0]), "+v"(s.af[sl][1]), "+v"(s.af[sl][2]), "+v"(s.af[sl][3]), "+v"(s.af[sl][4]) : "i"(9 * ahead) : "memory");
  if constexpr ((I & 1) == 0) TSTAMP(s);
#pragma unroll
  for (int t = 0; t < 4; ++t) s.wf[t] = cat8(ldstr(lp(s.rd[t]) + sl * WSLOT), ldstr(lp(s.rd[t]) + sl * WSLOT + E1 * 128));
  SBAR();
  const f32x4 z = {};
#define K3_MM(t, r) s.acc[t][r] = MFMA16(s.wf[t], s.af[sl][r], I == 0 ? z : s.acc[t][r])
#define K3_LD(j) if constexpr (I + DIST < NS) load<NS, I + DIST, j>(s)
  K3_LD(0); K3_MM(0, 0); K3_MM(0, 1); SBAR(); K3_LD(1); K3_MM(0, 2); K3_MM(0, 3); SBAR();
  K3_LD(2); K3_MM(0, 4); K3_MM(1, 0); SBAR(); K3_LD(3); K3_MM(1, 1); K3_MM(1, 2); SBAR();
  K3_LD(4); K3_MM(1, 3); K3_MM(1, 4); SBAR(); K3_LD(5); K3_MM(2, 0); K3_MM(2, 1); SBAR();
  K3_LD(6); K3_MM(2, 2); K3_MM(2, 3); SBAR(); K3_LD(7); K3_MM(2, 4); K3_MM(3, 0); SBAR();
  K3_LD(8); K3_MM(3, 1); K3_MM(3, 2); K3_MM(3, 3); K3_MM(3, 4); SBAR();
#undef K3_MM
#undef K3_LD
}
template <int NS, int E1, int... Is> DI void run(St& s, iseq<Is...>) { (step<NS, E1, Is>(s), ...); }
template <int NS, int... Is> DI void prologue(St& s, iseq<Is...>) { (load_all<NS, Is>(s, mkseq<9>{}), ...); }

DI void repack(const u16* wg, const u16* wu, char* ws, unsigned wgi, unsigned w, unsigned lane) {
  const unsigned task = wgi * 8u + w, blk = task >> 1, half = task & 1;
  const bool on = blk < 72u;
  const srd_t rG = mksrd(wg, DM * FF * 2), rU = mksrd(wu, DM * FF * 2), rP = mksrd(ws + WS_WGU, DM * FF * 4);
  const unsigned base = w * 20480u, row = lane >> 3;
  const unsigned vg = row * (FF * 2u) + (((lane & 7) ^ (((row >> 1) & 1) << 2)) << 4), vu = vg ^ 32u;
  const unsigned ssrc = on ? blk * 128u + half * 288u * (FF * 2u) : 0x10000000u;
  unsigned sdst[4];
#pragma unroll
  for (int p = 0; p < 4; ++p) sdst[p] = (on && (blk * 4u + p) % 9u >= 4u) ? (blk * 4u + p) * 36864u + half * 18432u : 0x10000000u;
  const unsigned hi = lane >> 5, mat = (lane >> 4) & 1, q = (lane >> 2) & 3;
  const unsigned rd = base + mat * 2048u + (8u * hi + q) * 128u + (((q >> 1) << 6) ^ (mat << 5)) + (lane & 3) * 8u;
  const bf16x8 ones = {0x3f80, 0x3f80, 0x3f80, 0x3f80, 0x3f80, 0x3f80, 0x3f80, 0x3f80};
  f32x16 cs[4] = {};
  auto fetch = [&](unsigned st, unsigned slot) {
#pragma unroll
    for (int j = 0; j < 4; ++j) dma16(base + slot * 4096u + j * 1024u, j < 2 ? vg : vu, j < 2 ? rG : rU, ssrc + (st * 16u + (j & 1) * 8u) * (FF * 2u));
  };
#pragma unroll
  for (int i = 0; i < 4; ++i) fetch(i, i);
  for (unsigned st = 0; st < 18u; ++st) {
    const unsigned slot = st % 5u;
    if (st < 4u) wait_vm<12>(); else wait_vm<24>();
    bf16x8 f[4];
#pragma unroll
    for (int p = 0; p < 4; ++p) { const unsigned a = (rd + slot * 4096u) ^ (p << 5); f[p] = cat8(ldstr(lp(a)), ldstr(lp(a) + 512)); }
    fetch(st + 4u < 18u ? st + 4u : st, (st + 4u) % 5u);
#pragma unroll
    for (int p = 0; p < 4; ++p) { cs[p] = MFMA32(f[p], ones, cs[p]); st128m<0>(__builtin_bit_cast(u32x4, f[p]), rP, lane * 16u, sdst[p] + st * 1024u); }
  }
  wait_vm<0>();
  if ((lane & 31) == 0) {
#pragma unroll
    for (int p = 0; p < 4; ++p)
#pragma unroll
      for (int r4 = 0; r4 < 4; ++r4) *(LAS f32x4*)(size_t)(base + p * 128u + r4 * 32u + hi * 16u) = f32x4{cs[p][4 * r4], cs[p][4 * r4 + 1], cs[p][4 * r4 + 2], cs[p][4 * r4 + 3]};
  }
  asm volatile("s_waitcnt lgkmcnt(0)\n\ts_barrier" ::: "memory");
  if (on && half == 0) {
    const f32x2 a = *(const LAS f32x2*)(size_t)(base + lane * 8u), c = *(const LAS f32x2*)(size_t)(base + 20480u + lane * 8u);
    *(f32x2*)(ws + WS_CS + blk * 512u + lane * 8u) = a + c;
  }
}
template <int NK32, bool FIRST> DI void body(const char* A, const u16* W, const u16* x, const u16* wg, const u16* wu, char* ws, u16* out) {
  constexpr int NS = NK32 / 8;
  const unsigned tid = threadIdx.x, lane = tid & 63, w = rfl(tid >> 6), g = lane >> 4, q2 = (lane >> 2) & 3, p2 = lane & 3;
  const unsigned b = blockIdx.x, tile = (b & 7) * 30u + (b >> 3);
  if (b >= 240u || tile >= 234u) {
    if constexpr (FIRST) repack(wg, wu, ws, b >= 240u ? b - 234u : tile - 234u, w, lane);
    return;
  }
  const unsigned rbk = tile / 9u, cb = tile - rbk * 9u;
  St s;
  TSTAMP(s);
  s.rA = mksrd(A, SEQ * NK32 * 64); s.rW = mksrd(W, NK32 * 32 * DM * 2);
  s.vl16 = lane * 16u;
  s.vW = (lane >> 3) * (DM * 2u) + (((lane & 7) ^ (((lane >> 4) & 3) << 1)) << 4);
  s.m0b = 81920u + w * (P * WSLOT);        s.sW = w * NS * 32u * (DM * 2u) + cb * 128u;
#pragma unroll
  for (int r = 0; r < 5; ++r) s.sA[r] = ((rbk * 5u + r) * NK32 + w * NS) * 1024u;
  s.rot = (K3_RA * cb + K3_RW * (rbk & 3)) % NS;
  const unsigned krow = 16u * (g >> 1) + 4u * (g & 1) + q2;
#pragma unroll
  for (int t = 0; t < 4; ++t) { s.rd[t] = s.m0b + krow * 128u + ((((krow >> 1) & 3) ^ t) << 5) + p2 * 8u; asm volatile("" : "+v"(s.rd[t])); }
  const unsigned pc = tid & 7, m0 = tid >> 3;
  unsigned vrm[2], vfr[2];
#pragma unroll
  for (int i = 0; i < 2; ++i) {
    const unsigned m = m0 + 64u * i, row = rbk * 80u + m; const bool ok = m < 80u && row < (unsigned)SEQ;
    vrm[i] = ok ? row * (DM * 2u) + cb * 128u + pc * 16u : 0x10000000u;
    vfr[i] = ok ? (row >> 5) * 36864u + (cb * 8u + pc) * 512u + (row & 31) * 16u : 0x10000000u;
  }
  u32x4 res[2];
  {
    const srd_t rR = FIRST ? mksrd(x, SEQ * DM * 2) : mksrd(ws + WS_H, SEQ * DM * 2);
    asm volatile("buffer_load_dwordx4 %0, %2, %4, 0 offen\n\tbuffer_load_dwordx4 %1, %3, %4, 0 offen"
                 : "=&v"(res[0]), "=&v"(res[1]) : "v"(FIRST ? vrm[0] : vfr[0]), "v"(FIRST ? vrm[1] : vfr[1]), "s"(rR) : "memory");
  }
  prologue<NS>(s, mkseq<DIST>{});
  TSTAMP(s);
  run<NS, 8>(s, mkseq<NS>{});
  TSTAMP(s);
  asm volatile("" : "+v"(res[0]), "+v"(res[1]));
  {
    unsigned pa = (w & 3) * 20480u + s.vl16; asm volatile("" : "+v"(pa));
    if (w < 4u) {
#pragma unroll
      for (int i = 0; i < 20; ++i) *(LAS f32x4*)((LAS char*)(size_t)pa + i * 1024) = s.acc[i / 5][i % 5];
    }
    asm volatile("s_waitcnt lgkmcnt(0)\n\ts_barrier" ::: "memory");
    if (w >= 4u) {
#pragma unroll
      for (int i = 0; i < 20; ++i) s.acc[i / 5][i % 5] += *(const LAS f32x4*)(lp(pa) + i * 1024);
#pragma unroll
      for (int t = 0; t < 4; ++t) {
        unsigned wa = (w & 3) * 20480u + (lane & 15) * 256u + (((4u * t + g) ^ (lane & 15)) << 4); asm volatile("" : "+v"(wa), "+v"(s.acc[3][4]));
#pragma unroll
        for (int r = 0; r < 5; ++r) *(LAS f32x4*)((LAS char*)(size_t)wa + r * 4096) = s.acc[t][r];
      }
    }
  }
  asm volatile("s_waitcnt lgkmcnt(0)\n\ts_barrier" ::: "memory");
  TSTAMP(s);
  const srd_t rO = FIRST ? mksrd(ws + WS_H, SEQ * DM * 2) : mksrd(out, SEQ * DM * 2);
#pragma unroll
  for (int i = 0; i < 2; ++i) {
    if (i == 1 && w >= 2) break;
    unsigned ra = (m0 + 64u * i) * 256u + (((2u * pc) ^ (m0 & 15)) << 4); asm volatile("" : "+v"(ra));
    f32x4 a = *(const LAS f32x4*)(lp(ra)), c = *(const LAS f32x4*)(lp(ra ^ 16u));
#pragma unroll
    for (int ww = 1; ww < 4; ++ww) { a += *(const LAS f32x4*)(lp(ra) + ww * 20480); c += *(const LAS f32x4*)(lp(ra ^ 16u) + ww * 20480); }
    const unsigned y[4] = {cvtpk(a[0], a[1]), cvtpk(a[2], a[3]), cvtpk(c[0], c[1]), cvtpk(c[2], c[3])};
    u32x4 o;
#pragma unroll
    for (int e = 0; e < 4; ++e) o[e] = cvtpk(bflo(res[i][e]) + bflo(y[e]), bfhi(res[i][e]) + bfhi(y[e]));
    st128m<FIRST ? ST3 : ST5>(o, rO, FIRST ? vfr[i] : vrm[i], 0);
    if constexpr (FIRST) {
      float sm = 0.f;
#pragma unroll
      for (int e = 0; e < 4; ++e) sm += bflo(o[e]) + bfhi(o[e]);
      const float mean = dpp_add<0x141>(dpp_add<0x4E>(dpp_add<0xB1>(sm))) * (1.f / 64.f);
      float q = 0.f;
#pragma unroll
      for (int e = 0; e < 4; ++e) { const float d0 = bflo(o[e]) - mean, d1 = bfhi(o[e]) - mean; q += d0 * d0 + d1 * d1; }
      q = dpp_add<0x141>(dpp_add<0x4E>(dpp_add<0xB1>(q)));
      const unsigned m = m0 + 64u * i, row = rbk * 80u + m;
      if (pc == 0 && m < 80u && row < (unsigned)SEQ) *(f32x2*)(ws + WS_ST + (row * 9u + cb) * 8u) = f32x2{mean, q};
    }
  }
}
}
__global__ __launch_bounds__(512, 2) void k3_oproj(const u16* __restrict__ wo, const u16* __restrict__ x, const u16* __restrict__ wg, const u16* __restrict__ wu, char* __restrict__ ws) { k3::body<128, true>(ws + WS_CTX, wo, x, wg, wu, ws, nullptr); }
__global__ __launch_bounds__(512, 2) void k5_down(const u16* __restrict__ wd, char* __restrict__ ws, u16* __restrict__ out) { k3::body<144, false>(ws + WS_ACT, wd, nullptr, nullptr, nullptr, ws, out); }

namespace k4 {
constexpr int SLOT = 36864, CSOFF = 4 * SLOT, LDS_BYTES = CSOFF + 2048, LA = 3, BB = 2, NU = 324;
DI const LAS char* lp(unsigned a) { return (const LAS char*)(size_t)a; }
constexpr int u_pair(int g) { return g < 112 ? g & 3 : g < 144 ? (g - 112) >> 3 : 4 + (g - 144) / 36; }
constexpr int u_kstep(int g) { return g < 112 ? g >> 2 : g < 144 ? 28 + (g & 7) : (g - 144) % 36; }
constexpr int pair_end(int p) { return p < 4 ? 119 + 8 * p : 144 + 36 * (p - 4) + 35; }
constexpr int NE = 13;
constexpr int eks(int e) { return e < 8 ? 2 * e : 18; }
constexpr int estart(int e) { return e < 8 ? 16 * e : 144 + 36 * (e - 8); }
constexpr int bslot(int e) { return estart(e) - BB; }
constexpr int epoch_of(int g) { int e = 0; while (e + 1 < NE && estart(e + 1) <= g) ++e; return e; }
#define K4_NPRO 12
#define K4_RATE 2
constexpr int NPRO = K4_NPRO;
constexpr int s_lo(int g) { const int n = NPRO + (g + K4_RATE - 1) / K4_RATE; return n < 54 ? n : 54; }
constexpr int SEND = (54 - NPRO) * K4_RATE;
constexpr int cfirst(int p) { return p == 4 ? SEND + 1 : p == 5 ? SEND + 16 : p == 6 ? bslot(7) + 3 : p == 7 ? bslot(8) + 1 : bslot(9) + 1; }
static_assert(cfirst(4) > bslot(3) && cfirst(5) > bslot(5), "slot reuse");
constexpr int op_at(int g) {
  for (int p = 4; p < 9; ++p) { const int j = g - cfirst(p); if (j >= 0 && j < 15 && j % 3 == 0) return 100 + 8 * (p - 4) + j / 3; }
  if (g == SEND) return 204;
  return -1;
}
constexpr int NEP = 30, SP = 24;
constexpr int ep_start(int p) { int s = 0; for (int q = 0; q <= p; ++q) { const int a = pair_end(q) + 3; s = (q == 0 || a > s + SP) ? a : s + SP; } return s; }
static_assert(ep_start(7) + NEP <= NU, "epilogues must fit");
constexpr int st_ops(int g) { for (int p = 0; p < 8; ++p) if (g == ep_start(p) + NEP - 2) return 1; return 0; }
constexpr bool sure(int op) { return op >= 0 && !(op >= 100 && op < 200 && ((op - 100) & 7) == 4) && op != 204; }
constexpr int vm_wait(int e) {
  const int to = e ? bslot(e) : 0;
  if (e < 7) { int n = s_lo(to) - 3 * eks(e + 1); for (int g = 0; g < to; ++g) n += sure(op_at(g)) ? 1 : 0; return n; }
  const int want = e == 7 ? 204 : 100 + 8 * (e - 8) + 4;
  int from = 0; while (op_at(from) != want) ++from;
  int n = st_ops(from);
  for (int g = from + 1; g < to; ++g) n += (sure(op_at(g)) ? 1 : 0) + st_ops(g);
  return n;
}
constexpr bool sched_ok() { for (int e = 1; e < 7; ++e) if (s_lo(bslot(e)) < 3 * eks(e + 1)) return false; return SEND < bslot(7); }
static_assert(sched_ok(), "stream ops must precede their barriers");
constexpr int ST0 = 2;
DI unsigned olane() { unsigned l; asm volatile("v_mbcnt_lo_u32_b32 %0, -1, 0\n\tv_mbcnt_hi_u32_b32 %0, -1, %0" : "=v"(l)); return l; }
struct St {
  bf16x8 hf[36], wf[NU]; f32x16 acc[9];
  f32x4 sp0, sp1; f32x2 sq, csg, csu, csg2, csu2; float ag, au, x0, x1, t0, t1, d0, o0, o1; u32x4 pk;
  float nmean, c1, c2;
  unsigned w, wp, wp1, wbA, vA, sA, sW, sHh, csb, vl16, vst, sAct, vsp, p0;
  srd_t rR, rW, rH, rS, rCS, rAct;
  unsigned tsv, nts;
};
template <int OP> DI void vop(St& s) {
  if constexpr (OP < 54) {
    constexpr int ks = OP / 3, j = OP % 3;
    if constexpr (j < 2) asm volatile("buffer_load_dwordx4 %0, %1, %2, %3 offen offset:%4" : "=v"(s.hf[2 * ks + j]) : "v"(s.vl16), "s"(s.rH), "s"(s.sHh + (ks >> 1) * 4096u), "i"(((ks & 1) * 2 + j) * 1024) : "memory");
    else dma16(ks * 8192u + s.w * 1024u, s.vA, s.rR, s.sA + ks * 32u * (FF * 2u));
  } else if constexpr (OP < 200) {
    constexpr int P = 4 + ((OP - 100) >> 3), J = (OP - 100) & 7;
    if (J < 4 || s.w < 4u) dma16((P & 3) * SLOT + J * 8192u + s.w * 1024u, s.vl16, s.rW, s.sW + P * 36864u + J * 8192u + s.w * 1024u);
  } else if constexpr (OP == 200) asm volatile("buffer_load_dwordx4 %0, %1, %2, 0 offen" : "=v"(s.sp0) : "v"(s.vsp), "s"(s.rS) : "memory");
  else if constexpr (OP == 201) asm volatile("buffer_load_dwordx4 %0, %1, %2, 0 offen offset:16" : "=v"(s.sp1) : "v"(s.vsp), "s"(s.rS) : "memory");
  else if constexpr (OP == 202) asm volatile("buffer_load_dwordx2 %0, %1, %2, 0 offen offset:64" : "=v"(s.sq) : "v"(s.csb), "s"(s.rS) : "memory");
  else {
    s.csb = CSOFF + (s.vl16 >> 9) * 16u; asm volatile("" : "+v"(s.csb));
    if (s.w < 2u) dma16(CSOFF + s.w * 1024u, s.vl16, s.rCS, s.p0 * 128u + s.w * 1024u);
  }
}
template <int O0, int... Os> DI void vops(St& s, iseq<Os...>) { (vop<O0 + Os>(s), ...); }
template <int G> DI void reads(St& s) {
  if constexpr (G < NU) {
    constexpr int p = u_pair(G), ks = u_kstep(G);
    if constexpr (G < 144) {
      constexpr int off = ((ks >> 1) - eks(epoch_of(G))) * 8192 + (ks & 1) * 2048 + (p >> 1) * 256; const unsigned a = (p & 1) ? s.wp1 : s.wp;
      s.wf[G] = cat8(ldstr(lp(a) + off), ldstr(lp(a) + off + 512));
    } else s.wf[G] = lds128(lp(s.wp) + ks * 1024);
  }
}
template <int K> DI void pin1(St& s) { asm volatile("" : "+v"(s.hf[2 * K]), "+v"(s.hf[2 * K + 1])); }
template <int E, int... Ks> DI void pin_h(St& s, iseq<Ks...>) { (pin1<eks(E) + Ks>(s), ...); }
template <int E> DI void barrier(St& s) {
  wait_bar<vm_wait(E)>();
  if constexpr (E < 8) {
    pin_h<E>(s, mkseq<eks(E + 1) - eks(E)>{});
    unsigned a = s.wbA + eks(E) * 8192u, b = (s.wbA ^ 32u) + eks(E) * 8192u; asm volatile("" : "+v"(a), "+v"(b)); s.wp = a; s.wp1 = b;
  } else { unsigned a = s.vl16 + ((E - 8) & 3) * SLOT; asm volatile("" : "+v"(a)); s.wp = a; }
  SBAR(); if constexpr (E == 0 || E == 4 || E >= 7) TSTAMP(s);
}
template <int K> DI void stats(St& s) {
  if constexpr (K == 0) { asm volatile("" : "+v"(s.sp0), "+v"(s.sp1), "+v"(s.sq)); s.t0 = (s.sp0[0] + s.sp0[2]) + (s.sp1[0] + s.sp1[2]); s.t1 = (s.sp0[1] + s.sp0[3]) + (s.sp1[1] + s.sp1[3]); }
  else if constexpr (K == 1) s.t0 = (swap32_add(s.t0) + s.sq[0]) * (1.f / 9.f);
  else if constexpr (K == 2) { const float d0 = s.sp0[0] - s.t0, d1 = s.sp0[2] - s.t0; s.t1 = __builtin_fmaf(64.f, d0 * d0 + d1 * d1, s.t1); }
  else if constexpr (K == 3) { const float d0 = s.sp1[0] - s.t0, d1 = s.sp1[2] - s.t0; s.t1 = __builtin_fmaf(64.f, d0 * d0 + d1 * d1, s.t1); }
  else if constexpr (K == 4) { const float d = s.sq[0] - s.t0; s.t1 = swap32_add(s.t1) + __builtin_fmaf(64.f * d, d, s.sq[1]); }
  else if constexpr (K == 5) s.t1 = rbf(rbf(s.t1 * (1.f / DM)) + 1e-5f);
  else if constexpr (K == 6) { s.t1 = rbf(__builtin_amdgcn_sqrtf(s.t1)); s.nmean = -rbf(s.t0); }
  else { s.c1 = -1.4426950408889634f * __builtin_amdgcn_rcpf(s.t1); s.c2 = s.t1 * s.t1; asm volatile("" : "+v"(s.c1), "+v"(s.c2), "+v"(s.nmean)); }
  if constexpr (K < 7) asm volatile("" : "+v"(s.t0), "+v"(s.t1));
}
template <int P, int H> DI void csread(St& s, f32x2& g, f32x2& u) { g = *(const LAS f32x2*)(lp(s.csb) + P * 128 + (H >> 1) * 32 + (H & 1) * 8); u = *(const LAS f32x2*)(lp(s.csb) + P * 128 + 64 + (H >> 1) * 32 + (H & 1) * 8); }
template <int P, int ST> DI void ep(St& s) {
  if constexpr (ST == 0) { csread<P, 0>(s, s.csg, s.csu); asm volatile("s_nop 3"); }
  constexpr int k = (ST + 2) % 3, v = (ST - 1) / 3;
  if constexpr (ST >= 1 && ST < 28) {
    if constexpr (k == 0) {
      if constexpr (v > 0) { s.d0 = __builtin_fmaf(s.x1, s.c2, s.c2); asm volatile("" : "+v"(s.d0)); }
      if constexpr (v < 8) {
        const f32x2& cg = (v & 2) ? s.csg2 : s.csg; const f32x2& cu = (v & 2) ? s.csu2 : s.csu;
        s.ag = __builtin_fmaf(s.nmean, cg[v & 1], s.acc[P][v]); s.au = __builtin_fmaf(s.nmean, cu[v & 1], s.acc[P][8 + v]); asm volatile("" : "+v"(s.ag), "+v"(s.au));
        if constexpr ((v & 1) == 0 && v < 6) { if constexpr (v & 2) csread<P, (v >> 1) + 1>(s, s.csg, s.csu); else csread<P, (v >> 1) + 1>(s, s.csg2, s.csu2); }
      }
    } else if constexpr (k == 1) {
      if constexpr (v > 0) { s.d0 = __builtin_amdgcn_rcpf(s.d0); asm volatile("" : "+v"(s.d0)); }
      if constexpr (v < 8) { s.x0 = s.ag * s.c1; s.t0 = s.ag * s.au; asm volatile("" : "+v"(s.x0), "+v"(s.t0)); }
    } else {
      if constexpr (v > 0) { float o = s.t1 * s.d0; asm volatile("" : "+v"(o)); if constexpr (v & 1) s.o0 = o; else s.o1 = o; }
      if constexpr (v < 8) { s.x1 = __builtin_amdgcn_exp2f(s.x0); s.t1 = s.t0; asm volatile("" : "+v"(s.x1)); }
    }
  }
  if constexpr (ST >= 10 && ST <= 28 && (ST - 10) % 6 == 0) { unsigned pkd; asm volatile("v_cvt_pk_bf16_f32 %0, %1, %2" : "=v"(pkd) : "v"(s.o0), "v"(s.o1)); s.pk[(ST - 10) / 6] = pkd; }
  if constexpr (ST == NEP - 1) st128m<ST4>(s.pk, s.rAct, s.vst, s.sAct + P * 512u);
}
template <int G, int P> DI void ep_try(St& s) {
  if constexpr (G >= ep_start(P) - 1 && G < ep_start(P) - 1 + NEP) ep<P, G - ep_start(P) + 1>(s); }
template <int G> DI void slot(St& s) {
  constexpr int en = epoch_of(G) + 1;
  if constexpr (en < NE && G == bslot(en)) { barrier<en>(s); reads<G + 2>(s); reads<G + 3>(s); }
  else if constexpr (!(en < NE && G + LA >= estart(en) && G < bslot(en))) reads<G + LA>(s);
  vops<s_lo(G)>(s, mkseq<s_lo(G + 1) - s_lo(G)>{});
  if constexpr (op_at(G) >= 0) vop<op_at(G)>(s);
  constexpr int p = u_pair(G), ks = u_kstep(G);
  if constexpr (ks == 0) MFMA32_NEW(s.acc[p], s.wf[G], s.hf[ks]); else MFMA32_ACC(s.acc[p], s.wf[G], s.hf[ks]);
  if constexpr (G >= ST0 && G < ST0 + 8) stats<G - ST0>(s);
  ep_try<G, 0>(s); ep_try<G, 1>(s); ep_try<G, 2>(s); ep_try<G, 3>(s); ep_try<G, 4>(s); ep_try<G, 5>(s); ep_try<G, 6>(s); ep_try<G, 7>(s);
  SBAR();
}
template <int... Gs> DI void run(St& s, iseq<Gs...>) { (slot<Gs>(s), ...); }
template <int... Ss> DI void ep_all(St& s, iseq<Ss...>) { (ep<8, Ss>(s), ...); }
}

__global__ __launch_bounds__(512, 2) void k4_up(const u16* __restrict__ wg, const u16* __restrict__ wu, char* __restrict__ ws) {
  using namespace k4;
  const unsigned tid = threadIdx.x, lane = tid & 63, w = rfl(tid >> 6), r = lane & 31, hi = lane >> 5, blk = (lane >> 4) & 1, q2 = (lane >> 2) & 3, p2 = lane & 3;
  const unsigned b = blockIdx.x, xcd = b & 7, ix = b >> 3;
  const unsigned rb = ix >> 2, cb = xcd * 4 + (ix & 3);
  St s;
  TSTAMP(s);
  s.w = w; s.rW = mksrd(ws + WS_WGU, DM * FF * 4); s.rAct = mksrd(ws + WS_ACT, SEQ * FF * 2);
  s.rH = mksrd(ws + WS_H, SEQ * DM * 2); s.rS = mksrd(ws + WS_ST, SEQ * 72); s.rCS = mksrd(ws + WS_CS, FF * 8);
  const unsigned row0 = rb * 256u + w * 32u;
  s.p0 = cb * 9u; s.sW = cb * 9u * 36864u; s.rR = mksrd(w < 4 ? wg : wu, DM * FF * 2);
  s.vA = (4u * hi + q2) * (FF * 2u) + blk * 64u + ((p2 ^ ((w >> 2) << 1)) << 4); s.sA = cb * 288u + (w & 3) * 8u * (FF * 2u);
  s.wbA = blk * 4096u + hi * 1024u + q2 * 64u + blk * 32u + p2 * 8u;
  s.vl16 = lane * 16u; s.sHh = (row0 >> 5) * 36864u;
  s.vst = (r >> 4) * 147456u + hi * 256u + (r & 15) * 16u;
  s.sAct = (rb * 16u + w * 2u) * 147456u + cb * 9u * 512u;
  s.csb = (row0 + r) * 72u; s.vsp = s.csb + hi * 32u;
  vops<200>(s, mkseq<3>{});
  vops<0>(s, mkseq<6>{});
  __builtin_amdgcn_s_barrier();
  vops<6>(s, mkseq<NPRO - 6>{});
  barrier<0>(s);
  reads<0>(s); reads<1>(s); reads<2>(s); SBAR();
  run(s, mkseq<NU>{});
  TSTAMP(s);
  asm volatile("s_nop 7\n\ts_nop 7" ::: "memory"); SBAR();
  ep_all(s, mkseq<NEP>{});
}

extern "C" void kernel_launch(void* const* d_in, const int* in_sizes, int n_in, void* d_out, int out_size, void* d_ws, size_t ws_size, hipStream_t stream) {
  const u16* x = (const u16*)d_in[0]; const u16* ckv = (const u16*)d_in[1]; const u16* wq = (const u16*)d_in[2]; const u16* wuk = (const u16*)d_in[3];
  const u16* wuv = (const u16*)d_in[4]; const u16* wo = (const u16*)d_in[5]; const u16* wg = (const u16*)d_in[7]; const u16* wu = (const u16*)d_in[8]; const u16* wd = (const u16*)d_in[9];
  char* ws = (char*)d_ws;
  (void)hipFuncSetAttribute((const void*)k1_kvproj, hipFuncAttributeMaxDynamicSharedMemorySize, k1::LDS_BYTES);
  k1_kvproj<<<256, 512, k1::LDS_BYTES, stream>>>(ckv, wuk, wuv, x, wq, ws);
  (void)hipFuncSetAttribute((const void*)k2_attn, hipFuncAttributeMaxDynamicSharedMemorySize, k2::LDS_BYTES);
  k2_attn<<<256, 512, k2::LDS_BYTES, stream>>>(wq, wo, wg, wu, wd, ws);
  (void)hipFuncSetAttribute((const void*)k3_oproj, hipFuncAttributeMaxDynamicSharedMemorySize, k3::LDS_BYTES);
  k3_oproj<<<k3::GRID, 512, k3::LDS_BYTES, stream>>>(wo, x, wg, wu, ws);
  (void)hipFuncSetAttribute((const void*)k4_up, hipFuncAttributeMaxDynamicSharedMemorySize, k4::LDS_BYTES);
  k4_up<<<256, 512, k4::LDS_BYTES, stream>>>(wg, wu, ws);
  (void)hipFuncSetAttribute((const void*)k5_down, hipFuncAttributeMaxDynamicSharedMemorySize, k3::LDS_BYTES);
  k5_down<<<k3::GRID, 512, k3::LDS_BYTES, stream>>>(wd, ws, (u16*)d_out);
}
```

```cpp
#include <hip/hip_runtime.h>
#include <utility>
typedef unsigned short u16;
typedef short bf16x8 __attribute__((ext_vector_type(8)));
typedef short s16x4 __attribute__((ext_vector_type(4)));
typedef float f32x16 __attribute__((ext_vector_type(16)));
typedef float f32x4 __attribute__((ext_vector_type(4)));
typedef float f32x2 __attribute__((ext_vector_type(2)));
typedef unsigned u32x4 __attribute__((ext_vector_type(4)));
typedef unsigned u32x2 __attribute__((ext_vector_type(2)));
typedef __bf16 bf16x2_t __attribute__((ext_vector_type(2)));
#define LAS __attribute__((address_space(3)))
#define DI __device__ __forceinline__
#define SBAR() __builtin_amdgcn_sched_barrier(0)
#define MFMA32(a, b, c) __builtin_amdgcn_mfma_f32_32x32x16_bf16((a), (b), (c), 0, 0, 0)
#define MFMA16(a, b, c) __builtin_amdgcn_mfma_f32_16x16x32_bf16((a), (b), (c), 0, 0, 0)
#define MFMA16_ACC(c, a, b) asm volatile("v_mfma_f32_16x16x32_bf16 %0, %1, %2, %0" : "+v"(c) : "v"(a), "v"(b))
#define MFMA16_NEW(c, a, b) asm volatile("v_mfma_f32_16x16x32_bf16 %0, %1, %2, 0" : "=v"(c) : "v"(a), "v"(b))
#define MFMA32_ACC(c, a, b) asm volatile("v_mfma_f32_32x32x16_bf16 %0, %1, %2, %0" : "+v"(c) : "v"(a), "v"(b))
#define MFMA32_NEW(c, a, b) asm volatile("v_mfma_f32_32x32x16_bf16 %0, %1, %2, 0" : "=v"(c) : "v"(a), "v"(b))
template <int... Is> using iseq = std::integer_sequence<int, Is...>;
template <int N> using mkseq = std::make_integer_sequence<int, N>;
typedef __amdgpu_buffer_rsrc_t srd_t;

DI unsigned rfl(unsigned x) { return __builtin_amdgcn_readfirstlane(x); }
DI float bf2f(unsigned v) { return __uint_as_float(v << 16); }
DI float bflo(unsigned w) { return __uint_as_float(w << 16); }
DI float bfhi(unsigned w) { return __uint_as_float(w & 0xffff0000u); }
DI unsigned cvtpk(float lo, float hi) { f32x2 v = {lo, hi}; bf16x2_t b = __builtin_convertvector(v, bf16x2_t); return __builtin_bit_cast(unsigned, b); }
DI float rbf(float f) { return bflo(cvtpk(f, f)); }
DI srd_t mksrd(const void* p, unsigned bytes) { return __builtin_amdgcn_make_buffer_rsrc((void*)p, (short)0, (int)bytes, 0x00020000); }
DI void dma16(unsigned m0v, unsigned voff, srd_t srd, unsigned soff) {
  asm volatile("s_mov_b32 m0, %0\n\ts_nop 0\n\tbuffer_load_dwordx4 %1, %2, %3 offen lds" ::"s"(m0v), "v"(voff), "s"(srd), "s"(soff) : "memory");
}
template <int N> DI void wait_vm() { asm volatile("s_waitcnt vmcnt(%0)" ::"i"(N) : "memory"); }
template <int N> DI void wait_bar() { asm volatile("s_waitcnt vmcnt(%0) lgkmcnt(0)\n\ts_barrier" ::"i"(N) : "memory"); }
DI bf16x8 lds128(const LAS char* p) { return *(const LAS bf16x8*)p; }
typedef short v4i16_t __attribute__((ext_vector_type(4)));
DI s16x4 ldstr(const LAS char* p) { return __builtin_bit_cast(s16x4, __builtin_amdgcn_ds_read_tr16_b64_v4i16((LAS v4i16_t*)p)); }
DI bf16x8 cat8(s16x4 a, s16x4 b) { return __builtin_shufflevector(a, b, 0, 1, 2, 3, 4, 5, 6, 7); }
DI u32x4 pack8(const f32x16& x, int s) {
  u32x4 w = {cvtpk(x[8 * s], x[8 * s + 1]), cvtpk(x[8 * s + 2], x[8 * s + 3]), cvtpk(x[8 * s + 4], x[8 * s + 5]), cvtpk(x[8 * s + 6], x[8 * s + 7])};
  return w;
}
template <int MODE> DI void st128m(u32x4 v, srd_t srd, unsigned voff, unsigned soff) {
  if constexpr (MODE == 0) asm volatile("buffer_store_dwordx4 %0, %1, %2, %3 offen\n\ts_nop 1" ::"v"(v), "v"(voff), "s"(srd), "s"(soff) : "memory");
  else if constexpr (MODE == 1) asm volatile("buffer_store_dwordx4 %0, %1, %2, %3 offen sc1\n\ts_nop 1" ::"v"(v), "v"(voff), "s"(srd), "s"(soff) : "memory");
  else if constexpr (MODE == 2) asm volatile("buffer_store_dwordx4 %0, %1, %2, %3 offen nt\n\ts_nop 1" ::"v"(v), "v"(voff), "s"(srd), "s"(soff) : "memory");
  else asm volatile("buffer_store_dwordx4 %0, %1, %2, %3 offen sc0 sc1\n\ts_nop 1" ::"v"(v), "v"(voff), "s"(srd), "s"(soff) : "memory");
}
#define ST1 1
#define ST2 1
#define ST3 1
#define ST4 1
#define ST6 0
#define ST5 1
template <int CTRL> DI float dpp_add(float x) { return x + __builtin_bit_cast(float, __builtin_amdgcn_update_dpp(0, __builtin_bit_cast(int, x), CTRL, 0xf, 0xf, true)); }
DI float swap32_add(float x) { auto r = __builtin_amdgcn_permlane32_swap(__float_as_uint(x), __float_as_uint(x), false, false); return __uint_as_float(r[0]) + __uint_as_float(r[1]); }
DI float swap16_add(float x) { auto r = __builtin_amdgcn_permlane16_swap(__float_as_uint(x), __float_as_uint(x), false, false); return __uint_as_float(r[0]) + __uint_as_float(r[1]); }
DI float swap32_max(float x) { auto r = __builtin_amdgcn_permlane32_swap(__float_as_uint(x), __float_as_uint(x), false, false); return __builtin_fmaxf(__uint_as_float(r[0]), __uint_as_float(r[1])); }
DI float wave_sum(float x) {
  x = dpp_add<0xB1>(x); x = dpp_add<0x4E>(x); x = dpp_add<0x141>(x); x = dpp_add<0x140>(x);
  x = swap16_add(x); return swap32_add(x);
}

constexpr int SEQ = 2048, DM = 576, DC = 512, NH = 32, HD = 128, FF = 4608, HDM = NH * HD;
constexpr size_t WS_XN = 0;
constexpr size_t WS_KF = WS_XN + (size_t)SEQ * DM * 2;
constexpr size_t WS_VF = WS_KF + (size_t)SEQ * HDM * 2;
constexpr size_t WS_CTX = WS_VF + (size_t)SEQ * HDM * 2;
constexpr size_t WS_QS = WS_CTX + (size_t)SEQ * HDM * 2;
constexpr size_t WS_WO = WS_QS + (size_t)256 * 32768;
constexpr size_t WS_WGU = WS_WO + (size_t)HDM * DM * 2;
constexpr size_t WS_WD = WS_WGU + (size_t)DM * FF * 4;
constexpr size_t WS_H = WS_WD + (size_t)FF * DM * 2;
constexpr size_t WS_ST = WS_H + (size_t)SEQ * DM * 2;
constexpr size_t WS_CS = WS_ST + (size_t)SEQ * 128;
constexpr size_t WS_ACT = WS_CS + (size_t)288 * 128;
constexpr size_t WS_XB = WS_ACT + (size_t)SEQ * FF * 2;
constexpr size_t WS_FLAG = WS_XB + (size_t)2 * 234 * 20480;
constexpr size_t WS_END = WS_FLAG + 65536;

#define TSTAMP(s) do {} while (0)

namespace k1 {
constexpr int SLOT = 53248, XSCR = 32768  , DUMP = 3 * SLOT  , LDS_BYTES = DUMP + 2048, LA = 3  , BB = 2  ;
constexpr int NC = 11, P1 = 160;
constexpr int cstart(int c) { return c <= 8 ? 20 * c : P1 + 32 * (c - 8); }
constexpr int chunk_of(int g) { return g < P1 ? g / 20 : 8 + ((g - P1) >> 5); }
constexpr int bslot(int c) { return cstart(c) - BB; }
constexpr int dcnt(int c) { return c < 8 ? 7 : (c < NC ? 4 : 0); }
constexpr int kt_of(int g) { const int c = chunk_of(g), i = g - cstart(c); return c < 7 ? (i % 10) : c == 7 ? (i >> 1) : (i & 1); }
constexpr int sloc_of(int g) { const int c = chunk_of(g), i = g - cstart(c); return c < 7 ? (i / 10) : c == 7 ? (i & 1) : (i >> 1); }
constexpr int kstep_of(int g) { const int c = chunk_of(g); return c < 8 ? 2 * c + sloc_of(g) : sloc_of(g); }
constexpr bool w_first(int g) { const int c = chunk_of(g), i = g - cstart(c); return c < 7 ? (i % 10) == 0 : (c == 7 && i < 2); }
constexpr int acc_of(int g) { const int c = chunk_of(g); return c < 8 ? kt_of(g) : 10 + 2 * (c - 8) + kt_of(g); }
constexpr int dma_at(int g) { for (int c = 0; c < NC - 2; ++c) { const int j = g - (c ? bslot(c) : 0); if (j >= 0 && j < 2 * dcnt(c + 2) && !(j & 1)) return (c + 2) * 8 + (j >> 1); } return -1; }
constexpr bool sure(int d) { return d >= 0 && (d & 7) != 6; }
constexpr int pk_at(int g) {
  for (int u = 0; u < 5; ++u) { if (g == 140 + 4 * u + 6) return 2 * u; if (g == 140 + 4 * u + 7) return 2 * u + 1; }
  for (int u = 5; u < 7; ++u) { if (g == cstart(u + 4) + 3) return 2 * u; if (g == cstart(u + 4) + 4) return 2 * u + 1; }
  return -1;
}
constexpr int st_at(int g) {
  if (g < 155 || (g - 155) % 7 != 0) return -1;
  constexpr int order[15] = {0, 1, 2, 3, 4, 5, 6, 10, 7, 11, 8, 9, 12, 13, -1};
  return order[(g - 155) / 7];
}
constexpr int XWARM = 15, XLD = 161, LN0 = 172, NLN = 48, LNRD = 226, LNST = 230, WARM = 236;
constexpr int ln_at(int g) { return (g >= LN0 && g < LN0 + NLN) ? g - LN0 : -1; }
constexpr int late_ops(int g) { return (st_at(g) >= 0 ? 1 : 0) + (g == LNST ? 2 : 0) + (g == WARM || g == XWARM ? 1 : 0) + (g == XLD ? 2 : 0); }
constexpr int ops_between(int from, int to) { int n = 0; for (int g = from + 1; g < to; ++g) n += (sure(dma_at(g)) ? 1 : 0) + late_ops(g); return n; }
constexpr int vm_wait(int c) {
  if (c == 0) return 6;
  if (c == 1) return ops_between(-1, bslot(1));
  const int from = (c == 2 ? 0 : bslot(c - 2)) + 2 * (dcnt(c) - 1);
  return late_ops(from) + ops_between(from, bslot(c));
}
DI unsigned olane() { unsigned l; asm volatile("v_mbcnt_lo_u32_b32 %0, -1, 0\n\tv_mbcnt_hi_u32_b32 %0, -1, %0" : "=v"(l)); return l; }
struct St {
  bf16x8 wf[16][2]; bf16x8 af[256]; f32x4 acc[16][2]; u32x4 pk[16];
  unsigned wp, ap[4];
  unsigned wb, ab0, ab1, voffA0, voffW, m0w, sA, sW, vst, sO;
  srd_t rA, rK, rV, rO, rXN, rX; unsigned sX;
  u32x4 x0, x1; float v[16], t0, t1, t2; unsigned lane, sXN; bool two;
  srd_t rWm; unsigned sWm;
};
template <int C, int J> DI void dma(St& s) {
  constexpr unsigned slot = (C % 3) * SLOT;
  if constexpr (C < 8) {
    if constexpr (J < 4) {
      constexpr unsigned mat = J >> 1, jj = J & 1;
      dma16(s.m0w * 2048u + slot + mat * 16384u + jj * 1024u, s.voffW, mat ? s.rV : s.rK, s.sW + s.m0w * 65536u + jj * 32768u + C * 524288u);
    } else if constexpr (J < 6) dma16(s.m0w * 2048u + slot + 32768u + (J - 4) * 1024u, s.voffA0, s.rA, s.sA + s.m0w * 16384u + (J - 4) * 8192u + C * 128u);
    else { if (s.m0w < 4u) dma16(s.m0w * 1024u + slot + 49152u, s.voffA0 ^ (((s.m0w ^ (s.m0w >> 1)) & 1u) << 6), s.rA, s.sA + 131072u + s.m0w * 8192u + C * 128u); }
  } else dma16(s.m0w * 4096u + slot + J * 1024u, (olane() ^ (8u * (s.m0w >> 2) + ((4u * s.m0w + J) & 7u))) << 4, s.rA, s.sA + (160u + (C - 8) * 32u + J) * 1024u + s.m0w * 4096u);
}
DI const LAS char* lp(unsigned a) { return (const LAS char*)(size_t)a; }
template <int G> DI void reads(St& s) {
  if constexpr (G < 256) {
    constexpr int c = chunk_of(G), kt = kt_of(G), sl = sloc_of(G), ks = kstep_of(G);
    if constexpr (w_first(G)) {
      s.wf[ks][0] = cat8(ldstr(lp(s.wp) + sl * 8192), ldstr(lp(s.wp) + sl * 8192 + 1024));
      s.wf[ks][1] = cat8(ldstr(lp(s.wp ^ 16u) + sl * 8192), ldstr(lp(s.wp ^ 16u) + sl * 8192 + 1024));
    }
    if constexpr (c < 8) s.af[G] = lds128(lp(s.ap[sl]) + (kt >> 1) * 4096 + (kt & 1) * 1024);
    else s.af[G] = lds128(lp(s.ap[sl & 3]) + (sl >> 2) * 256 + kt * 8192);
  }
}
template <int C> DI void set_bases(St& s) {
  constexpr unsigned slot = (C % 3) * SLOT;
  if constexpr (C < 8) {
    unsigned w = s.wb + slot, a0 = s.ab0 + slot, a1 = (s.ab0 ^ 64u) + slot;
    asm volatile("" : "+v"(w), "+v"(a0), "+v"(a1));
    s.wp = w; s.ap[0] = a0; s.ap[1] = a1;
  } else {
    if constexpr (C == 8) { const unsigned l = olane(), r = l & 15; s.ab1 = (16u * (r >> 3) + (r & 7)) * 1024u + (((l >> 4) ^ r) << 4); s.lane = l; s.two = l < 8u; }
    unsigned a0 = s.ab1 + slot, a1 = (s.ab1 ^ 64u) + slot, a2 = (s.ab1 ^ 128u) + slot, a3 = (s.ab1 ^ 192u) + slot;
    asm volatile("" : "+v"(a0), "+v"(a1), "+v"(a2), "+v"(a3));
    s.ap[0] = a0; s.ap[1] = a1; s.ap[2] = a2; s.ap[3] = a3;
  }
}
template <bool ISV, int ID> DI void pack(St& s) {
  constexpr int u = ID >> 1, j = ID & 1;
  const f32x4 a = ISV ? s.acc[2 * u][j] : s.acc[2 * u + j][0], b = ISV ? s.acc[2 * u + 1][j] : s.acc[2 * u + j][1];
  s.pk[ID] = u32x4{cvtpk(a[0], a[1]), cvtpk(a[2], a[3]), cvtpk(b[0], b[1]), cvtpk(b[2], b[3])};
  asm volatile("" : "+v"(s.pk[ID]));
}
template <int ID> DI void store(St& s) { st128m<ST1>(s.pk[ID], s.rO, s.vst, s.sO + (ID >> 1) * 8192u + (ID & 1) * 128u); }
template <int ST> DI void ln_stage(St& s) {
  if constexpr (ST < 4) {
    if constexpr (ST == 0) { asm volatile("s_waitcnt vmcnt(%2)" : "+v"(s.x0), "+v"(s.x1) : "i"(ops_between(XLD, LN0 + 1)) : "memory"); s.t0 = 0.f; s.t2 = 0.f; }
    s.v[2 * ST] = bflo(s.x0[ST]); s.v[2 * ST + 1] = bfhi(s.x0[ST]); s.t0 += s.v[2 * ST] + s.v[2 * ST + 1];
  } else if constexpr (ST < 8) { constexpr int i = ST - 4; s.v[8 + 2 * i] = bflo(s.x1[i]); s.v[9 + 2 * i] = bfhi(s.x1[i]); s.t2 += s.v[8 + 2 * i] + s.v[9 + 2 * i]; }
  else if constexpr (ST == 8) s.t0 = dpp_add<0xB1>(s.t0 + (s.two ? s.t2 : 0.f));
  else if constexpr (ST == 9) s.t0 = dpp_add<0x141>(dpp_add<0x4E>(s.t0));
  else if constexpr (ST == 10) s.t0 = dpp_add<0x140>(s.t0);
  else if constexpr (ST == 11) s.t0 = swap16_add(s.t0);
  else if constexpr (ST == 12) { s.t0 = swap32_add(s.t0) * (1.f / DM); s.t1 = 0.f; s.t2 = 0.f; }
  else if constexpr (ST < 21) { constexpr int i = 2 * (ST - 13); float& q = i < 8 ? s.t1 : s.t2; const float d = s.v[i] - s.t0, e = s.v[i + 1] - s.t0; q += d * d; q += e * e; }
  else if constexpr (ST == 21) s.t1 = dpp_add<0xB1>(s.t1 + (s.two ? s.t2 : 0.f));
  else if constexpr (ST == 22) s.t1 = dpp_add<0x141>(dpp_add<0x4E>(s.t1));
  else if constexpr (ST == 23) s.t1 = dpp_add<0x140>(s.t1);
  else if constexpr (ST == 24) s.t1 = swap16_add(s.t1);
  else if constexpr (ST == 25) s.t1 = swap32_add(s.t1) * (1.f / DM);
  else if constexpr (ST == 26) { s.t0 = rbf(s.t0); s.t1 = rbf(s.t1); }
  else if constexpr (ST == 27) s.t1 = rbf(s.t1 + 1e-5f);
  else if constexpr (ST == 28) s.t1 = rbf(__builtin_amdgcn_sqrtf(s.t1));
  else if constexpr (ST == 29) s.t1 = __builtin_amdgcn_rcpf(s.t1);
  else if constexpr (ST < 46) {
    constexpr int i = (ST - 30) >> 1; u32x4& o = i < 4 ? s.x0 : s.x1;
    if constexpr (((ST - 30) & 1) == 0) o[i & 3] = cvtpk(s.v[2 * i] - s.t0, s.v[2 * i + 1] - s.t0);
    else o[i & 3] = cvtpk(bflo(o[i & 3]) * s.t1, bfhi(o[i & 3]) * s.t1);
    asm volatile("" : "+v"(o));
  } else if constexpr (ST == 46) *(LAS u32x4*)(size_t)(XSCR + s.lane * 128u + ((s.m0w ^ (s.lane & 7)) << 4)) = s.x0;
  else { if (s.two) *(LAS u32x4*)(size_t)(XSCR + 8192u + s.lane * 128u + ((s.m0w ^ (s.lane & 7)) << 4)) = s.x1; }
  if constexpr (ST < 30) asm volatile("" : "+v"(s.t0), "+v"(s.t1), "+v"(s.t2));
}
template <bool ISV, int G> DI void slot(St& s) {
  constexpr int cn = chunk_of(G) + 1;
  constexpr bool isbar = cn < NC && G == bslot(cn);
  if constexpr (isbar) {
    wait_bar<vm_wait(cn)>(); set_bases<cn>(s); SBAR();
    reads<G + BB>(s); reads<G + BB + 1>(s);
  } else if constexpr (!(cn < NC && G + LA >= cstart(cn) && G < bslot(cn))) reads<G + LA>(s);
  if constexpr (dma_at(G) >= 0) dma<(dma_at(G) >> 3), (dma_at(G) & 7)>(s);
  constexpr int ai = acc_of(G), ks = kstep_of(G);
#pragma unroll
  for (int ct = 0; ct < 2; ++ct) {
    const bf16x8 a = ISV ? s.af[G] : s.wf[ks][ct], bq = ISV ? s.wf[ks][ct] : s.af[G];
    if constexpr (ks == 0) MFMA16_NEW(s.acc[ai][ct], a, bq); else MFMA16_ACC(s.acc[ai][ct], a, bq);
  }
  if constexpr (pk_at(G) >= 0) pack<ISV, pk_at(G)>(s);
  if constexpr (st_at(G) >= 0) store<st_at(G)>(s);
  if constexpr (G == XLD) asm volatile("buffer_load_dwordx4 %0, %2, %4, %5 offen\n\tbuffer_load_dwordx4 %1, %3, %4, %5 offen offset:1024"
                                       : "=&v"(s.x0), "=&v"(s.x1) : "v"(s.lane * 16u), "v"((s.lane & 7) * 16u), "s"(s.rX), "s"(s.sX) : "memory");
  if constexpr (ln_at(G) >= 0) ln_stage<ln_at(G)>(s);
  if constexpr (G == XWARM) { const unsigned l = olane();
    asm volatile("s_mov_b32 m0, %0\n\ts_nop 0\n\tbuffer_load_dword %1, %2, %3 offen lds" ::"s"(DUMP + s.m0w * 256u), "v"(l < 9u ? l * 128u : 0x10000000u), "s"(s.rX), "s"(s.sX) : "memory"); }
  if constexpr (G == LNRD) {
    s.x0 = *(const LAS u32x4*)(size_t)(XSCR + s.m0w * 1024u + s.lane * 16u); s.x1 = *(const LAS u32x4*)(size_t)(XSCR + 8192u + s.lane * 16u);
  }
  if constexpr (G == LNST) {
    const unsigned vo = (s.lane >> 3) * 512u + (((s.lane ^ (s.lane >> 3)) & 7) << 4);
    st128m<ST1>(s.x0, s.rXN, vo, s.sXN + s.m0w * 4096u); st128m<ST1>(s.x1, s.rXN, vo, s.m0w == 0 ? s.sXN + 32768u : 0x10000000u);
  }
  if constexpr (G == WARM)
    asm volatile("s_mov_b32 m0, %0\n\ts_nop 0\n\tbuffer_load_dword %1, %2, %3 offen lds" ::"s"(DUMP + s.m0w * 256u), "v"(s.lane < 18u ? (s.lane >> 1) * 8192u + (s.lane & 1) * 128u : 0x10000000u), "s"(s.rWm), "s"(s.sWm) : "memory");
  SBAR();
}
template <bool ISV, int... Gs> DI void run(St& s, iseq<Gs...>) {
  reads<0>(s); reads<1>(s); reads<2>(s); SBAR();
  (slot<ISV, Gs>(s), ...);
  asm volatile("s_nop 7\n\ts_nop 3" ::: "memory"); SBAR();
  pack<ISV, 14>(s); store<14>(s); pack<ISV, 15>(s); store<15>(s);
}
template <int C, int... Js> DI void dma_all(St& s, iseq<Js...>) { (dma<C, Js>(s), ...); }
}

__global__ __launch_bounds__(512, 2) void k1_kvproj(const u16* __restrict__ ckv, const u16* __restrict__ wuk, const u16* __restrict__ wuv, const u16* __restrict__ x,
    const u16* __restrict__ wq, char* __restrict__ ws) {
  using namespace k1;
  const unsigned tid = threadIdx.x, lane = tid & 63, w = rfl(tid >> 6), r = lane & 15, g = lane >> 4;
  const unsigned b = blockIdx.x, xcd = b & 7, ix = b >> 3, h = xcd * 4 + (ix & 3), rb = ix >> 2;
  St s;
  s.rA = mksrd(ckv, SEQ * DC * 2); s.rK = mksrd(wuk, DC * HDM * 2); s.rV = mksrd(wuv, DC * HDM * 2);
  s.rO = mksrd(ws + (w < 4 ? WS_KF : WS_VF), SEQ * HDM * 2); s.rXN = mksrd(ws + WS_XN, SEQ * DM * 2);
  s.m0w = w; s.sXN = (b >> 2) * 36864u + (b & 3) * 128u;
  const unsigned q2 = (lane >> 2) & 3, p2 = lane & 3, rrow = 16u * (r >> 3) + (r & 7);
  s.voffA0 = (lane >> 3) * 1024u + (((lane & 7) ^ (4u * (w & 1) + g)) << 4);
  s.voffW = q2 * 8192u + g * 64u + ((p2 ^ (w & 1u)) << 4);
  s.sA = rb * 256u * 1024u; s.sW = h * 256u;
  s.wb = (w >> 2) * 16384u + (w & 3) * 256u + g * 2048u + q2 * 64u + (p2 >> 1) * 32u + (g & 1) * 16u + (p2 & 1) * 8u;
  s.ab0 = 32768u + rrow * 128u + ((g ^ (r >> 1)) << 4);
  s.vst = (g >> 1) * (w < 4 ? 1024u : 4096u) + (rrow + 32u * (g & 1)) * 16u;
  s.sO = (h * 64u + rb * 8u) * 8192u + (w < 4 ? w * 2048u : (w - 4) * 1024u);
  s.rWm = mksrd(wq, DM * HDM * 2); s.sWm = (rb * 72u + w * 9u) * 8192u + h * 256u;
  s.rX = mksrd(x, SEQ * DM * 2); s.sX = (b * 8u + w) * (DM * 2u);
  if (tid < 2u) *(unsigned*)(ws + WS_FLAG + (tid * 256u + b) * 128u) = 0u;
  dma_all<0>(s, mkseq<7>{});
  __builtin_amdgcn_s_barrier();
  dma_all<1>(s, mkseq<7>{});
  wait_bar<vm_wait(0)>(); set_bases<0>(s); SBAR();
  if (w < 4) run<false>(s, mkseq<256>{}); else run<true>(s, mkseq<256>{});
}

namespace k2 {
constexpr unsigned KR = 0, VR = 65536, SP = 131072, QR = VR  , LDS_BYTES = 163840;
constexpr float THR = 8.f;
constexpr float QSCALE = 0.08838834764831845f * 1.4426950408889634f;
DI const LAS char* lp(unsigned a) { return (const LAS char*)(size_t)a; }
DI float max3f(float a, float b, float c) { float r; asm("v_max3_f32 %0, %1, %2, %3" : "=v"(r) : "v"(a), "v"(b), "v"(c)); return r; }
DI unsigned opaque_lane() { unsigned l; asm volatile("v_mbcnt_lo_u32_b32 %0, -1, 0\n\tv_mbcnt_hi_u32_b32 %0, -1, %0" : "=v"(l)); return l; }

constexpr int QLA = 3, QBB = 2;
constexpr int qb(int c) { return 16 * c - QBB; }
constexpr int qfirst(int c) { return c == 2 ? 0 : c == 3 ? 6 : qb(c - 3); }
constexpr int qop_chunk(int g) { for (int c = 2; c < 9; ++c) { const int j = g - qfirst(c); if (j >= 0 && j < 6) return c * 8 + j; } return -1; }
constexpr int qkv_at(int g) { return (g >= 96 && g < 112 && ((g - 96) & 3) == 0) ? (g - 96) >> 2 : -1; }
constexpr int qops(int g) { return (qop_chunk(g) >= 0) + (qkv_at(g) >= 0); }
constexpr int qwait(int c) {
  if (c == 0) return 6;
  int n = 0; const int from = c == 1 ? 0 : qfirst(c) + 6;
  for (int g = from; g < qb(c); ++g) n += qops(g);
  return n;
}
struct St {
  bf16x8 xf[36], wf[144];
  bf16x8 qf[8], fr[35]; f32x16 o[4], sc[2][2], negm, oo[2]; u32x4 pf[4], out[4]; f32x4 p[8]; f32x2 st; f32x4 la; bf16x8 sel;
  float m, mo, l, ls0, ls1, mx0, mx1, f, ca, cb; bool pend;
  unsigned w, vl16, wq_rd, vW, sX, sW, qw, kb, va, vb, sKV, n0, sC0;
  srd_t rW, rX, rK, rV, rC, rWm; unsigned vWm, sWm, dummy;
  unsigned tsv, nts;
};
struct Dm { unsigned m0k, m0v, kso, vso; };
DI unsigned tile_off(const St& s, unsigned c) { return s.sKV + (c < s.n0 ? c : c - s.n0) * 32768u; }
DI Dm mkdm(const St& s, int c) {
  const unsigned par = (unsigned)(c & 1) << 15;
  return Dm{KR + par + s.w * 4096u, VR + (par ^ 32768u) + s.w * 4096u, tile_off(s, c + 2), tile_off(s, c + 1)};
}
template <int J> DI void kvdma(const St& s, const Dm& d) {
  asm volatile("s_mov_b32 m0, %0\n\ts_nop 0\n\tbuffer_load_dwordx4 %1, %2, %3 offen offset:%4 lds" ::"s"(J < 4 ? d.m0k : d.m0v), "v"(s.vl16), "s"(J < 4 ? s.rK : s.rV), "s"(J < 4 ? d.kso : d.vso), "i"((J & 3) * 1024) : "memory");
}
template <int C, int J> DI void qload(St& s) {
  if constexpr (J < 2) dma16(QR + (C & 3) * 16384u + s.w * 2048u + J * 1024u, s.vW, s.rW, s.sW + (C * 64u + J * 4u) * 8192u);
  else asm volatile("buffer_load_dwordx4 %0, %1, %2, %3 offen offset:%4" : "=v"(s.xf[4 * C + J - 2]) : "v"(s.vl16), "s"(s.rX), "s"(s.sX + C * 4096u), "i"((J - 2) * 1024) : "memory");
}
template <int C> DI void qbar(St& s) {
  asm volatile("s_waitcnt vmcnt(%4) lgkmcnt(0)\n\ts_barrier" : "+v"(s.xf[4 * C]), "+v"(s.xf[4 * C + 1]), "+v"(s.xf[4 * C + 2]), "+v"(s.xf[4 * C + 3]) : "i"(qwait(C)) : "memory");
}
constexpr int q_t(int g) { return g < 128 ? (g & 3) : ((g - 128) >> 2); }
constexpr int q_s(int g) { return g < 128 ? ((g >> 2) & 3) : (g & 3); }
template <int G> DI void qread(St& s) {
  if constexpr (G < 144) { constexpr int off = ((G >> 4) & 3) * 16384 + q_s(G) * 4096 + q_t(G) * 256; s.wf[G] = cat8(ldstr(lp(s.wq_rd) + off), ldstr(lp(s.wq_rd) + off + 1024)); }
}
template <int F> DI void qpack(St& s) {
  constexpr int T = F >> 1, o8 = (F & 1) * 8;
  const u32x4 v = {cvtpk(s.o[T][o8] * QSCALE, s.o[T][o8 + 1] * QSCALE), cvtpk(s.o[T][o8 + 2] * QSCALE, s.o[T][o8 + 3] * QSCALE),
                   cvtpk(s.o[T][o8 + 4] * QSCALE, s.o[T][o8 + 5] * QSCALE), cvtpk(s.o[T][o8 + 6] * QSCALE, s.o[T][o8 + 7] * QSCALE)};
  s.qf[F] = __builtin_bit_cast(bf16x8, v);
  *(LAS bf16x8*)((LAS char*)(size_t)s.qw + F * 1024) = s.qf[F];
}
template <int G> DI void qslot(St& s) {
  constexpr int cn = (G >> 4) + 1;
  if constexpr (cn < 9 && G == qb(cn)) { qbar<cn>(s); SBAR(); if constexpr (cn == 3 || cn == 6) TSTAMP(s); qread<G + 2>(s); qread<G + 3>(s); }
  else if constexpr (!(cn < 9 && G + QLA >= 16 * cn && G < qb(cn))) qread<G + QLA>(s);
  if constexpr (qop_chunk(G) >= 0) qload<(qop_chunk(G) >> 3), (qop_chunk(G) & 7)>(s);
  if constexpr (qkv_at(G) >= 0) kvdma<qkv_at(G)>(s, mkdm(s, -2));
  constexpr int ks = 4 * (G >> 4) + q_s(G), t = q_t(G);
  const f32x16 z = {};
  s.o[t] = MFMA32(s.wf[G], s.xf[ks], ks == 0 ? z : s.o[t]);
  if constexpr (G >= 134 && ((G - 134) & 1) == 0) qpack<((G - 134) >> 1)>(s);
  SBAR();
}
template <int... Gs> DI void qrun(St& s, iseq<Gs...>) { (qslot<Gs>(s), ...); }
template <int C, int... Js> DI void qload_all(St& s, iseq<Js...>) { (qload<C, Js>(s), ...); }

template <int I> DI void frag(St& s) {
  if constexpr (I < 16) s.fr[I] = lds128(lp(s.kb) + (I & 1) * 8192 + (I >> 1) * 1024);
  else if constexpr (I < 32) s.fr[I] = lds128(lp((I & 2) ? s.vb : s.va) + (I - 16) * 1024);
  else s.fr[I] = lds128(lp(s.kb) + ((I - 32) & 1) * 8192 + ((I - 32) >> 1) * 1024);
}
template <int X> DI void mask_diag(St& s) {
  const unsigned ln = opaque_lane();
  const int dq = 32 * ((int)(s.w & 3) - 2 * (int)(s.w >> 2)) + (int)(ln & 31) - 4 * (int)(ln >> 5);
#pragma unroll
  for (int kb = 0; kb < 2; ++kb)
#pragma unroll
    for (int r = 0; r < 16; ++r) { const int c = 32 * kb + (r & 3) + 8 * (r >> 2); s.sc[X][kb][r] = c > dq ? -__builtin_inff() : s.sc[X][kb][r]; }
}
DI void sub_ip(f32x16& v, float d) {
#pragma unroll
  for (int r = 0; r < 16; ++r) { float e = v[r]; asm("v_sub_f32_e32 %0, %0, %1" : "+v"(e) : "v"(d)); v[r] = e; }
}
DI void mul_ip(f32x16& v, float d) {
#pragma unroll
  for (int r = 0; r < 16; ++r) { float e = v[r]; asm("v_mul_f32_e32 %0, %0, %1" : "+v"(e) : "v"(d)); v[r] = e; }
}
template <int X, int E> DI void sexp(St& s) { float e = __builtin_amdgcn_exp2f(s.sc[X][E >> 4][E & 15]); asm volatile("" : "+v"(e)); s.sc[X][E >> 4][E & 15] = e; }
template <int X, int E> DI void sadd(St& s) {
  float& ls = X ? s.ls1 : s.ls0; if constexpr (E == 0) ls = s.sc[X][0][0]; else { ls += s.sc[X][E >> 4][E & 15]; asm volatile("" : "+v"(ls)); } }
template <int X, int P> DI void scvt(St& s) { unsigned e = cvtpk(s.sc[X][P >> 3][(2 * P) & 15], s.sc[X][P >> 3][(2 * P + 1) & 15]); asm volatile("" : "+v"(e)); s.pf[P >> 2][P & 3] = e; }
DI void mg_stats(St& s, unsigned area, float m) {
  s.l = swap32_add(s.l);
  asm volatile("" : "+v"(m)); *(LAS f32x2*)(size_t)(area + s.w * 256u + (opaque_lane() & 31) * 8u) = f32x2{m, s.l}; }
template <int I> DI void mg_write(St& s, unsigned a) { const f32x16& v = s.o[2 + (I >> 2)]; *(LAS f32x4*)((LAS char*)(size_t)a + I * 1024) = f32x4{v[4 * (I & 3)], v[4 * (I & 3) + 1], v[4 * (I & 3) + 2], v[4 * (I & 3) + 3]}; }
DI void mg_read(St& s, unsigned a, unsigned area) {
  s.st = *(const LAS f32x2*)(size_t)(area + (s.w ^ 4u) * 256u + (opaque_lane() & 31) * 8u);
#pragma unroll
  for (int i = 0; i < 8; ++i) s.p[i] = *(const LAS f32x4*)(lp(a) + i * 1024);
}
DI void mg_coef(St& s, float m) {
  const float mm = __builtin_fmaxf(m, s.st[0]), fa = __builtin_amdgcn_exp2f(m - mm), fb = __builtin_amdgcn_exp2f(s.st[0] - mm);
  const float il = __builtin_amdgcn_rcpf(s.l * fa + s.st[1] * fb); s.ca = fa * il; s.cb = fb * il;
}
template <int I> DI void mg_final(St& s) {
  float v[8];
#pragma unroll
  for (int e = 0; e < 8; ++e) v[e] = s.oo[I >> 1][8 * (I & 1) + e] * s.ca + s.p[2 * I + (e >> 2)][e & 3] * s.cb;
  s.out[I] = u32x4{cvtpk(v[0], v[1]), cvtpk(v[2], v[3]), cvtpk(v[4], v[5]), cvtpk(v[6], v[7])};
  asm volatile("" : "+v"(s.out[I]));
}
template <int I> DI void mg_store(St& s, unsigned blk) {
  const unsigned ln = opaque_lane();
  st128m<ST2>(s.out[I], s.rC, ((ln >> 4) & 1) * 131072u + (ln & 15) * 16u + (ln >> 5) * 256u, s.sC0 + blk * 1048576u + I * 512u);
}
template <int X, int KIND, bool DIAG, int G> DI void aslot(St& s, int c) {
  constexpr int Y = X ^ 1; constexpr bool PREV = KIND != 0, FRESH = KIND == 0 || KIND == 2;
  if constexpr (G == 30) {
    wait_bar<KIND == 3 ? 5 : 1>();
    TSTAMP(s);
    s.kb ^= 32768u; s.va ^= 32768u; s.vb ^= 32768u; asm volatile("" : "+v"(s.kb), "+v"(s.va), "+v"(s.vb)); SBAR();
    frag<32>(s); frag<33>(s);
  } else if constexpr (G + 3 < 16) frag<G + 3>(s);
  else if constexpr (G + 3 < 32) { if constexpr (PREV) frag<G + 3>(s); }
  else if constexpr (G == 31) frag<34>(s);
  const f32x16 z = {};
  if constexpr (G < 16) s.sc[X][G & 1] = MFMA32(s.fr[G], s.qf[G >> 1], G < 2 ? (FRESH ? z : s.negm) : s.sc[X][G & 1]);
  else if constexpr (PREV) s.o[G & 3] = MFMA32(s.fr[G], __builtin_bit_cast(bf16x8, s.pf[(G - 16) >> 2]), (KIND == 3 && G < 20) ? z : s.o[G & 3]);
  if constexpr (KIND == 0) {
    if constexpr (G == 1) asm volatile("s_waitcnt lgkmcnt(0)\n\ts_barrier" ::: "memory");
    if constexpr (G >= 1 && G < 5) kvdma<G - 1>(s, mkdm(s, -1));
  } else if constexpr (KIND != 3) {
    if constexpr (G < 12 && (G & 1)) kvdma<(G >> 1) + 2>(s, mkdm(s, c - 1));
  }
  if constexpr (KIND == 3) {
    const Dm d = mkdm(s, c - 1);
    if constexpr (G == 0) { mg_stats(s, SP, s.mo); s.qw = (s.w < 4u ? d.m0k : d.m0v - 32768u) + s.w * 4096u + s.vl16; asm volatile("" : "+v"(s.qw)); }
    if constexpr (G >= 2 && G < 10) mg_write<G - 2>(s, s.qw);
    if constexpr (G == 10) {
      asm volatile("s_waitcnt lgkmcnt(0)\n\ts_barrier" ::: "memory");
      unsigned a = (s.w < 4u ? d.m0v : d.m0k - 16384u) + (s.w & 3) * 4096u + s.vl16; asm volatile("" : "+v"(a));
      mg_read(s, a, SP); s.oo[0] = s.o[0]; s.oo[1] = s.o[1];
    }
    if constexpr (G == 12) mg_coef(s, s.mo);
    if constexpr (G == 13) asm volatile("s_waitcnt lgkmcnt(0)\n\ts_barrier" ::: "memory");
    if constexpr (G >= 13 && G < 21) kvdma<G - 13>(s, d);
    if constexpr (G >= 13 && G < 25 && (G - 13) % 3 == 0) mg_final<(G - 13) / 3>(s);
    if constexpr (G >= 24 && G < 28) mg_store<G - 24>(s, s.n0 - 1u);
  }
  if constexpr (G == 28)
    asm volatile("buffer_load_dword %0, %1, %2, %3 offen" : "+v"(s.dummy) : "v"(s.vWm), "s"(s.rWm), "s"(s.sWm + c * 640u) : "memory");
  if constexpr (PREV) {
    if constexpr (G < 26) sexp<Y, G + 6>(s);
    if constexpr (G < 27) sadd<Y, G + 5>(s);
    if constexpr (G < 27 && !(G & 1)) scvt<Y, (G >> 1) + 2>(s);
    if constexpr (G == 27) { const float ls = Y ? s.ls1 : s.ls0; if constexpr (KIND == 3) s.l = ls; else s.l += ls; asm volatile("" : "+v"(s.l)); }
  }
  if constexpr (KIND == 0 && G == 16) asm volatile("s_nop 7\n\ts_nop 7" : "+v"(s.sc[X][0]), "+v"(s.sc[X][1]));
  if constexpr (DIAG && G == 16) {
    mask_diag<X>(s);
    unsigned qa = SP + (s.w & 3) * 8192u + s.vl16; asm volatile("" : "+v"(qa));
#pragma unroll
    for (int i = 0; i < 8; ++i) s.qf[i] = lds128(lp(qa) + i * 1024);
  }
  if constexpr (G >= 17 && G < 25) {
    constexpr int i = G - 17, kb = i >> 2, r = 4 * (i & 3);
    if constexpr (i == 0) { s.mx0 = max3f(s.sc[X][0][0], s.sc[X][0][1], s.sc[X][0][1]); s.mx1 = max3f(s.sc[X][0][2], s.sc[X][0][3], s.sc[X][0][3]); }
    else { s.mx0 = max3f(s.mx0, s.sc[X][kb][r], s.sc[X][kb][r + 1]); s.mx1 = max3f(s.mx1, s.sc[X][kb][r + 2], s.sc[X][kb][r + 3]); }
    asm volatile("" : "+v"(s.mx0), "+v"(s.mx1));
  }
  if constexpr (G == 25) {
    const float mx = max3f(s.mx0, s.mx1, s.mx1);
    if constexpr (FRESH) {
      const float rm = __builtin_fmaxf(swap32_max(mx), -1e30f);
      if constexpr (KIND == 2) s.mo = s.m; else s.l = 0.f;
      s.m = rm; sub_ip(s.sc[X][0], rm); sub_ip(s.sc[X][1], rm);
#pragma unroll
      for (int r = 0; r < 16; ++r) s.negm[r] = -rm;
    } else if (__builtin_expect(__any(mx > THR), 0)) {
      const float dl = __builtin_fmaxf(swap32_max(mx), 0.f);
      s.m += dl; s.f = __builtin_amdgcn_exp2f(-dl); s.pend = true;
      sub_ip(s.sc[X][0], dl); sub_ip(s.sc[X][1], dl);
#pragma unroll
      for (int r = 0; r < 16; ++r) { float e = s.negm[r]; asm("v_sub_f32_e32 %0, 0, %1" : "+v"(e) : "v"(s.m)); s.negm[r] = e; }
    }
  }
  if constexpr (G >= 26) sexp<X, G - 26>(s);
  if constexpr (G >= 27) sadd<X, G - 27>(s);
  if constexpr (G == 28 || G == 30) scvt<X, ((G - 28) >> 1)>(s);
  if constexpr (KIND != 2 && G >= 30) kvdma<G - 30>(s, mkdm(s, c));
  SBAR();
}
template <int X, int KIND, bool DIAG, int... Gs> DI void astep_(St& s, int c, iseq<Gs...>) {
  if constexpr (KIND == 0) { frag<32>(s); frag<33>(s); frag<34>(s); SBAR(); }
  s.fr[0] = s.fr[32]; s.fr[1] = s.fr[33]; s.fr[2] = s.fr[34];
  (aslot<X, KIND, DIAG, Gs>(s, c), ...);
  if constexpr (KIND == 1 || KIND == 3) {
    if (__builtin_expect(s.pend, 0)) {
      asm volatile("s_nop 7");
      mul_ip(s.o[0], s.f); mul_ip(s.o[1], s.f); mul_ip(s.o[2], s.f); mul_ip(s.o[3], s.f);
      s.l *= s.f;
      s.pend = false;
    }
  }
}
template <int X, int KIND, bool DIAG = false> DI void astep(St& s, int c) { astep_<X, KIND, DIAG>(s, c, mkseq<32>{}); }
template <int T> DI void tslot(St& s) {
  if constexpr (T == 0) { frag<16>(s); frag<17>(s); frag<18>(s); }
  else if constexpr (T + 17 < 32 && T >= 2) frag<T + 17>(s);
  if constexpr (T >= 2) s.o[(T - 2) & 3] = MFMA32(s.fr[T + 14], __builtin_bit_cast(bf16x8, s.pf[(T - 2) >> 2]), s.o[(T - 2) & 3]);
  if constexpr (T < 13) { sexp<1, 6 + 2 * T>(s); sexp<1, 7 + 2 * T>(s); }
  if constexpr (T > 0 && T < 14) sadd<1, 4 + 2 * T>(s);
  if constexpr (T < 14) { sadd<1, 5 + 2 * T>(s); scvt<1, T + 2>(s); }
  if constexpr (T == 14) { s.l += s.ls1; mg_stats(s, VR + 32768u, s.m); }
  SBAR();
}
template <int... Ts> DI void atail(St& s, iseq<Ts...>) { (tslot<Ts>(s), ...); }
template <int... Is> DI void mg_write_all(St& s, unsigned a, iseq<Is...>) { (mg_write<Is>(s, a), ...); }
}

__global__ __launch_bounds__(512, 2) void k2_attn(const u16* __restrict__ wq, const u16* __restrict__ wo, const u16* __restrict__ wg, const u16* __restrict__ wu, const u16* __restrict__ wd, char* __restrict__ ws) {
  using namespace k2;
  const unsigned tid = threadIdx.x, lane = tid & 63, w = rfl(tid >> 6), hi = lane >> 5, rq = w & 3, g = w >> 2;
  const unsigned b = blockIdx.x, xcd = b & 7, ix = b >> 3, h = xcd * 4 + (ix & 3), x = ix >> 2;
  St s;
  s.w = w; s.vl16 = lane * 16u;
  TSTAMP(s);
  const unsigned q2 = (lane >> 2) & 3, p2 = lane & 3, g4 = lane >> 4;
  s.rW = mksrd(wq, DM * HDM * 2); s.rX = mksrd(ws + WS_XN, SEQ * DM * 2); s.rK = mksrd(ws + WS_KF, SEQ * HDM * 2); s.rV = mksrd(ws + WS_VF, SEQ * HDM * 2);
  s.rC = mksrd(ws + WS_CTX, SEQ * HDM * 2);
  s.vW = q2 * 8192u + g4 * 64u + p2 * 16u; s.sW = h * 256u + w * 65536u;
  s.wq_rd = QR + hi * 2048u + q2 * 64u + (g4 & 1) * 32u + p2 * 8u; asm volatile("" : "+v"(s.wq_rd));
  s.sX = ((g == 0 ? x : 15u - x) * 4u + rq) * 36864u;
  s.sKV = h * 524288u + w * 4096u; s.n0 = x + 1u; s.sC0 = (2u * rq * 128u + 4u * h + 2u * g) * 1024u;
  s.qw = (g == 0 ? KR + 32768u : SP) + rq * 8192u + s.vl16; asm volatile("" : "+v"(s.qw));
  qload_all<0>(s, mkseq<6>{});
  __builtin_amdgcn_s_barrier();
  qload_all<1>(s, mkseq<6>{});
  qbar<0>(s); SBAR(); TSTAMP(s);
  qread<0>(s); qread<1>(s); qread<2>(s); SBAR();
  qrun(s, mkseq<144>{});
  qpack<5>(s); qpack<6>(s); qpack<7>(s);
  TSTAMP(s);
  wait_bar<0>();
  if (g == 1) {
    unsigned ra = KR + 32768u + rq * 8192u + s.vl16; asm volatile("" : "+v"(ra));
#pragma unroll
    for (int i = 0; i < 8; ++i) s.qf[i] = lds128(lp(ra) + i * 1024);
  }
  { const Dm d = mkdm(s, -1); kvdma<4>(s, d); kvdma<5>(s, d); kvdma<6>(s, d); kvdma<7>(s, d); }
  {
    const unsigned W = b * 8u + w;
    s.rWm = mksrd(W < 434u ? wo : W < 922u ? wg : W < 1410u ? wu : wd, W < 434u ? HDM * DM * 2 : DM * FF * 2);
    s.sWm = (W < 434u ? W : W < 922u ? W - 434u : W < 1410u ? W - 922u : W - 1410u) * 10880u;
    const unsigned l = opaque_lane(); s.vWm = l < 5u ? l * 128u : 0x10000000u;
    s.dummy = 0;
  }
  s.kb = KR + g * 16384u + s.vl16; s.va = VR + 32768u + g * 18432u + s.vl16; s.vb = VR + 32768u + g * 14336u + s.vl16; s.pend = false;
  asm volatile("" : "+v"(s.kb), "+v"(s.va), "+v"(s.vb));
#pragma unroll
  for (int d = 0; d < 4; ++d) s.o[d] = f32x16{};
  { const unsigned l = opaque_lane(), e = (((l >> 2) ^ (l >> 4)) & 1) ? 0u : 0x3f803f80u; s.sel = __builtin_bit_cast(bf16x8, u32x4{e, e, e, e}); s.la = f32x4{}; }
  TSTAMP(s);
  if (x == 0) astep<1, 0, true>(s, 0); else astep<0, 0>(s, 0);
  int c = 1;
  for (int pass = 0; pass < 2; ++pass) {
    if (pass == 1 || x != 0) {
      const int cend = pass == 0 ? (int)x : 16;
      for (; c + 1 < cend; c += 2) { astep<1, 1>(s, c); astep<0, 1>(s, c + 1); }
      if (c < cend) { astep<1, 1>(s, c); ++c; s.sc[0][0] = s.sc[1][0]; s.sc[0][1] = s.sc[1][1]; s.ls0 = s.ls1; }
      astep<1, 1, true>(s, c); ++c;
    }
    if (pass == 0) { astep<0, 2>(s, c); astep<1, 3>(s, c + 1); astep<0, 1>(s, c + 2); c += 3; }
  }
  TSTAMP(s);
  atail(s, mkseq<18>{});
  TSTAMP(s);
  {
    unsigned mine = (g == 0 ? SP : KR + 32768u) + rq * 8192u + s.vl16, theirs = (g == 0 ? KR + 32768u : SP) + rq * 8192u + s.vl16; asm volatile("" : "+v"(mine), "+v"(theirs));
    mg_write_all(s, mine, mkseq<8>{});
    asm volatile("s_waitcnt lgkmcnt(0)\n\ts_barrier" ::: "memory");
    TSTAMP(s);
    mg_read(s, theirs, VR + 32768u); s.oo[0] = s.o[0]; s.oo[1] = s.o[1];
    mg_coef(s, s.m);
    mg_final<0>(s); mg_store<0>(s, 15u - x); mg_final<1>(s); mg_store<1>(s, 15u - x); mg_final<2>(s); mg_store<2>(s, 15u - x); mg_final<3>(s); mg_store<3>(s, 15u - x);
  }
  TSTAMP(s);
  wait_vm<4>();
  asm volatile("" ::"v"(s.dummy));
}

namespace k3 {
#define K3_SOLO 24
constexpr int SOLO = K3_SOLO, NQS = SOLO / 4, P = 3, WSLOT = 4096, RING = 102400, CNT = RING + 4 * P * WSLOT, LDS_BYTES = 163840, GRID = 256;
DI const LAS char* lp(unsigned a) { return (const LAS char*)(size_t)a; }
struct St {
  bf16x8 af[P][5], wf[4]; f32x4 acc[4][5];
  unsigned vl16, vW, rd[4], m0b, sA[5], sW, kA, kB;
  srd_t rA, rW;
  unsigned tsv, nts;
};
constexpr int nops(bool own, int nsh, int i) { return i < nsh ? 7 : (own && i < nsh + NQS ? 9 : 0); }
template <bool OWN, int NSH, int I, int J> DI void load(St& s) {
  constexpr int sl = I % P, nw = nops(OWN, NSH, I) - 5;
  if constexpr (J < nops(OWN, NSH, I)) {
    const unsigned k = (I < NSH ? s.kA : s.kB) + I;
    if constexpr (J < nw) { constexpr unsigned pj = (I < NSH && !OWN) ? J + 2 : J; dma16(s.m0b + sl * WSLOT + pj * 1024u, s.vW, s.rW, s.sW + (k * 32u + pj * 8u) * (DM * 2u)); }
    else asm volatile("buffer_load_dwordx4 %0, %1, %2, %3 offen" : "=v"(s.af[sl][J - nw]) : "v"(s.vl16), "s"(s.rA), "s"(s.sA[J - nw] + k * 1024u) : "memory");
  }
}
template <bool OWN, int NSH, int I, int... Js> DI void load_all(St& s, iseq<Js...>) { (load<OWN, NSH, I, Js>(s), ...); }
template <bool OWN, int NSH, int I> DI void step(St& s) {
  constexpr int sl = I % P;
  if constexpr (I < NSH + 1) asm volatile("s_waitcnt vmcnt(%5)\n\ts_barrier" : "+v"(s.af[sl][0]), "+v"(s.af[sl][1]), "+v"(s.af[sl][2]), "+v"(s.af[sl][3]), "+v"(s.af[sl][4]) : "i"(nops(OWN, NSH, I + 1)) : "memory");
  else asm volatile("s_waitcnt vmcnt(%5)" : "+v"(s.af[sl][0]), "+v"(s.af[sl][1]), "+v"(s.af[sl][2]), "+v"(s.af[sl][3]), "+v"(s.af[sl][4]) : "i"(nops(OWN, NSH, I + 1)) : "memory");
  if constexpr ((I & 3) == 0) TSTAMP(s);
#pragma unroll
  for (int t = 0; t < 4; ++t) s.wf[t] = cat8(ldstr(lp(s.rd[t]) + sl * WSLOT), ldstr(lp(s.rd[t]) + sl * WSLOT + 1024));
  SBAR();
#define K3_MM(t, r) do { if constexpr (I == 0) MFMA16_NEW(s.acc[t][r], s.wf[t], s.af[sl][r]); else MFMA16_ACC(s.acc[t][r], s.wf[t], s.af[sl][r]); } while (0)
#define K3_LD(j) load<OWN, NSH, I + 2, j>(s)
  K3_LD(0); K3_MM(0, 0); K3_MM(0, 1); SBAR(); K3_LD(1); K3_MM(0, 2); K3_MM(0, 3); SBAR();
  K3_LD(2); K3_MM(0, 4); K3_MM(1, 0); SBAR(); K3_LD(3); K3_MM(1, 1); K3_MM(1, 2); SBAR();
  K3_LD(4); K3_MM(1, 3); K3_MM(1, 4); SBAR(); K3_LD(5); K3_MM(2, 0); K3_MM(2, 1); SBAR();
  K3_LD(6); K3_MM(2, 2); K3_MM(2, 3); SBAR(); K3_LD(7); K3_MM(2, 4); K3_MM(3, 0); SBAR();
  K3_LD(8); K3_MM(3, 1); K3_MM(3, 2); K3_MM(3, 3); K3_MM(3, 4); SBAR();
#undef K3_MM
#undef K3_LD
}
template <bool OWN, int NSH, int... Is> DI void run(St& s, iseq<Is...>) {
  load_all<OWN, NSH, 0>(s, mkseq<7>{}); __builtin_amdgcn_s_barrier(); load_all<OWN, NSH, 1>(s, mkseq<7>{});
  (step<OWN, NSH, Is>(s), ...);
  asm volatile("s_nop 7" ::: "memory"); SBAR();
}
DI void park(const St& s, unsigned kq, unsigned lane) {
#pragma unroll
  for (int t = 0; t < 4; ++t) {
    unsigned wa = kq * 20480u + (lane & 15) * 256u + (((4u * t + (lane >> 4)) ^ (lane & 15)) << 4); asm volatile("" : "+v"(wa));
#pragma unroll
    for (int r = 0; r < 5; ++r) *(LAS f32x4*)((LAS char*)(size_t)wa + r * 4096) = s.acc[t][r];
  }
}
DI void repack(const u16* wg, const u16* wu, char* ws, unsigned wgi, unsigned w, unsigned lane) {
  const unsigned task = wgi * 8u + w, blk = task >> 1, half = task & 1;
  const bool on = blk < 72u;
  const srd_t rG = mksrd(wg, DM * FF * 2), rU = mksrd(wu, DM * FF * 2), rP = mksrd(ws + WS_WGU, DM * FF * 4);
  const unsigned base = w * 20480u, row = lane >> 3;
  const unsigned vg = row * (FF * 2u) + (((lane & 7) ^ (((row >> 1) & 1) << 2)) << 4), vu = vg ^ 32u;
  const unsigned ssrc = on ? blk * 128u + half * 288u * (FF * 2u) : 0x10000000u;
  unsigned sdst[4];
#pragma unroll
  for (int p = 0; p < 4; ++p) sdst[p] = (on && (blk * 4u + p) % 9u >= 4u) ? (blk * 4u + p) * 36864u + half * 18432u : 0x10000000u;
  const unsigned hi = lane >> 5, mat = (lane >> 4) & 1, q = (lane >> 2) & 3;
  const unsigned rd = base + mat * 2048u + (8u * hi + q) * 128u + (((q >> 1) << 6) ^ (mat << 5)) + (lane & 3) * 8u;
  const bf16x8 ones = {0x3f80, 0x3f80, 0x3f80, 0x3f80, 0x3f80, 0x3f80, 0x3f80, 0x3f80};
  f32x16 cs[4] = {};
  auto fetch = [&](unsigned st, unsigned slot) {
#pragma unroll
    for (int j = 0; j < 4; ++j) dma16(base + slot * 4096u + j * 1024u, j < 2 ? vg : vu, j < 2 ? rG : rU, ssrc + (st * 16u + (j & 1) * 8u) * (FF * 2u));
  };
#pragma unroll
  for (int i = 0; i < 4; ++i) fetch(i, i);
  for (unsigned st = 0; st < 18u; ++st) {
    const unsigned slot = st % 5u;
    if (st < 4u) wait_vm<12>(); else wait_vm<24>();
    bf16x8 f[4];
#pragma unroll
    for (int p = 0; p < 4; ++p) { const unsigned a = (rd + slot * 4096u) ^ (p << 5); f[p] = cat8(ldstr(lp(a)), ldstr(lp(a) + 512)); }
    fetch(st + 4u < 18u ? st + 4u : st, (st + 4u) % 5u);
#pragma unroll
    for (int p = 0; p < 4; ++p) { cs[p] = MFMA32(f[p], ones, cs[p]); st128m<0>(__builtin_bit_cast(u32x4, f[p]), rP, lane * 16u, sdst[p] + st * 1024u); }
  }
  wait_vm<0>();
  if ((lane & 31) == 0) {
#pragma unroll
    for (int p = 0; p < 4; ++p)
#pragma unroll
      for (int r4 = 0; r4 < 4; ++r4) *(LAS f32x4*)(size_t)(base + p * 128u + r4 * 32u + hi * 16u) = f32x4{cs[p][4 * r4], cs[p][4 * r4 + 1], cs[p][4 * r4 + 2], cs[p][4 * r4 + 3]};
  }
  asm volatile("s_waitcnt lgkmcnt(0)\n\ts_barrier" ::: "memory");
  if (on && half == 0) {
    const f32x2 a = *(const LAS f32x2*)(size_t)(base + lane * 8u), c = *(const LAS f32x2*)(size_t)(base + 20480u + lane * 8u);
    *(f32x2*)(ws + WS_CS + blk * 512u + lane * 8u) = a + c;
  }
}
template <int NK32, bool FIRST> DI void body(const char* A, const u16* W, const u16* x, const u16* wg, const u16* wu, char* ws, u16* out) {
  constexpr int H = (NK32 - SOLO) / 2, NSH = H / 4;
  static_assert(H % 4 == 0, "quarters");
  const unsigned tid = threadIdx.x, lane = tid & 63, w = rfl(tid >> 6), g = lane >> 4, q2 = (lane >> 2) & 3, p2 = lane & 3, kq = w & 3, rh = w >> 2;
  const unsigned b = blockIdx.x, side = b & 1, id = ((b & 7) >> 1) * 30u + (b >> 3);
  if (b >= 240u || id >= 117u) {
    if constexpr (FIRST) repack(wg, wu, ws, b >= 240u ? b - 234u : (id - 117u) * 2u + side, w, lane);
    return;
  }
  const unsigned t160 = id / 9u, cb = id - t160 * 9u, rbk = t160 * 2u + side;
  St s;
  TSTAMP(s);
  s.rA = mksrd(A, SEQ * NK32 * 64); s.rW = mksrd(W, NK32 * 32 * DM * 2);
  s.vl16 = lane * 16u;
  s.vW = (lane >> 3) * (DM * 2u) + (((lane & 7) ^ (((lane >> 4) & 3) << 1)) << 4);
  s.m0b = RING + kq * (P * WSLOT); s.sW = cb * 128u;
  s.kA = side * (NK32 - H) + kq * NSH; s.kB = H + kq * NQS - NSH;
#pragma unroll
  for (int r = 0; r < 5; ++r) s.sA[r] = ((rbk ^ rh) * 5u + r) * (NK32 * 1024u);
  const unsigned krow = 16u * (g >> 1) + 4u * (g & 1) + q2;
#pragma unroll
  for (int t = 0; t < 4; ++t) { s.rd[t] = s.m0b + krow * 128u + ((((krow >> 1) & 3) ^ t) << 5) + p2 * 8u; asm volatile("" : "+v"(s.rd[t])); }
  if (tid < 2u) *(LAS unsigned*)(size_t)(CNT + tid * 4u) = 0u;
  const unsigned pc = tid & 7, m0 = tid >> 3;
  unsigned vrm[2], vfr[2];
#pragma unroll
  for (int i = 0; i < 2; ++i) {
    const unsigned m = m0 + 64u * i, row = rbk * 80u + m; const bool ok = m < 80u && row < (unsigned)SEQ;
    vrm[i] = ok ? row * (DM * 2u) + cb * 128u + pc * 16u : 0x10000000u;
    vfr[i] = ok ? (row >> 5) * 36864u + (cb * 8u + pc) * 512u + (row & 31) * 16u : 0x10000000u;
  }
  u32x4 res[2];
  {
    const srd_t rR = FIRST ? mksrd(x, SEQ * DM * 2) : mksrd(ws + WS_H, SEQ * DM * 2);
    asm volatile("buffer_load_dwordx4 %0, %2, %4, 0 offen\n\tbuffer_load_dwordx4 %1, %3, %4, 0 offen"
                 : "=&v"(res[0]), "=&v"(res[1]) : "v"(FIRST ? vrm[0] : vfr[0]), "v"(FIRST ? vrm[1] : vfr[1]), "s"(rR) : "memory");
  }
  asm volatile("s_waitcnt lgkmcnt(0)" ::: "memory");
  if (rh == 0) {
    run<true, NSH>(s, mkseq<NSH + NQS>{});
    TSTAMP(s);
    while (rfl(__hip_atomic_load((LAS unsigned*)(size_t)CNT, __ATOMIC_RELAXED, __HIP_MEMORY_SCOPE_WORKGROUP)) < 4u) __builtin_amdgcn_s_sleep(1);
    asm volatile("" ::: "memory");
    park(s, kq, lane);
  } else {
    run<false, NSH>(s, mkseq<NSH>{});
    asm volatile("s_barrier" ::: "memory");
    park(s, kq, lane);
    asm volatile("s_waitcnt lgkmcnt(0)" ::: "memory");
    if (lane == 0) __hip_atomic_fetch_add((LAS unsigned*)(size_t)(CNT + 4u), 1u, __ATOMIC_RELAXED, __HIP_MEMORY_SCOPE_WORKGROUP);
    asm volatile("s_waitcnt lgkmcnt(0)" ::: "memory");
    while (rfl(__hip_atomic_load((LAS unsigned*)(size_t)(CNT + 4u), __ATOMIC_RELAXED, __HIP_MEMORY_SCOPE_WORKGROUP)) < 4u) __builtin_amdgcn_s_sleep(1);
    asm volatile("" ::: "memory");
    const srd_t rX = mksrd(ws + (FIRST ? WS_XB : WS_XB + 234u * 20480u), 234u * 20480u);
    unsigned* flags = (unsigned*)(ws + WS_FLAG) + (FIRST ? 0 : 8192);
    unsigned ca = (tid & 255u) * 16u; asm volatile("" : "+v"(ca));
#pragma unroll
    for (int i = 0; i < 5; ++i) {
      const f32x4 v = (*(const LAS f32x4*)(lp(ca) + i * 4096) + *(const LAS f32x4*)(lp(ca) + 20480 + i * 4096)) + (*(const LAS f32x4*)(lp(ca) + 40960 + i * 4096) + *(const LAS f32x4*)(lp(ca + 61440u) + i * 4096));
      st128m<1>(__builtin_bit_cast(u32x4, v), rX, ca, (id * 2u + side) * 20480u + i * 4096u);
    }
    asm volatile("s_waitcnt vmcnt(0)" ::: "memory");
    unsigned last = 0;
    if (lane == 0) last = __hip_atomic_fetch_add((LAS unsigned*)(size_t)CNT, 1u, __ATOMIC_RELAXED, __HIP_MEMORY_SCOPE_WORKGROUP);
    if (lane == 0 && last == 3u) __hip_atomic_store(flags + (id * 2u + side) * 32u, 1u, __ATOMIC_RELAXED, __HIP_MEMORY_SCOPE_AGENT);
    TSTAMP(s);
    for (unsigned spin = 0; spin < (1u << 22); ++spin) {
      if (rfl(__hip_atomic_load(flags + (id * 2u + (side ^ 1u)) * 32u, __ATOMIC_RELAXED, __HIP_MEMORY_SCOPE_AGENT)) != 0u) break;
      __builtin_amdgcn_s_sleep(2);
    }
    u32x4 pv[5];
    asm volatile("buffer_load_dwordx4 %0, %5, %6, %7 offen sc1\n\tbuffer_load_dwordx4 %1, %5, %6, %8 offen sc1\n\tbuffer_load_dwordx4 %2, %5, %6, %9 offen sc1\n\t"
                 "buffer_load_dwordx4 %3, %5, %6, %10 offen sc1\n\tbuffer_load_dwordx4 %4, %5, %6, %11 offen sc1\n\ts_waitcnt vmcnt(0)"
                 : "=&v"(pv[0]), "=&v"(pv[1]), "=&v"(pv[2]), "=&v"(pv[3]), "=&v"(pv[4])
                 : "v"(ca), "s"(rX), "s"((id * 2u + (side ^ 1u)) * 20480u), "s"((id * 2u + (side ^ 1u)) * 20480u + 4096u), "s"((id * 2u + (side ^ 1u)) * 20480u + 8192u),
                   "s"((id * 2u + (side ^ 1u)) * 20480u + 12288u), "s"((id * 2u + (side ^ 1u)) * 20480u + 16384u) : "memory");
#pragma unroll
    for (int i = 0; i < 5; ++i) *(LAS u32x4*)((LAS char*)(size_t)(ca + 81920u) + i * 4096) = pv[i];
    TSTAMP(s);
  }
  asm volatile("s_waitcnt lgkmcnt(0)\n\ts_barrier" : "+v"(res[0]), "+v"(res[1])::"memory");
  TSTAMP(s);
  const srd_t rO = FIRST ? mksrd(ws + WS_H, SEQ * DM * 2) : mksrd(out, SEQ * DM * 2);
#pragma unroll
  for (int i = 0; i < 2; ++i) {
    if (i == 1 && w >= 2) break;
    unsigned ra = (m0 + 64u * i) * 256u + (((2u * pc) ^ (m0 & 15)) << 4), rb = ra + 61440u; asm volatile("" : "+v"(ra), "+v"(rb));
    f32x4 a = *(const LAS f32x4*)(lp(ra)), c = *(const LAS f32x4*)(lp(ra ^ 16u));
#pragma unroll
    for (int ww = 1; ww < 5; ++ww) { const unsigned base = ww < 3 ? ra : rb; a += *(const LAS f32x4*)(lp(base) + (ww % 3) * 20480); c += *(const LAS f32x4*)(lp(base ^ 16u) + (ww % 3) * 20480); }
    const unsigned y[4] = {cvtpk(a[0], a[1]), cvtpk(a[2], a[3]), cvtpk(c[0], c[1]), cvtpk(c[2], c[3])};
    u32x4 o;
#pragma unroll
    for (int e = 0; e < 4; ++e) o[e] = cvtpk(bflo(res[i][e]) + bflo(y[e]), bfhi(res[i][e]) + bfhi(y[e]));
    st128m<FIRST ? ST3 : ST5>(o, rO, FIRST ? vfr[i] : vrm[i], 0);
    if constexpr (FIRST) {
      float sm = 0.f;
#pragma unroll
      for (int e = 0; e < 4; ++e) sm += bflo(o[e]) + bfhi(o[e]);
      const float mean = dpp_add<0x141>(dpp_add<0x4E>(dpp_add<0xB1>(sm))) * (1.f / 64.f);
      float q = 0.f;
#pragma unroll
      for (int e = 0; e < 4; ++e) { const float d0 = bflo(o[e]) - mean, d1 = bfhi(o[e]) - mean; q += d0 * d0 + d1 * d1; }
      q = dpp_add<0x141>(dpp_add<0x4E>(dpp_add<0xB1>(q)));
      const unsigned m = m0 + 64u * i, row = rbk * 80u + m;
      if (pc == 0 && m < 80u && row < (unsigned)SEQ) *(f32x2*)(ws + WS_ST + (row * 9u + cb) * 8u) = f32x2{mean, q};
    }
  }
}
}
__global__ __launch_bounds__(512, 2) void k3_oproj(const u16* __restrict__ wo, const u16* __restrict__ x, const u16* __restrict__ wg, const u16* __restrict__ wu, char* __restrict__ ws) { k3::body<128, true>(ws + WS_CTX, wo, x, wg, wu, ws, nullptr); }
__global__ __launch_bounds__(512, 2) void k5_down(const u16* __restrict__ wd, char* __restrict__ ws, u16* __restrict__ out) { k3::body<144, false>(ws + WS_ACT, wd, nullptr, nullptr, nullptr, ws, out); }

namespace k4 {
constexpr int SLOT = 36864, CSOFF = 4 * SLOT, LDS_BYTES = CSOFF + 2048, LA = 3, BB = 2, NU = 324;
DI const LAS char* lp(unsigned a) { return (const LAS char*)(size_t)a; }
constexpr int u_pair(int g) { return g < 112 ? g & 3 : g < 144 ? (g - 112) >> 3 : 4 + (g - 144) / 36; }
constexpr int u_kstep(int g) { return g < 112 ? g >> 2 : g < 144 ? 28 + (g & 7) : (g - 144) % 36; }
constexpr int pair_end(int p) { return p < 4 ? 119 + 8 * p : 144 + 36 * (p - 4) + 35; }
constexpr int NE = 13;
constexpr int eks(int e) { return e < 8 ? 2 * e : 18; }
constexpr int estart(int e) { return e < 8 ? 16 * e : 144 + 36 * (e - 8); }
constexpr int bslot(int e) { return estart(e) - BB; }
constexpr int epoch_of(int g) { int e = 0; while (e + 1 < NE && estart(e + 1) <= g) ++e; return e; }
#define K4_NPRO 12
#define K4_RATE 2
constexpr int NPRO = K4_NPRO;
constexpr int s_lo(int g) { const int n = NPRO + (g + K4_RATE - 1) / K4_RATE; return n < 54 ? n : 54; }
constexpr int SEND = (54 - NPRO) * K4_RATE;
constexpr int cfirst(int p) { return p == 4 ? SEND + 1 : p == 5 ? SEND + 16 : p == 6 ? bslot(7) + 3 : p == 7 ? bslot(8) + 1 : bslot(9) + 1; }
static_assert(cfirst(4) > bslot(3) && cfirst(5) > bslot(5), "slot reuse");
constexpr int op_at(int g) {
  for (int p = 4; p < 9; ++p) { const int j = g - cfirst(p); if (j >= 0 && j < 15 && j % 3 == 0) return 100 + 8 * (p - 4) + j / 3; }
  if (g == SEND) return 204;
  return -1;
}
constexpr int NEP = 30, SP = 24;
constexpr int ep_start(int p) { int s = 0; for (int q = 0; q <= p; ++q) { const int a = pair_end(q) + 3; s = (q == 0 || a > s + SP) ? a : s + SP; } return s; }
static_assert(ep_start(7) + NEP <= NU, "epilogues must fit");
constexpr int st_ops(int g) { for (int p = 0; p < 8; ++p) if (g == ep_start(p) + NEP - 2) return 1; return 0; }
constexpr bool sure(int op) { return op >= 0 && !(op >= 100 && op < 200 && ((op - 100) & 7) == 4) && op != 204; }
constexpr int vm_wait(int e) {
  const int to = e ? bslot(e) : 0;
  if (e < 7) { int n = s_lo(to) - 3 * eks(e + 1); for (int g = 0; g < to; ++g) n += sure(op_at(g)) ? 1 : 0; return n; }
  const int want = e == 7 ? 204 : 100 + 8 * (e - 8) + 4;
  int from = 0; while (op_at(from) != want) ++from;
  int n = st_ops(from);
  for (int g = from + 1; g < to; ++g) n += (sure(op_at(g)) ? 1 : 0) + st_ops(g);
  return n;
}
constexpr bool sched_ok() { for (int e = 1; e < 7; ++e) if (s_lo(bslot(e)) < 3 * eks(e + 1)) return false; return SEND < bslot(7); }
static_assert(sched_ok(), "stream ops must precede their barriers");
constexpr int ST0 = 2;
DI unsigned olane() { unsigned l; asm volatile("v_mbcnt_lo_u32_b32 %0, -1, 0\n\tv_mbcnt_hi_u32_b32 %0, -1, %0" : "=v"(l)); return l; }
struct St {
  bf16x8 hf[36], wf[NU]; f32x16 acc[9];
  f32x4 sp0, sp1; f32x2 sq, csg, csu, csg2, csu2; float ag, au, x0, x1, t0, t1, d0, o0, o1; u32x4 pk;
  float nmean, c1, c2;
  unsigned w, wp, wp1, wbA, vA, sA, sW, sHh, csb, vl16, vst, sAct, vsp, p0;
  srd_t rR, rW, rH, rS, rCS, rAct;
  unsigned tsv, nts;
};
template <int OP> DI void vop(St& s) {
  if constexpr (OP < 54) {
    constexpr int ks = OP / 3, j = OP % 3;
    if constexpr (j < 2) asm volatile("buffer_load_dwordx4 %0, %1, %2, %3 offen offset:%4" : "=v"(s.hf[2 * ks + j]) : "v"(s.vl16), "s"(s.rH), "s"(s.sHh + (ks >> 1) * 4096u), "i"(((ks & 1) * 2 + j) * 1024) : "memory");
    else dma16(ks * 8192u + s.w * 1024u, s.vA, s.rR, s.sA + ks * 32u * (FF * 2u));
  } else if constexpr (OP < 200) {
    constexpr int P = 4 + ((OP - 100) >> 3), J = (OP - 100) & 7;
    if (J < 4 || s.w < 4u) dma16((P & 3) * SLOT + J * 8192u + s.w * 1024u, s.vl16, s.rW, s.sW + P * 36864u + J * 8192u + s.w * 1024u);
  } else if constexpr (OP == 200) asm volatile("buffer_load_dwordx4 %0, %1, %2, 0 offen" : "=v"(s.sp0) : "v"(s.vsp), "s"(s.rS) : "memory");
  else if constexpr (OP == 201) asm volatile("buffer_load_dwordx4 %0, %1, %2, 0 offen offset:16" : "=v"(s.sp1) : "v"(s.vsp), "s"(s.rS) : "memory");
  else if constexpr (OP == 202) asm volatile("buffer_load_dwordx2 %0, %1, %2, 0 offen offset:64" : "=v"(s.sq) : "v"(s.csb), "s"(s.rS) : "memory");
  else {
    s.csb = CSOFF + (s.vl16 >> 9) * 16u; asm volatile("" : "+v"(s.csb));
    if (s.w < 2u) dma16(CSOFF + s.w * 1024u, s.vl16, s.rCS, s.p0 * 128u + s.w * 1024u);
  }
}
template <int O0, int... Os> DI void vops(St& s, iseq<Os...>) { (vop<O0 + Os>(s), ...); }
template <int G> DI void reads(St& s) {
  if constexpr (G < NU) {
    constexpr int p = u_pair(G), ks = u_kstep(G);
    if constexpr (G < 144) {
      constexpr int off = ((ks >> 1) - eks(epoch_of(G))) * 8192 + (ks & 1) * 2048 + (p >> 1) * 256; const unsigned a = (p & 1) ? s.wp1 : s.wp;
      s.wf[G] = cat8(ldstr(lp(a) + off), ldstr(lp(a) + off + 512));
    } else s.wf[G] = lds128(lp(s.wp) + ks * 1024);
  }
}
template <int K> DI void pin1(St& s) { asm volatile("" : "+v"(s.hf[2 * K]), "+v"(s.hf[2 * K + 1])); }
template <int E, int... Ks> DI void pin_h(St& s, iseq<Ks...>) { (pin1<eks(E) + Ks>(s), ...); }
template <int E> DI void barrier(St& s) {
  wait_bar<vm_wait(E)>();
  if constexpr (E < 8) {
    pin_h<E>(s, mkseq<eks(E + 1) - eks(E)>{});
    unsigned a = s.wbA + eks(E) * 8192u, b = (s.wbA ^ 32u) + eks(E) * 8192u; asm volatile("" : "+v"(a), "+v"(b)); s.wp = a; s.wp1 = b;
  } else { unsigned a = s.vl16 + ((E - 8) & 3) * SLOT; asm volatile("" : "+v"(a)); s.wp = a; }
  SBAR(); if constexpr (E == 0 || E == 4 || E >= 7) TSTAMP(s);
}
template <int K> DI void stats(St& s) {
  if constexpr (K == 0) { asm volatile("" : "+v"(s.sp0), "+v"(s.sp1), "+v"(s.sq)); s.t0 = (s.sp0[0] + s.sp0[2]) + (s.sp1[0] + s.sp1[2]); s.t1 = (s.sp0[1] + s.sp0[3]) + (s.sp1[1] + s.sp1[3]); }
  else if constexpr (K == 1) s.t0 = (swap32_add(s.t0) + s.sq[0]) * (1.f / 9.f);
  else if constexpr (K == 2) { const float d0 = s.sp0[0] - s.t0, d1 = s.sp0[2] - s.t0; s.t1 = __builtin_fmaf(64.f, d0 * d0 + d1 * d1, s.t1); }
  else if constexpr (K == 3) { const float d0 = s.sp1[0] - s.t0, d1 = s.sp1[2] - s.t0; s.t1 = __builtin_fmaf(64.f, d0 * d0 + d1 * d1, s.t1); }
  else if constexpr (K == 4) { const float d = s.sq[0] - s.t0; s.t1 = swap32_add(s.t1) + __builtin_fmaf(64.f * d, d, s.sq[1]); }
  else if constexpr (K == 5) s.t1 = rbf(rbf(s.t1 * (1.f / DM)) + 1e-5f);
  else if constexpr (K == 6) { s.t1 = rbf(__builtin_amdgcn_sqrtf(s.t1)); s.nmean = -rbf(s.t0); }
  else { s.c1 = -1.4426950408889634f * __builtin_amdgcn_rcpf(s.t1); s.c2 = s.t1 * s.t1; asm volatile("" : "+v"(s.c1), "+v"(s.c2), "+v"(s.nmean)); }
  if constexpr (K < 7) asm volatile("" : "+v"(s.t0), "+v"(s.t1));
}
template <int P, int H> DI void csread(St& s, f32x2& g, f32x2& u) { g = *(const LAS f32x2*)(lp(s.csb) + P * 128 + (H >> 1) * 32 + (H & 1) * 8); u = *(const LAS f32x2*)(lp(s.csb) + P * 128 + 64 + (H >> 1) * 32 + (H & 1) * 8); }
template <int P, int ST> DI void ep(St& s) {
  if constexpr (ST == 0) { csread<P, 0>(s, s.csg, s.csu); asm volatile("s_nop 3"); }
  constexpr int k = (ST + 2) % 3, v = (ST - 1) / 3;
  if constexpr (ST >= 1 && ST < 28) {
    if constexpr (k == 0) {
      if constexpr (v > 0) { s.d0 = __builtin_fmaf(s.x1, s.c2, s.c2); asm volatile("" : "+v"(s.d0)); }
      if constexpr (v < 8) {
        const f32x2& cg = (v & 2) ? s.csg2 : s.csg; const f32x2& cu = (v & 2) ? s.csu2 : s.csu;
        s.ag = __builtin_fmaf(s.nmean, cg[v & 1], s.acc[P][v]); s.au = __builtin_fmaf(s.nmean, cu[v & 1], s.acc[P][8 + v]); asm volatile("" : "+v"(s.ag), "+v"(s.au));
        if constexpr ((v & 1) == 0 && v < 6) { if constexpr (v & 2) csread<P, (v >> 1) + 1>(s, s.csg, s.csu); else csread<P, (v >> 1) + 1>(s, s.csg2, s.csu2); }
      }
    } else if constexpr (k == 1) {
      if constexpr (v > 0) { s.d0 = __builtin_amdgcn_rcpf(s.d0); asm volatile("" : "+v"(s.d0)); }
      if constexpr (v < 8) { s.x0 = s.ag * s.c1; s.t0 = s.ag * s.au; asm volatile("" : "+v"(s.x0), "+v"(s.t0)); }
    } else {
      if constexpr (v > 0) { float o = s.t1 * s.d0; asm volatile("" : "+v"(o)); if constexpr (v & 1) s.o0 = o; else s.o1 = o; }
      if constexpr (v < 8) { s.x1 = __builtin_amdgcn_exp2f(s.x0); s.t1 = s.t0; asm volatile("" : "+v"(s.x1)); }
    }
  }
  if constexpr (ST >= 10 && ST <= 28 && (ST - 10) % 6 == 0) { unsigned pkd; asm volatile("v_cvt_pk_bf16_f32 %0, %1, %2" : "=v"(pkd) : "v"(s.o0), "v"(s.o1)); s.pk[(ST - 10) / 6] = pkd; }
  if constexpr (ST == NEP - 1) st128m<ST4>(s.pk, s.rAct, s.vst, s.sAct + P * 512u);
}
template <int G, int P> DI void ep_try(St& s) {
  if constexpr (G >= ep_start(P) - 1 && G < ep_start(P) - 1 + NEP) ep<P, G - ep_start(P) + 1>(s); }
template <int G> DI void slot(St& s) {
  constexpr int en = epoch_of(G) + 1;
  if constexpr (en < NE && G == bslot(en)) { barrier<en>(s); reads<G + 2>(s); reads<G + 3>(s); }
  else if constexpr (!(en < NE && G + LA >= estart(en) && G < bslot(en))) reads<G + LA>(s);
  vops<s_lo(G)>(s, mkseq<s_lo(G + 1) - s_lo(G)>{});
  if constexpr (op_at(G) >= 0) vop<op_at(G)>(s);
  constexpr int p = u_pair(G), ks = u_kstep(G);
  if constexpr (ks == 0) MFMA32_NEW(s.acc[p], s.wf[G], s.hf[ks]); else MFMA32_ACC(s.acc[p], s.wf[G], s.hf[ks]);
  if constexpr (G >= ST0 && G < ST0 + 8) stats<G - ST0>(s);
  ep_try<G, 0>(s); ep_try<G, 1>(s); ep_try<G, 2>(s); ep_try<G, 3>(s); ep_try<G, 4>(s); ep_try<G, 5>(s); ep_try<G, 6>(s); ep_try<G, 7>(s);
  SBAR();
}
template <int... Gs> DI void run(St& s, iseq<Gs...>) { (slot<Gs>(s), ...); }
template <int... Ss> DI void ep_all(St& s, iseq<Ss...>) { (ep<8, Ss>(s), ...); }
}

__global__ __launch_bounds__(512, 2) void k4_up(const u16* __restrict__ wg, const u16* __restrict__ wu, char* __restrict__ ws) {
  using namespace k4;
  const unsigned tid = threadIdx.x, lane = tid & 63, w = rfl(tid >> 6), r = lane & 31, hi = lane >> 5, blk = (lane >> 4) & 1, q2 = (lane >> 2) & 3, p2 = lane & 3;
  const unsigned b = blockIdx.x, xcd = b & 7, ix = b >> 3;
  const unsigned rb = ix >> 2, cb = xcd * 4 + (ix & 3);
  St s;
  TSTAMP(s);
  s.w = w; s.rW = mksrd(ws + WS_WGU, DM * FF * 4); s.rAct = mksrd(ws + WS_ACT, SEQ * FF * 2);
  s.rH = mksrd(ws + WS_H, SEQ * DM * 2); s.rS = mksrd(ws + WS_ST, SEQ * 72); s.rCS = mksrd(ws + WS_CS, FF * 8);
  const unsigned row0 = rb * 256u + w * 32u;
  s.p0 = cb * 9u; s.sW = cb * 9u * 36864u; s.rR = mksrd(w < 4 ? wg : wu, DM * FF * 2);
  s.vA = (4u * hi + q2) * (FF * 2u) + blk * 64u + ((p2 ^ ((w >> 2) << 1)) << 4); s.sA = cb * 288u + (w & 3) * 8u * (FF * 2u);
  s.wbA = blk * 4096u + hi * 1024u + q2 * 64u + blk * 32u + p2 * 8u;
  s.vl16 = lane * 16u; s.sHh = (row0 >> 5) * 36864u;
  s.vst = (r >> 4) * 147456u + hi * 256u + (r & 15) * 16u;
  s.sAct = (rb * 16u + w * 2u) * 147456u + cb * 9u * 512u;
  s.csb = (row0 + r) * 72u; s.vsp = s.csb + hi * 32u;
  vops<200>(s, mkseq<3>{});
  vops<0>(s, mkseq<6>{});
  __builtin_amdgcn_s_barrier();
  vops<6>(s, mkseq<NPRO - 6>{});
  barrier<0>(s);
  reads<0>(s); reads<1>(s); reads<2>(s); SBAR();
  run(s, mkseq<NU>{});
  TSTAMP(s);
  asm volatile("s_nop 7\n\ts_nop 7" ::: "memory"); SBAR();
  ep_all(s, mkseq<NEP>{});
}

extern "C" void kernel_launch(void* const* d_in, const int* in_sizes, int n_in, void* d_out, int out_size, void* d_ws, size_t ws_size, hipStream_t stream) {
  const u16* x = (const u16*)d_in[0]; const u16* ckv = (const u16*)d_in[1]; const u16* wq = (const u16*)d_in[2]; const u16* wuk = (const u16*)d_in[3];
  const u16* wuv = (const u16*)d_in[4]; const u16* wo = (const u16*)d_in[5]; const u16* wg = (const u16*)d_in[7]; const u16* wu = (const u16*)d_in[8]; const u16* wd = (const u16*)d_in[9];
  char* ws = (char*)d_ws;
  (void)hipFuncSetAttribute((const void*)k1_kvproj, hipFuncAttributeMaxDynamicSharedMemorySize, k1::LDS_BYTES);
  k1_kvproj<<<256, 512, k1::LDS_BYTES, stream>>>(ckv, wuk, wuv, x, wq, ws);
  (void)hipFuncSetAttribute((const void*)k2_attn, hipFuncAttributeMaxDynamicSharedMemorySize, k2::LDS_BYTES);
  k2_attn<<<256, 512, k2::LDS_BYTES, stream>>>(wq, wo, wg, wu, wd, ws);
  (void)hipFuncSetAttribute((const void*)k3_oproj, hipFuncAttributeMaxDynamicSharedMemorySize, k3::LDS_BYTES);
  k3_oproj<<<k3::GRID, 512, k3::LDS_BYTES, stream>>>(wo, x, wg, wu, ws);
  (void)hipFuncSetAttribute((const void*)k4_up, hipFuncAttributeMaxDynamicSharedMemorySize, k4::LDS_BYTES);
  k4_up<<<256, 512, k4::LDS_BYTES, stream>>>(wg, wu, ws);
  (void)hipFuncSetAttribute((const void*)k5_down, hipFuncAttributeMaxDynamicSharedMemorySize, k3::LDS_BYTES);
  k5_down<<<k3::GRID, 512, k3::LDS_BYTES, stream>>>(wd, ws, (u16*)d_out);
}
```

```cpp
#include <hip/hip_runtime.h>
#include <utility>
typedef unsigned short u16;
typedef short bf16x8 __attribute__((ext_vector_type(8)));
typedef short s16x4 __attribute__((ext_vector_type(4)));
typedef float f32x16 __attribute__((ext_vector_type(16)));
typedef float f32x4 __attribute__((ext_vector_type(4)));
typedef float f32x2 __attribute__((ext_vector_type(2)));
typedef unsigned u32x4 __attribute__((ext_vector_type(4)));
typedef unsigned u32x2 __attribute__((ext_vector_type(2)));
typedef __bf16 bf16x2_t __attribute__((ext_vector_type(2)));
#define LAS __attribute__((address_space(3)))
#define DI __device__ __forceinline__
#define SBAR() __builtin_amdgcn_sched_barrier(0)
#define MFMA32(a, b, c) __builtin_amdgcn_mfma_f32_32x32x16_bf16((a), (b), (c), 0, 0, 0)
#define MFMA16(a, b, c) __builtin_amdgcn_mfma_f32_16x16x32_bf16((a), (b), (c), 0, 0, 0)
#define MFMA16_ACC(c, a, b) asm volatile("v_mfma_f32_16x16x32_bf16 %0, %1, %2, %0" : "+v"(c) : "v"(a), "v"(b))
#define MFMA16_NEW(c, a, b) asm volatile("v_mfma_f32_16x16x32_bf16 %0, %1, %2, 0" : "=v"(c) : "v"(a), "v"(b))
#define MFMA32_ACC(c, a, b) asm volatile("v_mfma_f32_32x32x16_bf16 %0, %1, %2, %0" : "+v"(c) : "v"(a), "v"(b))
#define MFMA32_NEW(c, a, b) asm volatile("v_mfma_f32_32x32x16_bf16 %0, %1, %2, 0" : "=v"(c) : "v"(a), "v"(b))
template <int... Is> using iseq = std::integer_sequence<int, Is...>;
template <int N> using mkseq = std::make_integer_sequence<int, N>;
typedef __amdgpu_buffer_rsrc_t srd_t;

DI unsigned rfl(unsigned x) { return __builtin_amdgcn_readfirstlane(x); }
DI float bf2f(unsigned v) { return __uint_as_float(v << 16); }
DI float bflo(unsigned w) { return __uint_as_float(w << 16); }
DI float bfhi(unsigned w) { return __uint_as_float(w & 0xffff0000u); }
DI unsigned cvtpk(float lo, float hi) { f32x2 v = {lo, hi}; bf16x2_t b = __builtin_convertvector(v, bf16x2_t); return __builtin_bit_cast(unsigned, b); }
DI float rbf(float f) { return bflo(cvtpk(f, f)); }
DI srd_t mksrd(const void* p, unsigned bytes) { return __builtin_amdgcn_make_buffer_rsrc((void*)p, (short)0, (int)bytes, 0x00020000); }
DI void dma16(unsigned m0v, unsigned voff, srd_t srd, unsigned soff) {
  asm volatile("s_mov_b32 m0, %0\n\ts_nop 0\n\tbuffer_load_dwordx4 %1, %2, %3 offen lds" ::"s"(m0v), "v"(voff), "s"(srd), "s"(soff) : "memory");
}
DI void dma16s(unsigned m0v, unsigned voff, srd_t srd, unsigned soff) {
  asm volatile("s_mov_b32 m0, %0\n\ts_nop 0\n\tbuffer_load_dwordx4 %1, %2, %3 offen sc1 lds" ::"s"(m0v), "v"(voff), "s"(srd), "s"(soff) : "memory");
}
template <int N> DI void wait_vm() { asm volatile("s_waitcnt vmcnt(%0)" ::"i"(N) : "memory"); }
template <int N> DI void wait_bar() { asm volatile("s_waitcnt vmcnt(%0) lgkmcnt(0)\n\ts_barrier" ::"i"(N) : "memory"); }
DI bf16x8 lds128(const LAS char* p) { return *(const LAS bf16x8*)p; }
typedef short v4i16_t __attribute__((ext_vector_type(4)));
DI s16x4 ldstr(const LAS char* p) { return __builtin_bit_cast(s16x4, __builtin_amdgcn_ds_read_tr16_b64_v4i16((LAS v4i16_t*)p)); }
DI bf16x8 cat8(s16x4 a, s16x4 b) { return __builtin_shufflevector(a, b, 0, 1, 2, 3, 4, 5, 6, 7); }
DI u32x4 pack8(const f32x16& x, int s) {
  u32x4 w = {cvtpk(x[8 * s], x[8 * s + 1]), cvtpk(x[8 * s + 2], x[8 * s + 3]), cvtpk(x[8 * s + 4], x[8 * s + 5]), cvtpk(x[8 * s + 6], x[8 * s + 7])};
  return w;
}
template <int MODE> DI void st128m(u32x4 v, srd_t srd, unsigned voff, unsigned soff) {
  if constexpr (MODE == 0) asm volatile("buffer_store_dwordx4 %0, %1, %2, %3 offen\n\ts_nop 1" ::"v"(v), "v"(voff), "s"(srd), "s"(soff) : "memory");
  else if constexpr (MODE == 1) asm volatile("buffer_store_dwordx4 %0, %1, %2, %3 offen sc1\n\ts_nop 1" ::"v"(v), "v"(voff), "s"(srd), "s"(soff) : "memory");
  else if constexpr (MODE == 2) asm volatile("buffer_store_dwordx4 %0, %1, %2, %3 offen nt\n\ts_nop 1" ::"v"(v), "v"(voff), "s"(srd), "s"(soff) : "memory");
  else asm volatile("buffer_store_dwordx4 %0, %1, %2, %3 offen sc0 sc1\n\ts_nop 1" ::"v"(v), "v"(voff), "s"(srd), "s"(soff) : "memory");
}
#define ST1 1
#define ST2 1
#define ST3 1
#define ST4 1
#define ST6 0
#define ST5 1
template <int CTRL> DI float dpp_add(float x) { return x + __builtin_bit_cast(float, __builtin_amdgcn_update_dpp(0, __builtin_bit_cast(int, x), CTRL, 0xf, 0xf, true)); }
DI float swap32_add(float x) { auto r = __builtin_amdgcn_permlane32_swap(__float_as_uint(x), __float_as_uint(x), false, false); return __uint_as_float(r[0]) + __uint_as_float(r[1]); }
DI float swap16_add(float x) { auto r = __builtin_amdgcn_permlane16_swap(__float_as_uint(x), __float_as_uint(x), false, false); return __uint_as_float(r[0]) + __uint_as_float(r[1]); }
DI float swap32_max(float x) { auto r = __builtin_amdgcn_permlane32_swap(__float_as_uint(x), __float_as_uint(x), false, false); return __builtin_fmaxf(__uint_as_float(r[0]), __uint_as_float(r[1])); }
DI float wave_sum(float x) {
  x = dpp_add<0xB1>(x); x = dpp_add<0x4E>(x); x = dpp_add<0x141>(x); x = dpp_add<0x140>(x);
  x = swap16_add(x); return swap32_add(x);
}

constexpr int SEQ = 2048, DM = 576, DC = 512, NH = 32, HD = 128, FF = 4608, HDM = NH * HD;
constexpr size_t WS_XN = 0;
constexpr size_t WS_KF = WS_XN + (size_t)SEQ * DM * 2;
constexpr size_t WS_VF = WS_KF + (size_t)SEQ * HDM * 2;
constexpr size_t WS_CTX = WS_VF + (size_t)SEQ * HDM * 2;
constexpr size_t WS_QS = WS_CTX + (size_t)SEQ * HDM * 2;
constexpr size_t WS_WO = WS_QS + (size_t)256 * 32768;
constexpr size_t WS_WGU = WS_WO + (size_t)HDM * DM * 2;
constexpr size_t WS_WD = WS_WGU + (size_t)DM * FF * 4;
constexpr size_t WS_H = WS_WD + (size_t)FF * DM * 2;
constexpr size_t WS_ST = WS_H + (size_t)SEQ * DM * 2;
constexpr size_t WS_CS = WS_ST + (size_t)SEQ * 128;
constexpr size_t WS_ACT = WS_CS + (size_t)288 * 128;
constexpr size_t WS_XB = WS_ACT + (size_t)SEQ * FF * 2;
constexpr size_t WS_FLAG = WS_XB + (size_t)2 * 234 * 20480;
constexpr size_t WS_CNT = WS_FLAG + 65536;
constexpr size_t WS_END = WS_CNT + 4096;
DI void arrive(char* ws, unsigned i) { __hip_atomic_fetch_add((unsigned*)(ws + WS_CNT) + i * 32u, 1u, __ATOMIC_RELAXED, __HIP_MEMORY_SCOPE_AGENT); }
DI unsigned peek(char* ws, unsigned i) { return rfl(__hip_atomic_load((unsigned*)(ws + WS_CNT) + i * 32u, __ATOMIC_RELAXED, __HIP_MEMORY_SCOPE_AGENT)); }
DI void await(char* ws, unsigned i, unsigned n) {
  for (unsigned spin = 0; spin < (1u << 22); ++spin) { if (peek(ws, i) >= n) break; __builtin_amdgcn_s_sleep(2); }
  asm volatile("" ::: "memory");
}

#define TSTAMP(s) do {} while (0)

namespace k1 {
constexpr int SLOT = 53248, XSCR = 32768  , DUMP = 3 * SLOT  , LDS_BYTES = DUMP + 2048, LA = 3  , BB = 2  ;
constexpr int NC = 11, P1 = 160;
constexpr int cstart(int c) { return c <= 8 ? 20 * c : P1 + 32 * (c - 8); }
constexpr int chunk_of(int g) { return g < P1 ? g / 20 : 8 + ((g - P1) >> 5); }
constexpr int bslot(int c) { return cstart(c) - BB; }
constexpr int dcnt(int c) { return c < 8 ? 7 : (c < NC ? 4 : 0); }
constexpr int kt_of(int g) { const int c = chunk_of(g), i = g - cstart(c); return c < 7 ? (i % 10) : c == 7 ? (i >> 1) : (i & 1); }
constexpr int sloc_of(int g) { const int c = chunk_of(g), i = g - cstart(c); return c < 7 ? (i / 10) : c == 7 ? (i & 1) : (i >> 1); }
constexpr int kstep_of(int g) { const int c = chunk_of(g); return c < 8 ? 2 * c + sloc_of(g) : sloc_of(g); }
constexpr bool w_first(int g) { const int c = chunk_of(g), i = g - cstart(c); return c < 7 ? (i % 10) == 0 : (c == 7 && i < 2); }
constexpr int acc_of(int g) { const int c = chunk_of(g); return c < 8 ? kt_of(g) : 10 + 2 * (c - 8) + kt_of(g); }
constexpr int dma_at(int g) { for (int c = 0; c < NC - 2; ++c) { const int j = g - (c ? bslot(c) : 0); if (j >= 0 && j < 2 * dcnt(c + 2) && !(j & 1)) return (c + 2) * 8 + (j >> 1); } return -1; }
constexpr bool sure(int d) { return d >= 0 && (d & 7) != 6; }
constexpr int pk_at(int g) {
  for (int u = 0; u < 5; ++u) { if (g == 140 + 4 * u + 6) return 2 * u; if (g == 140 + 4 * u + 7) return 2 * u + 1; }
  for (int u = 5; u < 7; ++u) { if (g == cstart(u + 4) + 3) return 2 * u; if (g == cstart(u + 4) + 4) return 2 * u + 1; }
  return -1;
}
constexpr int st_at(int g) {
  if (g < 155 || (g - 155) % 7 != 0) return -1;
  constexpr int order[15] = {0, 1, 2, 3, 4, 5, 6, 10, 7, 11, 8, 9, 12, 13, -1};
  return order[(g - 155) / 7];
}
constexpr int XWARM = 15, XLD = 161, LN0 = 172, NLN = 48, LNRD = 226, LNST = 230, WARM = 236;
constexpr int ln_at(int g) { return (g >= LN0 && g < LN0 + NLN) ? g - LN0 : -1; }
constexpr int late_ops(int g) { return (st_at(g) >= 0 ? 1 : 0) + (g == LNST ? 2 : 0) + (g == WARM || g == XWARM ? 1 : 0) + (g == XLD ? 2 : 0); }
constexpr int ops_between(int from, int to) { int n = 0; for (int g = from + 1; g < to; ++g) n += (sure(dma_at(g)) ? 1 : 0) + late_ops(g); return n; }
constexpr int vm_wait(int c) {
  if (c == 0) return 6;
  if (c == 1) return ops_between(-1, bslot(1));
  const int from = (c == 2 ? 0 : bslot(c - 2)) + 2 * (dcnt(c) - 1);
  return late_ops(from) + ops_between(from, bslot(c));
}
DI unsigned olane() { unsigned l; asm volatile("v_mbcnt_lo_u32_b32 %0, -1, 0\n\tv_mbcnt_hi_u32_b32 %0, -1, %0" : "=v"(l)); return l; }
struct St {
  bf16x8 wf[16][2]; bf16x8 af[256]; f32x4 acc[16][2]; u32x4 pk[16];
  unsigned wp, ap[4];
  unsigned wb, ab0, ab1, voffA0, voffW, m0w, sA, sW, vst, sO;
  srd_t rA, rK, rV, rO, rXN, rX; unsigned sX;
  u32x4 x0, x1; float v[16], t0, t1, t2; unsigned lane, sXN; bool two;
  srd_t rWm; unsigned sWm;
};
template <int C, int J> DI void dma(St& s) {
  constexpr unsigned slot = (C % 3) * SLOT;
  if constexpr (C < 8) {
    if constexpr (J < 4) {
      constexpr unsigned mat = J >> 1, jj = J & 1;
      dma16(s.m0w * 2048u + slot + mat * 16384u + jj * 1024u, s.voffW, mat ? s.rV : s.rK, s.sW + s.m0w * 65536u + jj * 32768u + C * 524288u);
    } else if constexpr (J < 6) dma16(s.m0w * 2048u + slot + 32768u + (J - 4) * 1024u, s.voffA0, s.rA, s.sA + s.m0w * 16384u + (J - 4) * 8192u + C * 128u);
    else { if (s.m0w < 4u) dma16(s.m0w * 1024u + slot + 49152u, s.voffA0 ^ (((s.m0w ^ (s.m0w >> 1)) & 1u) << 6), s.rA, s.sA + 131072u + s.m0w * 8192u + C * 128u); }
  } else dma16(s.m0w * 4096u + slot + J * 1024u, (olane() ^ (8u * (s.m0w >> 2) + ((4u * s.m0w + J) & 7u))) << 4, s.rA, s.sA + (160u + (C - 8) * 32u + J) * 1024u + s.m0w * 4096u);
}
DI const LAS char* lp(unsigned a) { return (const LAS char*)(size_t)a; }
template <int G> DI void reads(St& s) {
  if constexpr (G < 256) {
    constexpr int c = chunk_of(G), kt = kt_of(G), sl = sloc_of(G), ks = kstep_of(G);
    if constexpr (w_first(G)) {
      s.wf[ks][0] = cat8(ldstr(lp(s.wp) + sl * 8192), ldstr(lp(s.wp) + sl * 8192 + 1024));
      s.wf[ks][1] = cat8(ldstr(lp(s.wp ^ 16u) + sl * 8192), ldstr(lp(s.wp ^ 16u) + sl * 8192 + 1024));
    }
    if constexpr (c < 8) s.af[G] = lds128(lp(s.ap[sl]) + (kt >> 1) * 4096 + (kt & 1) * 1024);
    else s.af[G] = lds128(lp(s.ap[sl & 3]) + (sl >> 2) * 256 + kt * 8192);
  }
}
template <int C> DI void set_bases(St& s) {
  constexpr unsigned slot = (C % 3) * SLOT;
  if constexpr (C < 8) {
    unsigned w = s.wb + slot, a0 = s.ab0 + slot, a1 = (s.ab0 ^ 64u) + slot;
    asm volatile("" : "+v"(w), "+v"(a0), "+v"(a1));
    s.wp = w; s.ap[0] = a0; s.ap[1] = a1;
  } else {
    if constexpr (C == 8) { const unsigned l = olane(), r = l & 15; s.ab1 = (16u * (r >> 3) + (r & 7)) * 1024u + (((l >> 4) ^ r) << 4); s.lane = l; s.two = l < 8u; }
    unsigned a0 = s.ab1 + slot, a1 = (s.ab1 ^ 64u) + slot, a2 = (s.ab1 ^ 128u) + slot, a3 = (s.ab1 ^ 192u) + slot;
    asm volatile("" : "+v"(a0), "+v"(a1), "+v"(a2), "+v"(a3));
    s.ap[0] = a0; s.ap[1] = a1; s.ap[2] = a2; s.ap[3] = a3;
  }
}
template <bool ISV, int ID> DI void pack(St& s) {
  constexpr int u = ID >> 1, j = ID & 1;
  const f32x4 a = ISV ? s.acc[2 * u][j] : s.acc[2 * u + j][0], b = ISV ? s.acc[2 * u + 1][j] : s.acc[2 * u + j][1];
  s.pk[ID] = u32x4{cvtpk(a[0], a[1]), cvtpk(a[2], a[3]), cvtpk(b[0], b[1]), cvtpk(b[2], b[3])};
  asm volatile("" : "+v"(s.pk[ID]));
}
template <int ID> DI void store(St& s) { st128m<ST1>(s.pk[ID], s.rO, s.vst, s.sO + (ID >> 1) * 8192u + (ID & 1) * 128u); }
template <int ST> DI void ln_stage(St& s) {
  if constexpr (ST < 4) {
    if constexpr (ST == 0) { asm volatile("s_waitcnt vmcnt(%2)" : "+v"(s.x0), "+v"(s.x1) : "i"(ops_between(XLD, LN0 + 1)) : "memory"); s.t0 = 0.f; s.t2 = 0.f; }
    s.v[2 * ST] = bflo(s.x0[ST]); s.v[2 * ST + 1] = bfhi(s.x0[ST]); s.t0 += s.v[2 * ST] + s.v[2 * ST + 1];
  } else if constexpr (ST < 8) { constexpr int i = ST - 4; s.v[8 + 2 * i] = bflo(s.x1[i]); s.v[9 + 2 * i] = bfhi(s.x1[i]); s.t2 += s.v[8 + 2 * i] + s.v[9 + 2 * i]; }
  else if constexpr (ST == 8) s.t0 = dpp_add<0xB1>(s.t0 + (s.two ? s.t2 : 0.f));
  else if constexpr (ST == 9) s.t0 = dpp_add<0x141>(dpp_add<0x4E>(s.t0));
  else if constexpr (ST == 10) s.t0 = dpp_add<0x140>(s.t0);
  else if constexpr (ST == 11) s.t0 = swap16_add(s.t0);
  else if constexpr (ST == 12) { s.t0 = swap32_add(s.t0) * (1.f / DM); s.t1 = 0.f; s.t2 = 0.f; }
  else if constexpr (ST < 21) { constexpr int i = 2 * (ST - 13); float& q = i < 8 ? s.t1 : s.t2; const float d = s.v[i] - s.t0, e = s.v[i + 1] - s.t0; q += d * d; q += e * e; }
  else if constexpr (ST == 21) s.t1 = dpp_add<0xB1>(s.t1 + (s.two ? s.t2 : 0.f));
  else if constexpr (ST == 22) s.t1 = dpp_add<0x141>(dpp_add<0x4E>(s.t1));
  else if constexpr (ST == 23) s.t1 = dpp_add<0x140>(s.t1);
  else if constexpr (ST == 24) s.t1 = swap16_add(s.t1);
  else if constexpr (ST == 25) s.t1 = swap32_add(s.t1) * (1.f / DM);
  else if constexpr (ST == 26) { s.t0 = rbf(s.t0); s.t1 = rbf(s.t1); }
  else if constexpr (ST == 27) s.t1 = rbf(s.t1 + 1e-5f);
  else if constexpr (ST == 28) s.t1 = rbf(__builtin_amdgcn_sqrtf(s.t1));
  else if constexpr (ST == 29) s.t1 = __builtin_amdgcn_rcpf(s.t1);
  else if constexpr (ST < 46) {
    constexpr int i = (ST - 30) >> 1; u32x4& o = i < 4 ? s.x0 : s.x1;
    if constexpr (((ST - 30) & 1) == 0) o[i & 3] = cvtpk(s.v[2 * i] - s.t0, s.v[2 * i + 1] - s.t0);
    else o[i & 3] = cvtpk(bflo(o[i & 3]) * s.t1, bfhi(o[i & 3]) * s.t1);
    asm volatile("" : "+v"(o));
  } else if constexpr (ST == 46) *(LAS u32x4*)(size_t)(XSCR + s.lane * 128u + ((s.m0w ^ (s.lane & 7)) << 4)) = s.x0;
  else { if (s.two) *(LAS u32x4*)(size_t)(XSCR + 8192u + s.lane * 128u + ((s.m0w ^ (s.lane & 7)) << 4)) = s.x1; }
  if constexpr (ST < 30) asm volatile("" : "+v"(s.t0), "+v"(s.t1), "+v"(s.t2));
}
template <bool ISV, int G> DI void slot(St& s) {
  constexpr int cn = chunk_of(G) + 1;
  constexpr bool isbar = cn < NC && G == bslot(cn);
  if constexpr (isbar) {
    wait_bar<vm_wait(cn)>(); set_bases<cn>(s); SBAR();
    reads<G + BB>(s); reads<G + BB + 1>(s);
  } else if constexpr (!(cn < NC && G + LA >= cstart(cn) && G < bslot(cn))) reads<G + LA>(s);
  if constexpr (dma_at(G) >= 0) dma<(dma_at(G) >> 3), (dma_at(G) & 7)>(s);
  constexpr int ai = acc_of(G), ks = kstep_of(G);
#pragma unroll
  for (int ct = 0; ct < 2; ++ct) {
    const bf16x8 a = ISV ? s.af[G] : s.wf[ks][ct], bq = ISV ? s.wf[ks][ct] : s.af[G];
    if constexpr (ks == 0) MFMA16_NEW(s.acc[ai][ct], a, bq); else MFMA16_ACC(s.acc[ai][ct], a, bq);
  }
  if constexpr (pk_at(G) >= 0) pack<ISV, pk_at(G)>(s);
  if constexpr (st_at(G) >= 0) store<st_at(G)>(s);
  if constexpr (G == XLD) asm volatile("buffer_load_dwordx4 %0, %2, %4, %5 offen\n\tbuffer_load_dwordx4 %1, %3, %4, %5 offen offset:1024"
                                       : "=&v"(s.x0), "=&v"(s.x1) : "v"(s.lane * 16u), "v"((s.lane & 7) * 16u), "s"(s.rX), "s"(s.sX) : "memory");
  if constexpr (ln_at(G) >= 0) ln_stage<ln_at(G)>(s);
  if constexpr (G == XWARM) { const unsigned l = olane();
    asm volatile("s_mov_b32 m0, %0\n\ts_nop 0\n\tbuffer_load_dword %1, %2, %3 offen lds" ::"s"(DUMP + s.m0w * 256u), "v"(l < 9u ? l * 128u : 0x10000000u), "s"(s.rX), "s"(s.sX) : "memory"); }
  if constexpr (G == LNRD) {
    s.x0 = *(const LAS u32x4*)(size_t)(XSCR + s.m0w * 1024u + s.lane * 16u); s.x1 = *(const LAS u32x4*)(size_t)(XSCR + 8192u + s.lane * 16u);
  }
  if constexpr (G == LNST) {
    const unsigned vo = (s.lane >> 3) * 512u + (((s.lane ^ (s.lane >> 3)) & 7) << 4);
    st128m<ST1>(s.x0, s.rXN, vo, s.sXN + s.m0w * 4096u); st128m<ST1>(s.x1, s.rXN, vo, s.m0w == 0 ? s.sXN + 32768u : 0x10000000u);
  }
  if constexpr (G == WARM)
    asm volatile("s_mov_b32 m0, %0\n\ts_nop 0\n\tbuffer_load_dword %1, %2, %3 offen lds" ::"s"(DUMP + s.m0w * 256u), "v"(s.lane < 18u ? (s.lane >> 1) * 8192u + (s.lane & 1) * 128u : 0x10000000u), "s"(s.rWm), "s"(s.sWm) : "memory");
  SBAR();
}
template <bool ISV, int... Gs> DI void run(St& s, iseq<Gs...>) {
  reads<0>(s); reads<1>(s); reads<2>(s); SBAR();
  (slot<ISV, Gs>(s), ...);
  asm volatile("s_nop 7\n\ts_nop 3" ::: "memory"); SBAR();
  pack<ISV, 14>(s); store<14>(s); pack<ISV, 15>(s); store<15>(s);
}
template <int C, int... Js> DI void dma_all(St& s, iseq<Js...>) { (dma<C, Js>(s), ...); }
}

__global__ __launch_bounds__(512, 2) void k1_kvproj(const u16* __restrict__ ckv, const u16* __restrict__ wuk, const u16* __restrict__ wuv, const u16* __restrict__ x,
    const u16* __restrict__ wq, char* __restrict__ ws) {
  using namespace k1;
  const unsigned tid = threadIdx.x, lane = tid & 63, w = rfl(tid >> 6), r = lane & 15, g = lane >> 4;
  const unsigned b = blockIdx.x, xcd = b & 7, ix = b >> 3, h = xcd * 4 + (ix & 3), rb = ix >> 2;
  St s;
  s.rA = mksrd(ckv, SEQ * DC * 2); s.rK = mksrd(wuk, DC * HDM * 2); s.rV = mksrd(wuv, DC * HDM * 2);
  s.rO = mksrd(ws + (w < 4 ? WS_KF : WS_VF), SEQ * HDM * 2); s.rXN = mksrd(ws + WS_XN, SEQ * DM * 2);
  s.m0w = w; s.sXN = (b >> 2) * 36864u + (b & 3) * 128u;
  const unsigned q2 = (lane >> 2) & 3, p2 = lane & 3, rrow = 16u * (r >> 3) + (r & 7);
  s.voffA0 = (lane >> 3) * 1024u + (((lane & 7) ^ (4u * (w & 1) + g)) << 4);
  s.voffW = q2 * 8192u + g * 64u + ((p2 ^ (w & 1u)) << 4);
  s.sA = rb * 256u * 1024u; s.sW = h * 256u;
  s.wb = (w >> 2) * 16384u + (w & 3) * 256u + g * 2048u + q2 * 64u + (p2 >> 1) * 32u + (g & 1) * 16u + (p2 & 1) * 8u;
  s.ab0 = 32768u + rrow * 128u + ((g ^ (r >> 1)) << 4);
  s.vst = (g >> 1) * (w < 4 ? 1024u : 4096u) + (rrow + 32u * (g & 1)) * 16u;
  s.sO = (h * 64u + rb * 8u) * 8192u + (w < 4 ? w * 2048u : (w - 4) * 1024u);
  s.rWm = mksrd(wq, DM * HDM * 2); s.sWm = (rb * 72u + w * 9u) * 8192u + h * 256u;
  s.rX = mksrd(x, SEQ * DM * 2); s.sX = (b * 8u + w) * (DM * 2u);
  if (tid < 2u) *(unsigned*)(ws + WS_FLAG + (tid * 256u + b) * 128u) = 0u;
  if (b == 0 && tid >= 64u && tid < 96u) *(unsigned*)(ws + WS_CNT + (tid - 64u) * 128u) = 0u;
  dma_all<0>(s, mkseq<7>{});
  __builtin_amdgcn_s_barrier();
  dma_all<1>(s, mkseq<7>{});
  wait_bar<vm_wait(0)>(); set_bases<0>(s); SBAR();
  if (w < 4) run<false>(s, mkseq<256>{}); else run<true>(s, mkseq<256>{});
}

namespace k2 {
constexpr unsigned KR = 0, VR = 65536, SP = 131072, QR = VR  , LDS_BYTES = 163840;
constexpr float THR = 8.f;
constexpr float QSCALE = 0.08838834764831845f * 1.4426950408889634f;
DI const LAS char* lp(unsigned a) { return (const LAS char*)(size_t)a; }
DI float max3f(float a, float b, float c) { float r; asm("v_max3_f32 %0, %1, %2, %3" : "=v"(r) : "v"(a), "v"(b), "v"(c)); return r; }
DI unsigned opaque_lane() { unsigned l; asm volatile("v_mbcnt_lo_u32_b32 %0, -1, 0\n\tv_mbcnt_hi_u32_b32 %0, -1, %0" : "=v"(l)); return l; }

constexpr int QLA = 3, QBB = 2;
constexpr int qb(int c) { return 16 * c - QBB; }
constexpr int qfirst(int c) { return c == 2 ? 0 : c == 3 ? 6 : qb(c - 3); }
constexpr int qop_chunk(int g) { for (int c = 2; c < 9; ++c) { const int j = g - qfirst(c); if (j >= 0 && j < 6) return c * 8 + j; } return -1; }
constexpr int qkv_at(int g) { return (g >= 96 && g < 112 && ((g - 96) & 3) == 0) ? (g - 96) >> 2 : -1; }
constexpr int qops(int g) { return (qop_chunk(g) >= 0) + (qkv_at(g) >= 0); }
constexpr int qwait(int c) {
  if (c == 0) return 6;
  int n = 0; const int from = c == 1 ? 0 : qfirst(c) + 6;
  for (int g = from; g < qb(c); ++g) n += qops(g);
  return n;
}
struct St {
  bf16x8 xf[36], wf[144];
  bf16x8 qf[8], fr[35]; f32x16 o[4], sc[2][2], negm, oo[2]; u32x4 pf[4], out[4]; f32x4 p[8]; f32x2 st; f32x4 la; bf16x8 sel;
  float m, mo, l, ls0, ls1, mx0, mx1, f, ca, cb; bool pend;
  unsigned w, vl16, wq_rd, vW, sX, sW, qw, kb, va, vb, sKV, n0, sC0;
  srd_t rW, rX, rK, rV, rC, rWm; unsigned vWm, sWm, dummy;
  unsigned tsv, nts;
};
struct Dm { unsigned m0k, m0v, kso, vso; };
DI unsigned tile_off(const St& s, unsigned c) { return s.sKV + (c < s.n0 ? c : c - s.n0) * 32768u; }
DI Dm mkdm(const St& s, int c) {
  const unsigned par = (unsigned)(c & 1) << 15;
  return Dm{KR + par + s.w * 4096u, VR + (par ^ 32768u) + s.w * 4096u, tile_off(s, c + 2), tile_off(s, c + 1)};
}
template <int J> DI void kvdma(const St& s, const Dm& d) {
  asm volatile("s_mov_b32 m0, %0\n\ts_nop 0\n\tbuffer_load_dwordx4 %1, %2, %3 offen offset:%4 lds" ::"s"(J < 4 ? d.m0k : d.m0v), "v"(s.vl16), "s"(J < 4 ? s.rK : s.rV), "s"(J < 4 ? d.kso : d.vso), "i"((J & 3) * 1024) : "memory");
}
template <int C, int J> DI void qload(St& s) {
  if constexpr (J < 2) dma16(QR + (C & 3) * 16384u + s.w * 2048u + J * 1024u, s.vW, s.rW, s.sW + (C * 64u + J * 4u) * 8192u);
  else asm volatile("buffer_load_dwordx4 %0, %1, %2, %3 offen offset:%4" : "=v"(s.xf[4 * C + J - 2]) : "v"(s.vl16), "s"(s.rX), "s"(s.sX + C * 4096u), "i"((J - 2) * 1024) : "memory");
}
template <int C> DI void qbar(St& s) {
  asm volatile("s_waitcnt vmcnt(%4) lgkmcnt(0)\n\ts_barrier" : "+v"(s.xf[4 * C]), "+v"(s.xf[4 * C + 1]), "+v"(s.xf[4 * C + 2]), "+v"(s.xf[4 * C + 3]) : "i"(qwait(C)) : "memory");
}
constexpr int q_t(int g) { return g < 128 ? (g & 3) : ((g - 128) >> 2); }
constexpr int q_s(int g) { return g < 128 ? ((g >> 2) & 3) : (g & 3); }
template <int G> DI void qread(St& s) {
  if constexpr (G < 144) { constexpr int off = ((G >> 4) & 3) * 16384 + q_s(G) * 4096 + q_t(G) * 256; s.wf[G] = cat8(ldstr(lp(s.wq_rd) + off), ldstr(lp(s.wq_rd) + off + 1024)); }
}
template <int F> DI void qpack(St& s) {
  constexpr int T = F >> 1, o8 = (F & 1) * 8;
  const u32x4 v = {cvtpk(s.o[T][o8] * QSCALE, s.o[T][o8 + 1] * QSCALE), cvtpk(s.o[T][o8 + 2] * QSCALE, s.o[T][o8 + 3] * QSCALE),
                   cvtpk(s.o[T][o8 + 4] * QSCALE, s.o[T][o8 + 5] * QSCALE), cvtpk(s.o[T][o8 + 6] * QSCALE, s.o[T][o8 + 7] * QSCALE)};
  s.qf[F] = __builtin_bit_cast(bf16x8, v);
  *(LAS bf16x8*)((LAS char*)(size_t)s.qw + F * 1024) = s.qf[F];
}
template <int G> DI void qslot(St& s) {
  constexpr int cn = (G >> 4) + 1;
  if constexpr (cn < 9 && G == qb(cn)) { qbar<cn>(s); SBAR(); if constexpr (cn == 3 || cn == 6) TSTAMP(s); qread<G + 2>(s); qread<G + 3>(s); }
  else if constexpr (!(cn < 9 && G + QLA >= 16 * cn && G < qb(cn))) qread<G + QLA>(s);
  if constexpr (qop_chunk(G) >= 0) qload<(qop_chunk(G) >> 3), (qop_chunk(G) & 7)>(s);
  if constexpr (qkv_at(G) >= 0) kvdma<qkv_at(G)>(s, mkdm(s, -2));
  constexpr int ks = 4 * (G >> 4) + q_s(G), t = q_t(G);
  const f32x16 z = {};
  s.o[t] = MFMA32(s.wf[G], s.xf[ks], ks == 0 ? z : s.o[t]);
  if constexpr (G >= 134 && ((G - 134) & 1) == 0) qpack<((G - 134) >> 1)>(s);
  SBAR();
}
template <int... Gs> DI void qrun(St& s, iseq<Gs...>) { (qslot<Gs>(s), ...); }
template <int C, int... Js> DI void qload_all(St& s, iseq<Js...>) { (qload<C, Js>(s), ...); }

template <int I> DI void frag(St& s) {
  if constexpr (I < 16) s.fr[I] = lds128(lp(s.kb) + (I & 1) * 8192 + (I >> 1) * 1024);
  else if constexpr (I < 32) s.fr[I] = lds128(lp((I & 2) ? s.vb : s.va) + (I - 16) * 1024);
  else s.fr[I] = lds128(lp(s.kb) + ((I - 32) & 1) * 8192 + ((I - 32) >> 1) * 1024);
}
template <int X> DI void mask_diag(St& s) {
  const unsigned ln = opaque_lane();
  const int dq = 32 * ((int)(s.w & 3) - 2 * (int)(s.w >> 2)) + (int)(ln & 31) - 4 * (int)(ln >> 5);
#pragma unroll
  for (int kb = 0; kb < 2; ++kb)
#pragma unroll
    for (int r = 0; r < 16; ++r) { const int c = 32 * kb + (r & 3) + 8 * (r >> 2); s.sc[X][kb][r] = c > dq ? -__builtin_inff() : s.sc[X][kb][r]; }
}
DI void sub_ip(f32x16& v, float d) {
#pragma unroll
  for (int r = 0; r < 16; ++r) { float e = v[r]; asm("v_sub_f32_e32 %0, %0, %1" : "+v"(e) : "v"(d)); v[r] = e; }
}
DI void mul_ip(f32x16& v, float d) {
#pragma unroll
  for (int r = 0; r < 16; ++r) { float e = v[r]; asm("v_mul_f32_e32 %0, %0, %1" : "+v"(e) : "v"(d)); v[r] = e; }
}
template <int X, int E> DI void sexp(St& s) { float e = __builtin_amdgcn_exp2f(s.sc[X][E >> 4][E & 15]); asm volatile("" : "+v"(e)); s.sc[X][E >> 4][E & 15] = e; }
template <int X, int E> DI void sadd(St& s) {
  float& ls = X ? s.ls1 : s.ls0; if constexpr (E == 0) ls = s.sc[X][0][0]; else { ls += s.sc[X][E >> 4][E & 15]; asm volatile("" : "+v"(ls)); } }
template <int X, int P> DI void scvt(St& s) { unsigned e = cvtpk(s.sc[X][P >> 3][(2 * P) & 15], s.sc[X][P >> 3][(2 * P + 1) & 15]); asm volatile("" : "+v"(e)); s.pf[P >> 2][P & 3] = e; }
DI void mg_stats(St& s, unsigned area, float m) {
  s.l = swap32_add(s.l);
  asm volatile("" : "+v"(m)); *(LAS f32x2*)(size_t)(area + s.w * 256u + (opaque_lane() & 31) * 8u) = f32x2{m, s.l}; }
template <int I> DI void mg_write(St& s, unsigned a) { const f32x16& v = s.o[2 + (I >> 2)]; *(LAS f32x4*)((LAS char*)(size_t)a + I * 1024) = f32x4{v[4 * (I & 3)], v[4 * (I & 3) + 1], v[4 * (I & 3) + 2], v[4 * (I & 3) + 3]}; }
DI void mg_read(St& s, unsigned a, unsigned area) {
  s.st = *(const LAS f32x2*)(size_t)(area + (s.w ^ 4u) * 256u + (opaque_lane() & 31) * 8u);
#pragma unroll
  for (int i = 0; i < 8; ++i) s.p[i] = *(const LAS f32x4*)(lp(a) + i * 1024);
}
DI void mg_coef(St& s, float m) {
  const float mm = __builtin_fmaxf(m, s.st[0]), fa = __builtin_amdgcn_exp2f(m - mm), fb = __builtin_amdgcn_exp2f(s.st[0] - mm);
  const float il = __builtin_amdgcn_rcpf(s.l * fa + s.st[1] * fb); s.ca = fa * il; s.cb = fb * il;
}
template <int I> DI void mg_final(St& s) {
  float v[8];
#pragma unroll
  for (int e = 0; e < 8; ++e) v[e] = s.oo[I >> 1][8 * (I & 1) + e] * s.ca + s.p[2 * I + (e >> 2)][e & 3] * s.cb;
  s.out[I] = u32x4{cvtpk(v[0], v[1]), cvtpk(v[2], v[3]), cvtpk(v[4], v[5]), cvtpk(v[6], v[7])};
  asm volatile("" : "+v"(s.out[I]));
}
template <int I> DI void mg_store(St& s, unsigned blk) {
  const unsigned ln = opaque_lane();
  st128m<ST2>(s.out[I], s.rC, ((ln >> 4) & 1) * 131072u + (ln & 15) * 16u + (ln >> 5) * 256u, s.sC0 + blk * 1048576u + I * 512u);
}
template <int X, int KIND, bool DIAG, int G> DI void aslot(St& s, int c) {
  constexpr int Y = X ^ 1; constexpr bool PREV = KIND != 0, FRESH = KIND == 0 || KIND == 2;
  if constexpr (G == 30) {
    wait_bar<KIND == 3 ? 5 : 1>();
    TSTAMP(s);
    s.kb ^= 32768u; s.va ^= 32768u; s.vb ^= 32768u; asm volatile("" : "+v"(s.kb), "+v"(s.va), "+v"(s.vb)); SBAR();
    frag<32>(s); frag<33>(s);
  } else if constexpr (G + 3 < 16) frag<G + 3>(s);
  else if constexpr (G + 3 < 32) { if constexpr (PREV) frag<G + 3>(s); }
  else if constexpr (G == 31) frag<34>(s);
  const f32x16 z = {};
  if constexpr (G < 16) s.sc[X][G & 1] = MFMA32(s.fr[G], s.qf[G >> 1], G < 2 ? (FRESH ? z : s.negm) : s.sc[X][G & 1]);
  else if constexpr (PREV) s.o[G & 3] = MFMA32(s.fr[G], __builtin_bit_cast(bf16x8, s.pf[(G - 16) >> 2]), (KIND == 3 && G < 20) ? z : s.o[G & 3]);
  if constexpr (KIND == 0) {
    if constexpr (G == 1) asm volatile("s_waitcnt lgkmcnt(0)\n\ts_barrier" ::: "memory");
    if constexpr (G >= 1 && G < 5) kvdma<G - 1>(s, mkdm(s, -1));
  } else if constexpr (KIND != 3) {
    if constexpr (G < 12 && (G & 1)) kvdma<(G >> 1) + 2>(s, mkdm(s, c - 1));
  }
  if constexpr (KIND == 3) {
    const Dm d = mkdm(s, c - 1);
    if constexpr (G == 0) { mg_stats(s, SP, s.mo); s.qw = (s.w < 4u ? d.m0k : d.m0v - 32768u) + s.w * 4096u + s.vl16; asm volatile("" : "+v"(s.qw)); }
    if constexpr (G >= 2 && G < 10) mg_write<G - 2>(s, s.qw);
    if constexpr (G == 10) {
      asm volatile("s_waitcnt lgkmcnt(0)\n\ts_barrier" ::: "memory");
      unsigned a = (s.w < 4u ? d.m0v : d.m0k - 16384u) + (s.w & 3) * 4096u + s.vl16; asm volatile("" : "+v"(a));
      mg_read(s, a, SP); s.oo[0] = s.o[0]; s.oo[1] = s.o[1];
    }
    if constexpr (G == 12) mg_coef(s, s.mo);
    if constexpr (G == 13) asm volatile("s_waitcnt lgkmcnt(0)\n\ts_barrier" ::: "memory");
    if constexpr (G >= 13 && G < 21) kvdma<G - 13>(s, d);
    if constexpr (G >= 13 && G < 25 && (G - 13) % 3 == 0) mg_final<(G - 13) / 3>(s);
    if constexpr (G >= 24 && G < 28) mg_store<G - 24>(s, s.n0 - 1u);
  }
  if constexpr (G == 28)
    asm volatile("buffer_load_dword %0, %1, %2, %3 offen" : "+v"(s.dummy) : "v"(s.vWm), "s"(s.rWm), "s"(s.sWm + c * 640u) : "memory");
  if constexpr (PREV) {
    if constexpr (G < 26) sexp<Y, G + 6>(s);
    if constexpr (G < 27) sadd<Y, G + 5>(s);
    if constexpr (G < 27 && !(G & 1)) scvt<Y, (G >> 1) + 2>(s);
    if constexpr (G == 27) { const float ls = Y ? s.ls1 : s.ls0; if constexpr (KIND == 3) s.l = ls; else s.l += ls; asm volatile("" : "+v"(s.l)); }
  }
  if constexpr (KIND == 0 && G == 16) asm volatile("s_nop 7\n\ts_nop 7" : "+v"(s.sc[X][0]), "+v"(s.sc[X][1]));
  if constexpr (DIAG && G == 16) {
    mask_diag<X>(s);
    unsigned qa = SP + (s.w & 3) * 8192u + s.vl16; asm volatile("" : "+v"(qa));
#pragma unroll
    for (int i = 0; i < 8; ++i) s.qf[i] = lds128(lp(qa) + i * 1024);
  }
  if constexpr (G >= 17 && G < 25) {
    constexpr int i = G - 17, kb = i >> 2, r = 4 * (i & 3);
    if constexpr (i == 0) { s.mx0 = max3f(s.sc[X][0][0], s.sc[X][0][1], s.sc[X][0][1]); s.mx1 = max3f(s.sc[X][0][2], s.sc[X][0][3], s.sc[X][0][3]); }
    else { s.mx0 = max3f(s.mx0, s.sc[X][kb][r], s.sc[X][kb][r + 1]); s.mx1 = max3f(s.mx1, s.sc[X][kb][r + 2], s.sc[X][kb][r + 3]); }
    asm volatile("" : "+v"(s.mx0), "+v"(s.mx1));
  }
  if constexpr (G == 25) {
    const float mx = max3f(s.mx0, s.mx1, s.mx1);
    if constexpr (FRESH) {
      const float rm = __builtin_fmaxf(swap32_max(mx), -1e30f);
      if constexpr (KIND == 2) s.mo = s.m; else s.l = 0.f;
      s.m = rm; sub_ip(s.sc[X][0], rm); sub_ip(s.sc[X][1], rm);
#pragma unroll
      for (int r = 0; r < 16; ++r) s.negm[r] = -rm;
    } else if (__builtin_expect(__any(mx > THR), 0)) {
      const float dl = __builtin_fmaxf(swap32_max(mx), 0.f);
      s.m += dl; s.f = __builtin_amdgcn_exp2f(-dl); s.pend = true;
      sub_ip(s.sc[X][0], dl); sub_ip(s.sc[X][1], dl);
#pragma unroll
      for (int r = 0; r < 16; ++r) { float e = s.negm[r]; asm("v_sub_f32_e32 %0, 0, %1" : "+v"(e) : "v"(s.m)); s.negm[r] = e; }
    }
  }
  if constexpr (G >= 26) sexp<X, G - 26>(s);
  if constexpr (G >= 27) sadd<X, G - 27>(s);
  if constexpr (G == 28 || G == 30) scvt<X, ((G - 28) >> 1)>(s);
  if constexpr (KIND != 2 && G >= 30) kvdma<G - 30>(s, mkdm(s, c));
  SBAR();
}
template <int X, int KIND, bool DIAG, int... Gs> DI void astep_(St& s, int c, iseq<Gs...>) {
  if constexpr (KIND == 0) { frag<32>(s); frag<33>(s); frag<34>(s); SBAR(); }
  s.fr[0] = s.fr[32]; s.fr[1] = s.fr[33]; s.fr[2] = s.fr[34];
  (aslot<X, KIND, DIAG, Gs>(s, c), ...);
  if constexpr (KIND == 1 || KIND == 3) {
    if (__builtin_expect(s.pend, 0)) {
      asm volatile("s_nop 7");
      mul_ip(s.o[0], s.f); mul_ip(s.o[1], s.f); mul_ip(s.o[2], s.f); mul_ip(s.o[3], s.f);
      s.l *= s.f;
      s.pend = false;
    }
  }
}
template <int X, int KIND, bool DIAG = false> DI void astep(St& s, int c) { astep_<X, KIND, DIAG>(s, c, mkseq<32>{}); }
template <int T> DI void tslot(St& s) {
  if constexpr (T == 0) { frag<16>(s); frag<17>(s); frag<18>(s); }
  else if constexpr (T + 17 < 32 && T >= 2) frag<T + 17>(s);
  if constexpr (T >= 2) s.o[(T - 2) & 3] = MFMA32(s.fr[T + 14], __builtin_bit_cast(bf16x8, s.pf[(T - 2) >> 2]), s.o[(T - 2) & 3]);
  if constexpr (T < 13) { sexp<1, 6 + 2 * T>(s); sexp<1, 7 + 2 * T>(s); }
  if constexpr (T > 0 && T < 14) sadd<1, 4 + 2 * T>(s);
  if constexpr (T < 14) { sadd<1, 5 + 2 * T>(s); scvt<1, T + 2>(s); }
  if constexpr (T == 14) { s.l += s.ls1; mg_stats(s, VR + 32768u, s.m); }
  SBAR();
}
template <int... Ts> DI void atail(St& s, iseq<Ts...>) { (tslot<Ts>(s), ...); }
template <int... Is> DI void mg_write_all(St& s, unsigned a, iseq<Is...>) { (mg_write<Is>(s, a), ...); }
}

__global__ __launch_bounds__(512, 2) void k2_attn(const u16* __restrict__ wq, const u16* __restrict__ wo, const u16* __restrict__ wg, const u16* __restrict__ wu, const u16* __restrict__ wd, char* __restrict__ ws) {
  using namespace k2;
  const unsigned tid = threadIdx.x, lane = tid & 63, w = rfl(tid >> 6), hi = lane >> 5, rq = w & 3, g = w >> 2;
  const unsigned b = blockIdx.x, xcd = b & 7, ix = b >> 3, h = xcd * 4 + (ix & 3), x = ix >> 2;
  St s;
  s.w = w; s.vl16 = lane * 16u;
  TSTAMP(s);
  const unsigned q2 = (lane >> 2) & 3, p2 = lane & 3, g4 = lane >> 4;
  s.rW = mksrd(wq, DM * HDM * 2); s.rX = mksrd(ws + WS_XN, SEQ * DM * 2); s.rK = mksrd(ws + WS_KF, SEQ * HDM * 2); s.rV = mksrd(ws + WS_VF, SEQ * HDM * 2);
  s.rC = mksrd(ws + WS_CTX, SEQ * HDM * 2);
  s.vW = q2 * 8192u + g4 * 64u + p2 * 16u; s.sW = h * 256u + w * 65536u;
  s.wq_rd = QR + hi * 2048u + q2 * 64u + (g4 & 1) * 32u + p2 * 8u; asm volatile("" : "+v"(s.wq_rd));
  s.sX = ((g == 0 ? x : 15u - x) * 4u + rq) * 36864u;
  s.sKV = h * 524288u + w * 4096u; s.n0 = x + 1u; s.sC0 = (2u * rq * 128u + 4u * h + 2u * g) * 1024u;
  s.qw = (g == 0 ? KR + 32768u : SP) + rq * 8192u + s.vl16; asm volatile("" : "+v"(s.qw));
  qload_all<0>(s, mkseq<6>{});
  __builtin_amdgcn_s_barrier();
  qload_all<1>(s, mkseq<6>{});
  qbar<0>(s); SBAR(); TSTAMP(s);
  qread<0>(s); qread<1>(s); qread<2>(s); SBAR();
  qrun(s, mkseq<144>{});
  qpack<5>(s); qpack<6>(s); qpack<7>(s);
  TSTAMP(s);
  wait_bar<0>();
  if (g == 1) {
    unsigned ra = KR + 32768u + rq * 8192u + s.vl16; asm volatile("" : "+v"(ra));
#pragma unroll
    for (int i = 0; i < 8; ++i) s.qf[i] = lds128(lp(ra) + i * 1024);
  }
  { const Dm d = mkdm(s, -1); kvdma<4>(s, d); kvdma<5>(s, d); kvdma<6>(s, d); kvdma<7>(s, d); }
  {
    const unsigned W = b * 8u + w;
    s.rWm = mksrd(W < 434u ? wo : W < 922u ? wg : W < 1410u ? wu : wd, W < 434u ? HDM * DM * 2 : DM * FF * 2);
    s.sWm = (W < 434u ? W : W < 922u ? W - 434u : W < 1410u ? W - 922u : W - 1410u) * 10880u;
    const unsigned l = opaque_lane(); s.vWm = l < 5u ? l * 128u : 0x10000000u;
    s.dummy = 0;
  }
  s.kb = KR + g * 16384u + s.vl16; s.va = VR + 32768u + g * 18432u + s.vl16; s.vb = VR + 32768u + g * 14336u + s.vl16; s.pend = false;
  asm volatile("" : "+v"(s.kb), "+v"(s.va), "+v"(s.vb));
#pragma unroll
  for (int d = 0; d < 4; ++d) s.o[d] = f32x16{};
  { const unsigned l = opaque_lane(), e = (((l >> 2) ^ (l >> 4)) & 1) ? 0u : 0x3f803f80u; s.sel = __builtin_bit_cast(bf16x8, u32x4{e, e, e, e}); s.la = f32x4{}; }
  TSTAMP(s);
  if (x == 0) astep<1, 0, true>(s, 0); else astep<0, 0>(s, 0);
  int c = 1;
  for (int pass = 0; pass < 2; ++pass) {
    if (pass == 1 || x != 0) {
      const int cend = pass == 0 ? (int)x : 16;
      for (; c + 1 < cend; c += 2) { astep<1, 1>(s, c); astep<0, 1>(s, c + 1); }
      if (c < cend) { astep<1, 1>(s, c); ++c; s.sc[0][0] = s.sc[1][0]; s.sc[0][1] = s.sc[1][1]; s.ls0 = s.ls1; }
      astep<1, 1, true>(s, c); ++c;
    }
    if (pass == 0) { astep<0, 2>(s, c); astep<1, 3>(s, c + 1); astep<0, 1>(s, c + 2); c += 3; }
  }
  TSTAMP(s);
  atail(s, mkseq<18>{});
  TSTAMP(s);
  {
    unsigned mine = (g == 0 ? SP : KR + 32768u) + rq * 8192u + s.vl16, theirs = (g == 0 ? KR + 32768u : SP) + rq * 8192u + s.vl16; asm volatile("" : "+v"(mine), "+v"(theirs));
    mg_write_all(s, mine, mkseq<8>{});
    asm volatile("s_waitcnt lgkmcnt(0)\n\ts_barrier" ::: "memory");
    TSTAMP(s);
    mg_read(s, theirs, VR + 32768u); s.oo[0] = s.o[0]; s.oo[1] = s.o[1];
    mg_coef(s, s.m);
    mg_final<0>(s); mg_store<0>(s, 15u - x); mg_final<1>(s); mg_store<1>(s, 15u - x); mg_final<2>(s); mg_store<2>(s, 15u - x); mg_final<3>(s); mg_store<3>(s, 15u - x);
  }
  TSTAMP(s);
  wait_vm<4>();
  asm volatile("" ::"v"(s.dummy));
}

namespace k3 {
#define K3_SOLO 24
constexpr int SOLO = K3_SOLO, NQS = SOLO / 4, P = 3, WSLOT = 4096, RING = 102400, CNT = RING + 4 * P * WSLOT, LDS_BYTES = 163840, GRID = 256;
DI const LAS char* lp(unsigned a) { return (const LAS char*)(size_t)a; }
DI unsigned olane() { unsigned l; asm volatile("v_mbcnt_lo_u32_b32 %0, -1, 0\n\tv_mbcnt_hi_u32_b32 %0, -1, %0" : "=v"(l)); return l; }
struct St {
  bf16x8 af[P][5], wf[4]; f32x4 acc[4][5];
  unsigned vl16, vW, rd[4], m0b, sA[5], sW, kA, kB, kq, c0, c1, f0, f1; char* ws;
  srd_t rA, rW;
  unsigned tsv, nts;
};
constexpr int nops(bool own, int nsh, int i) { return i < nsh ? 7 : (own && i < nsh + NQS ? 9 : 0); }
constexpr int NEARLY = 8;
template <bool FIRST, int NSH, int I> DI unsigned kof(const St& s) {
  if constexpr (FIRST) return (I < NSH ? s.kA : s.kB) + I;
  else if constexpr (I < NEARLY) { const unsigned e = s.kA + I; return 9u * (e >> 2) + (e & 3) + ((e & 2) ? 3u : 0u); }
  else { const unsigned l = (I < NSH ? s.kq : s.kB) + I, q = (l * 205u) >> 10, r = l - 5u * q; return 9u * q + 2u + r + (r >= 3u ? 2u : 0u); }
}
template <bool FIRST, bool OWN, int NSH, int I, int J> DI void load(St& s) {
  constexpr int sl = I % P, nw = nops(OWN, NSH, I) - 5;
  if constexpr (J < nops(OWN, NSH, I)) {
    const unsigned k = kof<FIRST, NSH, I>(s);
    if constexpr (J < nw) { constexpr unsigned pj = (I < NSH && !OWN) ? J + 2 : J; dma16(s.m0b + sl * WSLOT + pj * 1024u, s.vW, s.rW, s.sW + (k * 32u + pj * 8u) * (DM * 2u)); }
    else if constexpr (FIRST) asm volatile("buffer_load_dwordx4 %0, %1, %2, %3 offen" : "=v"(s.af[sl][J - nw]) : "v"(s.vl16), "s"(s.rA), "s"(s.sA[J - nw] + k * 1024u) : "memory");
    else asm volatile("buffer_load_dwordx4 %0, %1, %2, %3 offen sc1" : "=v"(s.af[sl][J - nw]) : "v"(s.vl16), "s"(s.rA), "s"(s.sA[J - nw] + k * 1024u) : "memory");
  }
}
template <bool FIRST, bool OWN, int NSH, int I, int J0, int... Js> DI void load_all(St& s, iseq<Js...>) { (load<FIRST, OWN, NSH, I, J0 + Js>(s), ...); }
template <bool FIRST, bool OWN, int NSH, int I> DI void step(St& s) {
  constexpr int sl = I % P;
  asm volatile("s_waitcnt vmcnt(%5)" : "+v"(s.af[sl][0]), "+v"(s.af[sl][1]), "+v"(s.af[sl][2]), "+v"(s.af[sl][3]), "+v"(s.af[sl][4]) : "i"(I == 0 ? 5 : nops(OWN, NSH, I + 1)) : "memory");
  if constexpr (!FIRST && OWN && I == NEARLY - 2) {
    if (s.m0b == (unsigned)RING) { asm volatile("" : "+v"(s.f0), "+v"(s.f1)); if (rfl(s.f0) < 32u) await(s.ws, s.c0 + 8u, 32u); if (rfl(s.f1) < 32u) await(s.ws, s.c1 + 8u, 32u); }
  }
  if constexpr (I < NSH + 1) asm volatile("s_barrier" ::: "memory");
  if constexpr (!FIRST && OWN && I == 0) { if (s.m0b == (unsigned)RING && s.vl16 == 0u) arrive(s.ws, 24u + (blockIdx.x >> 5)); }
  if constexpr (!FIRST && OWN && I == NEARLY - 4) {
    if (s.m0b == (unsigned)RING) asm volatile("global_load_dword %0, %2, off sc1\n\tglobal_load_dword %1, %3, off sc1" : "=&v"(s.f0), "=&v"(s.f1)
                                              : "v"(s.ws + WS_CNT + (s.c0 + 8u) * 128u), "v"(s.ws + WS_CNT + (s.c1 + 8u) * 128u) : "memory");
  }
  if constexpr ((I & 3) == 0) TSTAMP(s);
#pragma unroll
  for (int t = 0; t < 4; ++t) s.wf[t] = cat8(ldstr(lp(s.rd[t]) + sl * WSLOT), ldstr(lp(s.rd[t]) + sl * WSLOT + 1024));
  SBAR();
#define K3_MM(t, r) do { if constexpr (I == 0) MFMA16_NEW(s.acc[t][r], s.wf[t], s.af[sl][r]); else MFMA16_ACC(s.acc[t][r], s.wf[t], s.af[sl][r]); } while (0)
#define K3_LD(j) load<FIRST, OWN, NSH, I + 2, j>(s)
  K3_LD(0); K3_MM(0, 0); K3_MM(0, 1); SBAR(); K3_LD(1); K3_MM(0, 2); K3_MM(0, 3); SBAR();
  K3_LD(2); K3_MM(0, 4); K3_MM(1, 0); SBAR(); K3_LD(3); K3_MM(1, 1); K3_MM(1, 2); SBAR();
  K3_LD(4); K3_MM(1, 3); K3_MM(1, 4); SBAR(); K3_LD(5); K3_MM(2, 0); K3_MM(2, 1); SBAR();
  K3_LD(6); K3_MM(2, 2); K3_MM(2, 3); SBAR(); K3_LD(7); K3_MM(2, 4); K3_MM(3, 0); SBAR();
  K3_LD(8); K3_MM(3, 1); K3_MM(3, 2); K3_MM(3, 3); K3_MM(3, 4); SBAR();
#undef K3_MM
#undef K3_LD
}
template <bool FIRST, bool OWN, int NSH, int... Is> DI void run(St& s, iseq<Is...>) {
  load_all<FIRST, OWN, NSH, 0, 0>(s, mkseq<2>{}); load_all<FIRST, OWN, NSH, 1, 0>(s, mkseq<2>{});
  if constexpr (!FIRST) {
    if (OWN && s.m0b == (unsigned)RING) { await(s.ws, s.c0, 32u); await(s.ws, s.c1, 32u); }
    asm volatile("s_barrier" ::: "memory");
  }
  load_all<FIRST, OWN, NSH, 0, 2>(s, mkseq<5>{}); __builtin_amdgcn_s_barrier(); load_all<FIRST, OWN, NSH, 1, 2>(s, mkseq<5>{});
  (step<FIRST, OWN, NSH, Is>(s), ...);
  asm volatile("s_nop 7" ::: "memory"); SBAR();
}
DI void park(const St& s, unsigned kq, unsigned lane) {
#pragma unroll
  for (int t = 0; t < 4; ++t) {
    unsigned wa = kq * 20480u + (lane & 15) * 256u + (((4u * t + (lane >> 4)) ^ (lane & 15)) << 4); asm volatile("" : "+v"(wa));
#pragma unroll
    for (int r = 0; r < 5; ++r) *(LAS f32x4*)((LAS char*)(size_t)wa + r * 4096) = s.acc[t][r];
  }
}
DI void repack(const u16* wg, const u16* wu, char* ws, unsigned wgi, unsigned w, unsigned lane) {
  const unsigned task = wgi * 8u + w, blk = task >> 1, half = task & 1;
  const bool on = blk < 72u;
  const srd_t rG = mksrd(wg, DM * FF * 2), rU = mksrd(wu, DM * FF * 2), rP = mksrd(ws + WS_WGU, DM * FF * 4);
  const unsigned base = w * 20480u, row = lane >> 3;
  const unsigned vg = row * (FF * 2u) + (((lane & 7) ^ (((row >> 1) & 1) << 2)) << 4), vu = vg ^ 32u;
  const unsigned ssrc = on ? blk * 128u + half * 288u * (FF * 2u) : 0x10000000u;
  unsigned sdst[4];
#pragma unroll
  for (int p = 0; p < 4; ++p) sdst[p] = (on && (blk * 4u + p) % 9u >= 4u) ? (blk * 4u + p) * 36864u + half * 18432u : 0x10000000u;
  const unsigned hi = lane >> 5, mat = (lane >> 4) & 1, q = (lane >> 2) & 3;
  const unsigned rd = base + mat * 2048u + (8u * hi + q) * 128u + (((q >> 1) << 6) ^ (mat << 5)) + (lane & 3) * 8u;
  const bf16x8 ones = {0x3f80, 0x3f80, 0x3f80, 0x3f80, 0x3f80, 0x3f80, 0x3f80, 0x3f80};
  f32x16 cs[4] = {};
  auto fetch = [&](unsigned st, unsigned slot) {
#pragma unroll
    for (int j = 0; j < 4; ++j) dma16(base + slot * 4096u + j * 1024u, j < 2 ? vg : vu, j < 2 ? rG : rU, ssrc + (st * 16u + (j & 1) * 8u) * (FF * 2u));
  };
#pragma unroll
  for (int i = 0; i < 4; ++i) fetch(i, i);
  for (unsigned st = 0; st < 18u; ++st) {
    const unsigned slot = st % 5u;
    if (st < 4u) wait_vm<12>(); else wait_vm<24>();
    bf16x8 f[4];
#pragma unroll
    for (int p = 0; p < 4; ++p) { const unsigned a = (rd + slot * 4096u) ^ (p << 5); f[p] = cat8(ldstr(lp(a)), ldstr(lp(a) + 512)); }
    fetch(st + 4u < 18u ? st + 4u : st, (st + 4u) % 5u);
#pragma unroll
    for (int p = 0; p < 4; ++p) { cs[p] = MFMA32(f[p], ones, cs[p]); st128m<0>(__builtin_bit_cast(u32x4, f[p]), rP, lane * 16u, sdst[p] + st * 1024u); }
  }
  wait_vm<0>();
  if ((lane & 31) == 0) {
#pragma unroll
    for (int p = 0; p < 4; ++p)
#pragma unroll
      for (int r4 = 0; r4 < 4; ++r4) *(LAS f32x4*)(size_t)(base + p * 128u + r4 * 32u + hi * 16u) = f32x4{cs[p][4 * r4], cs[p][4 * r4 + 1], cs[p][4 * r4 + 2], cs[p][4 * r4 + 3]};
  }
  asm volatile("s_waitcnt lgkmcnt(0)\n\ts_barrier" ::: "memory");
  if (on && half == 0) {
    const f32x2 a = *(const LAS f32x2*)(size_t)(base + lane * 8u), c = *(const LAS f32x2*)(size_t)(base + 20480u + lane * 8u);
    *(f32x2*)(ws + WS_CS + blk * 512u + lane * 8u) = a + c;
  }
  wait_bar<0>();
  if (wgi < 18u && w == 0u) {
    __builtin_amdgcn_fence(__ATOMIC_RELEASE, "agent");
    asm volatile("s_waitcnt vmcnt(0)" ::: "memory");
    if (lane == 0) arrive(ws, 8u);
  }
}
template <int NK32, bool FIRST> DI unsigned body(const char* A, const u16* W, const u16* x, const u16* wg, const u16* wu, char* ws, u16* out) {
  constexpr int H = (NK32 - SOLO) / 2, NSH = H / 4;
  static_assert(H % 4 == 0, "quarters");
  const unsigned w = rfl(threadIdx.x >> 6), lane = olane(), tid = w * 64u + lane, g = lane >> 4, q2 = (lane >> 2) & 3, p2 = lane & 3, kq = w & 3, rh = w >> 2;
  const unsigned b = blockIdx.x, side = b & 1, id = ((b & 7) >> 1) * 30u + (b >> 3);
  if (b >= 240u || id >= 117u) {
    if constexpr (FIRST) repack(wg, wu, ws, b >= 240u ? b - 234u : (id - 117u) * 2u + side, w, lane);
    else { wait_bar<0>(); if (tid == 0) arrive(ws, 24u + (b >> 5)); }
    return 0xffu;
  }
  const unsigned t160 = id / 9u, cb = id - t160 * 9u, rbk = t160 * 2u + side;
  St s;
  TSTAMP(s);
  s.rA = mksrd(A, SEQ * NK32 * 64); s.rW = mksrd(W, NK32 * 32 * DM * 2);
  s.vl16 = lane * 16u;
  s.vW = (lane >> 3) * (DM * 2u) + (((lane & 7) ^ (((lane >> 4) & 3) << 1)) << 4);
  s.m0b = RING + kq * (P * WSLOT); s.sW = cb * 128u;
  if constexpr (FIRST) { s.kA = side * (NK32 - H) + kq * NSH; s.kB = H + kq * NQS - NSH; }
  else { s.kA = (side * 4u + kq) * NEARLY; s.kq = (side * 4u + kq) * (NSH - NEARLY) - NEARLY; s.kB = 8u * (NSH - NEARLY) + kq * NQS - NSH; }
  s.ws = ws; s.c0 = 16u + (t160 * 160u >> 8); s.c1 = 16u + (t160 == 12u ? 7u : (t160 * 160u + 159u) >> 8); s.f0 = 0; s.f1 = 0;
#pragma unroll
  for (int r = 0; r < 5; ++r) s.sA[r] = ((rbk ^ rh) * 5u + r) * (NK32 * 1024u);
  const unsigned krow = 16u * (g >> 1) + 4u * (g & 1) + q2;
#pragma unroll
  for (int t = 0; t < 4; ++t) { s.rd[t] = s.m0b + krow * 128u + ((((krow >> 1) & 3) ^ t) << 5) + p2 * 8u; asm volatile("" : "+v"(s.rd[t])); }
  if (tid < 2u) *(LAS unsigned*)(size_t)(CNT + tid * 4u) = 0u;
  const unsigned pc = tid & 7, m0 = tid >> 3;
  unsigned vrm[2], vfr[2];
#pragma unroll
  for (int i = 0; i < 2; ++i) {
    const unsigned m = m0 + 64u * i, row = rbk * 80u + m; const bool ok = m < 80u && row < (unsigned)SEQ;
    vrm[i] = ok ? row * (DM * 2u) + cb * 128u + pc * 16u : 0x10000000u;
    vfr[i] = ok ? (row >> 5) * 36864u + (cb * 8u + pc) * 512u + (row & 31) * 16u : 0x10000000u;
  }
  u32x4 res[2];
  {
    const srd_t rR = FIRST ? mksrd(x, SEQ * DM * 2) : mksrd(ws + WS_H, SEQ * DM * 2);
    asm volatile("buffer_load_dwordx4 %0, %2, %4, 0 offen\n\tbuffer_load_dwordx4 %1, %3, %4, 0 offen"
                 : "=&v"(res[0]), "=&v"(res[1]) : "v"(FIRST ? vrm[0] : vfr[0]), "v"(FIRST ? vrm[1] : vfr[1]), "s"(rR) : "memory");
  }
  asm volatile("s_waitcnt lgkmcnt(0)" ::: "memory");
  if (rh == 0) {
    run<FIRST, true, NSH>(s, mkseq<NSH + NQS>{});
    TSTAMP(s);
    while (rfl(__hip_atomic_load((LAS unsigned*)(size_t)CNT, __ATOMIC_RELAXED, __HIP_MEMORY_SCOPE_WORKGROUP)) < 4u) __builtin_amdgcn_s_sleep(1);
    asm volatile("" ::: "memory");
    park(s, kq, lane);
  } else {
    run<FIRST, false, NSH>(s, mkseq<NSH>{});
    asm volatile("s_barrier" ::: "memory");
    park(s, kq, lane);
    asm volatile("s_waitcnt lgkmcnt(0)" ::: "memory");
    if (lane == 0) __hip_atomic_fetch_add((LAS unsigned*)(size_t)(CNT + 4u), 1u, __ATOMIC_RELAXED, __HIP_MEMORY_SCOPE_WORKGROUP);
    asm volatile("s_waitcnt lgkmcnt(0)" ::: "memory");
    while (rfl(__hip_atomic_load((LAS unsigned*)(size_t)(CNT + 4u), __ATOMIC_RELAXED, __HIP_MEMORY_SCOPE_WORKGROUP)) < 4u) __builtin_amdgcn_s_sleep(1);
    asm volatile("" ::: "memory");
    const srd_t rX = mksrd(ws + (FIRST ? WS_XB : WS_XB + 234u * 20480u), 234u * 20480u);
    unsigned* flags = (unsigned*)(ws + WS_FLAG) + (FIRST ? 0 : 8192);
    unsigned ca = (tid & 255u) * 16u; asm volatile("" : "+v"(ca));
#pragma unroll
    for (int i = 0; i < 5; ++i) {
      const f32x4 v = (*(const LAS f32x4*)(lp(ca) + i * 4096) + *(const LAS f32x4*)(lp(ca) + 20480 + i * 4096)) + (*(const LAS f32x4*)(lp(ca) + 40960 + i * 4096) + *(const LAS f32x4*)(lp(ca + 61440u) + i * 4096));
      st128m<1>(__builtin_bit_cast(u32x4, v), rX, ca, (id * 2u + side) * 20480u + i * 4096u);
    }
    asm volatile("s_waitcnt vmcnt(0)" ::: "memory");
    unsigned last = 0;
    if (lane == 0) last = __hip_atomic_fetch_add((LAS unsigned*)(size_t)CNT, 1u, __ATOMIC_RELAXED, __HIP_MEMORY_SCOPE_WORKGROUP);
    if (lane == 0 && last == 3u) __hip_atomic_store(flags + (id * 2u + side) * 32u, 1u, __ATOMIC_RELAXED, __HIP_MEMORY_SCOPE_AGENT);
    TSTAMP(s);
    for (unsigned spin = 0; spin < (1u << 22); ++spin) {
      if (rfl(__hip_atomic_load(flags + (id * 2u + (side ^ 1u)) * 32u, __ATOMIC_RELAXED, __HIP_MEMORY_SCOPE_AGENT)) != 0u) break;
      __builtin_amdgcn_s_sleep(2);
    }
    u32x4 pv[5];
    asm volatile("buffer_load_dwordx4 %0, %5, %6, %7 offen sc1\n\tbuffer_load_dwordx4 %1, %5, %6, %8 offen sc1\n\tbuffer_load_dwordx4 %2, %5, %6, %9 offen sc1\n\t"
                 "buffer_load_dwordx4 %3, %5, %6, %10 offen sc1\n\tbuffer_load_dwordx4 %4, %5, %6, %11 offen sc1\n\ts_waitcnt vmcnt(0)"
                 : "=&v"(pv[0]), "=&v"(pv[1]), "=&v"(pv[2]), "=&v"(pv[3]), "=&v"(pv[4])
                 : "v"(ca), "s"(rX), "s"((id * 2u + (side ^ 1u)) * 20480u), "s"((id * 2u + (side ^ 1u)) * 20480u + 4096u), "s"((id * 2u + (side ^ 1u)) * 20480u + 8192u),
                   "s"((id * 2u + (side ^ 1u)) * 20480u + 12288u), "s"((id * 2u + (side ^ 1u)) * 20480u + 16384u) : "memory");
#pragma unroll
    for (int i = 0; i < 5; ++i) *(LAS u32x4*)((LAS char*)(size_t)(ca + 81920u) + i * 4096) = pv[i];
    TSTAMP(s);
  }
  asm volatile("s_waitcnt lgkmcnt(0)\n\ts_barrier" : "+v"(res[0]), "+v"(res[1])::"memory");
  TSTAMP(s);
  const srd_t rO = FIRST ? mksrd(ws + WS_H, SEQ * DM * 2) : mksrd(out, SEQ * DM * 2);
#pragma unroll
  for (int i = 0; i < 2; ++i) {
    if (i == 1 && w >= 2) break;
    unsigned ra = (m0 + 64u * i) * 256u + (((2u * pc) ^ (m0 & 15)) << 4), rb = ra + 61440u; asm volatile("" : "+v"(ra), "+v"(rb));
    f32x4 a = *(const LAS f32x4*)(lp(ra)), c = *(const LAS f32x4*)(lp(ra ^ 16u));
#pragma unroll
    for (int ww = 1; ww < 5; ++ww) { const unsigned base = ww < 3 ? ra : rb; a += *(const LAS f32x4*)(lp(base) + (ww % 3) * 20480); c += *(const LAS f32x4*)(lp(base ^ 16u) + (ww % 3) * 20480); }
    const unsigned y[4] = {cvtpk(a[0], a[1]), cvtpk(a[2], a[3]), cvtpk(c[0], c[1]), cvtpk(c[2], c[3])};
    u32x4 o;
#pragma unroll
    for (int e = 0; e < 4; ++e) o[e] = cvtpk(bflo(res[i][e]) + bflo(y[e]), bfhi(res[i][e]) + bfhi(y[e]));
    st128m<FIRST ? ST3 : ST5>(o, rO, FIRST ? vfr[i] : vrm[i], 0);
    if constexpr (FIRST) {
      float sm = 0.f;
#pragma unroll
      for (int e = 0; e < 4; ++e) sm += bflo(o[e]) + bfhi(o[e]);
      const float mean = dpp_add<0x141>(dpp_add<0x4E>(dpp_add<0xB1>(sm))) * (1.f / 64.f);
      float q = 0.f;
#pragma unroll
      for (int e = 0; e < 4; ++e) { const float d0 = bflo(o[e]) - mean, d1 = bfhi(o[e]) - mean; q += d0 * d0 + d1 * d1; }
      q = dpp_add<0x141>(dpp_add<0x4E>(dpp_add<0xB1>(q)));
      const unsigned m = m0 + 64u * i, row = rbk * 80u + m;
      if (pc == 0 && m < 80u && row < (unsigned)SEQ)
        __hip_atomic_store((unsigned long long*)(ws + WS_ST + (row * 9u + cb) * 8u), ((unsigned long long)__float_as_uint(q) << 32) | __float_as_uint(mean), __ATOMIC_RELAXED, __HIP_MEMORY_SCOPE_AGENT);
    }
  }
  return rbk;
}
}

namespace k4 {
constexpr int SLOT = 36864, CSOFF = 4 * SLOT, LDS_BYTES = CSOFF + 2048, LA = 3, BB = 2, NU = 324;
DI const LAS char* lp(unsigned a) { return (const LAS char*)(size_t)a; }
constexpr int u_pair(int g) { return g < 112 ? g & 3 : g < 144 ? (g - 112) >> 3 : 4 + (g - 144) / 36; }
constexpr int u_kstep(int g) { return g < 112 ? g >> 2 : g < 144 ? 28 + (g & 7) : (g - 144) % 36; }
constexpr int pair_end(int p) { return p < 4 ? 119 + 8 * p : 144 + 36 * (p - 4) + 35; }
constexpr int NE = 13;
constexpr int eks(int e) { return e < 8 ? 2 * e : 18; }
constexpr int estart(int e) { return e < 8 ? 16 * e : 144 + 36 * (e - 8); }
constexpr int bslot(int e) { return estart(e) - BB; }
constexpr int epoch_of(int g) { int e = 0; while (e + 1 < NE && estart(e + 1) <= g) ++e; return e; }
#define K4_WPRE 9
constexpr int WPRE = K4_WPRE, NPRO = 8, RATE = 2, NSL = 54 - WPRE;
constexpr int pos_end(int ks) { return 2 * ks + (ks > WPRE ? ks - WPRE : 0); }
constexpr int sop(int i) { int ks = 0; while (pos_end(ks + 1) <= i) ++ks; return 3 * ks + (i - pos_end(ks)); }
constexpr int s_lo(int g) { const int n = NPRO + (g + RATE - 1) / RATE; return n < NSL ? n : NSL; }
constexpr int SEND = 84, WPEEK = 30, WCHK = 64;
static_assert(s_lo(SEND) == NSL && SEND > bslot(5), "late ops follow the stream and the weights' acquire");
constexpr int cfirst(int p) { return p == 4 ? SEND + 1 : p == 5 ? SEND + 16 : p == 6 ? bslot(7) + 3 : p == 7 ? bslot(8) + 1 : bslot(9) + 1; }
static_assert(cfirst(4) > bslot(3) && cfirst(5) > bslot(5), "slot reuse");
constexpr int op_at(int g) {
  for (int p = 4; p < 9; ++p) { const int j = g - cfirst(p); if (j >= 0 && j < 15 && j % 3 == 0) return 100 + 8 * (p - 4) + j / 3; }
  if (g == SEND) return 204;
  return -1;
}
constexpr int NEP = 30, SP = 24;
constexpr int ep_start(int p) { int s = 0; for (int q = 0; q <= p; ++q) { const int a = pair_end(q) + 3; s = (q == 0 || a > s + SP) ? a : s + SP; } return s; }
static_assert(ep_start(7) + NEP <= NU, "epilogues must fit");
constexpr int st_ops(int g) { for (int p = 0; p < 8; ++p) if (g == ep_start(p) + NEP - 2) return 1; return 0; }
constexpr bool sure(int op) { return op >= 0 && !(op >= 100 && op < 200 && ((op - 100) & 7) == 4) && op != 204; }
constexpr int vm_wait(int e) {
  const int to = e ? bslot(e) : 0;
  if (e == 12) return 1;
  if (e < 7) { int n = s_lo(to) - pos_end(eks(e + 1)); for (int g = 0; g < to; ++g) n += sure(op_at(g)) ? 1 : 0; return n; }
  const int want = e == 7 ? 204 : 100 + 8 * (e - 8) + 4;
  int from = 0; while (op_at(from) != want) ++from;
  int n = st_ops(from);
  for (int g = from + 1; g < to; ++g) n += (sure(op_at(g)) ? 1 : 0) + st_ops(g);
  return n;
}
constexpr bool sched_ok() { for (int e = 1; e < 7; ++e) if (s_lo(bslot(e)) < pos_end(eks(e + 1))) return false; return SEND < bslot(7); }
static_assert(sched_ok(), "stream ops must precede their barriers");
constexpr int ST0 = 2;
DI unsigned olane() { unsigned l; asm volatile("v_mbcnt_lo_u32_b32 %0, -1, 0\n\tv_mbcnt_hi_u32_b32 %0, -1, %0" : "=v"(l)); return l; }
struct St {
  bf16x8 hf[36], wf[NU]; f32x16 acc[9];
  f32x4 sp0, sp1; f32x2 sq, csg, csu, csg2, csu2; float ag, au, x0, x1, t0, t1, d0, o0, o1; u32x4 pk;
  float nmean, c1, c2;
  unsigned w, wp, wp1, wbA, vA, sA, sW, sHh, csb, vl16, vst, sAct, vsp, p0, rb, wr; char* ws;
  srd_t rR, rW, rH, rS, rCS, rAct;
  unsigned tsv, nts;
};
template <int OP> DI void vop(St& s) {
  if constexpr (OP < 54) {
    constexpr int ks = OP / 3, j = OP % 3;
    if constexpr (j < 2) asm volatile("buffer_load_dwordx4 %0, %1, %2, %3 offen offset:%4 sc1" : "=v"(s.hf[2 * ks + j]) : "v"(s.vl16), "s"(s.rH), "s"(s.sHh + (ks >> 1) * 4096u), "i"(((ks & 1) * 2 + j) * 1024) : "memory");
    else dma16(ks * 8192u + s.w * 1024u, s.vA, s.rR, s.sA + ks * 32u * (FF * 2u));
  } else if constexpr (OP < 200) {
    constexpr int P = 4 + ((OP - 100) >> 3), J = (OP - 100) & 7;
    if (J < 4 || s.w < 4u) dma16s((P & 3) * SLOT + J * 8192u + s.w * 1024u, s.vl16, s.rW, s.sW + P * 36864u + J * 8192u + s.w * 1024u);
  } else if constexpr (OP == 200) asm volatile("buffer_load_dwordx4 %0, %1, %2, 0 offen sc1" : "=v"(s.sp0) : "v"(s.vsp), "s"(s.rS) : "memory");
  else if constexpr (OP == 201) asm volatile("buffer_load_dwordx4 %0, %1, %2, 0 offen offset:16 sc1" : "=v"(s.sp1) : "v"(s.vsp), "s"(s.rS) : "memory");
  else if constexpr (OP == 202) asm volatile("buffer_load_dwordx2 %0, %1, %2, 0 offen offset:64 sc1" : "=v"(s.sq) : "v"(s.csb), "s"(s.rS) : "memory");
  else {
    s.csb = CSOFF + (s.vl16 >> 9) * 16u; asm volatile("" : "+v"(s.csb));
    if (s.w < 2u) dma16s(CSOFF + s.w * 1024u, s.vl16, s.rCS, s.p0 * 128u + s.w * 1024u);
  }
}
template <int O0, int... Os> DI void vops(St& s, iseq<Os...>) { (vop<O0 + Os>(s), ...); }
template <int O0, int... Os> DI void sops(St& s, iseq<Os...>) { (vop<sop(O0 + Os)>(s), ...); }
template <int... Ks> DI void wpre(St& s, iseq<Ks...>) { (vop<3 * Ks + 2>(s), ...); }
template <int G> DI void reads(St& s) {
  if constexpr (G < NU) {
    constexpr int p = u_pair(G), ks = u_kstep(G);
    if constexpr (G < 144) {
      constexpr int off = ((ks >> 1) - eks(epoch_of(G))) * 8192 + (ks & 1) * 2048 + (p >> 1) * 256; const unsigned a = (p & 1) ? s.wp1 : s.wp;
      s.wf[G] = cat8(ldstr(lp(a) + off), ldstr(lp(a) + off + 512));
    } else s.wf[G] = lds128(lp(s.wp) + ks * 1024);
  }
}
template <int K> DI void pin1(St& s) { asm volatile("" : "+v"(s.hf[2 * K]), "+v"(s.hf[2 * K + 1])); }
template <int E, int... Ks> DI void pin_h(St& s, iseq<Ks...>) { (pin1<eks(E) + Ks>(s), ...); }
template <int E> DI void barrier(St& s) {
  wait_bar<vm_wait(E)>();
  if constexpr (E == 12) { if (s.w == 0u && s.vl16 == 0u) arrive(s.ws, 16u + s.rb); }
  if constexpr (E < 8) {
    pin_h<E>(s, mkseq<eks(E + 1) - eks(E)>{});
    unsigned a = s.wbA + eks(E) * 8192u, b = (s.wbA ^ 32u) + eks(E) * 8192u; asm volatile("" : "+v"(a), "+v"(b)); s.wp = a; s.wp1 = b;
  } else { unsigned a = s.vl16 + ((E - 8) & 3) * SLOT; asm volatile("" : "+v"(a)); s.wp = a; }
  SBAR(); if constexpr (E == 0 || E == 4 || E >= 7) TSTAMP(s);
}
template <int K> DI void stats(St& s) {
  if constexpr (K == 0) { asm volatile("" : "+v"(s.sp0), "+v"(s.sp1), "+v"(s.sq)); s.t0 = (s.sp0[0] + s.sp0[2]) + (s.sp1[0] + s.sp1[2]); s.t1 = (s.sp0[1] + s.sp0[3]) + (s.sp1[1] + s.sp1[3]); }
  else if constexpr (K == 1) s.t0 = (swap32_add(s.t0) + s.sq[0]) * (1.f / 9.f);
  else if constexpr (K == 2) { const float d0 = s.sp0[0] - s.t0, d1 = s.sp0[2] - s.t0; s.t1 = __builtin_fmaf(64.f, d0 * d0 + d1 * d1, s.t1); }
  else if constexpr (K == 3) { const float d0 = s.sp1[0] - s.t0, d1 = s.sp1[2] - s.t0; s.t1 = __builtin_fmaf(64.f, d0 * d0 + d1 * d1, s.t1); }
  else if constexpr (K == 4) { const float d = s.sq[0] - s.t0; s.t1 = swap32_add(s.t1) + __builtin_fmaf(64.f * d, d, s.sq[1]); }
  else if constexpr (K == 5) s.t1 = rbf(rbf(s.t1 * (1.f / DM)) + 1e-5f);
  else if constexpr (K == 6) { s.t1 = rbf(__builtin_amdgcn_sqrtf(s.t1)); s.nmean = -rbf(s.t0); }
  else { s.c1 = -1.4426950408889634f * __builtin_amdgcn_rcpf(s.t1); s.c2 = s.t1 * s.t1; asm volatile("" : "+v"(s.c1), "+v"(s.c2), "+v"(s.nmean)); }
  if constexpr (K < 7) asm volatile("" : "+v"(s.t0), "+v"(s.t1));
}
template <int P, int H> DI void csread(St& s, f32x2& g, f32x2& u) { g = *(const LAS f32x2*)(lp(s.csb) + P * 128 + (H >> 1) * 32 + (H & 1) * 8); u = *(const LAS f32x2*)(lp(s.csb) + P * 128 + 64 + (H >> 1) * 32 + (H & 1) * 8); }
template <int P, int ST> DI void ep(St& s) {
  if constexpr (ST == 0) { csread<P, 0>(s, s.csg, s.csu); asm volatile("s_nop 3"); }
  constexpr int k = (ST + 2) % 3, v = (ST - 1) / 3;
  if constexpr (ST >= 1 && ST < 28) {
    if constexpr (k == 0) {
      if constexpr (v > 0) { s.d0 = __builtin_fmaf(s.x1, s.c2, s.c2); asm volatile("" : "+v"(s.d0)); }
      if constexpr (v < 8) {
        const f32x2& cg = (v & 2) ? s.csg2 : s.csg; const f32x2& cu = (v & 2) ? s.csu2 : s.csu;
        s.ag = __builtin_fmaf(s.nmean, cg[v & 1], s.acc[P][v]); s.au = __builtin_fmaf(s.nmean, cu[v & 1], s.acc[P][8 + v]); asm volatile("" : "+v"(s.ag), "+v"(s.au));
        if constexpr ((v & 1) == 0 && v < 6) { if constexpr (v & 2) csread<P, (v >> 1) + 1>(s, s.csg, s.csu); else csread<P, (v >> 1) + 1>(s, s.csg2, s.csu2); }
      }
    } else if constexpr (k == 1) {
      if constexpr (v > 0) { s.d0 = __builtin_amdgcn_rcpf(s.d0); asm volatile("" : "+v"(s.d0)); }
      if constexpr (v < 8) { s.x0 = s.ag * s.c1; s.t0 = s.ag * s.au; asm volatile("" : "+v"(s.x0), "+v"(s.t0)); }
    } else {
      if constexpr (v > 0) { float o = s.t1 * s.d0; asm volatile("" : "+v"(o)); if constexpr (v & 1) s.o0 = o; else s.o1 = o; }
      if constexpr (v < 8) { s.x1 = __builtin_amdgcn_exp2f(s.x0); s.t1 = s.t0; asm volatile("" : "+v"(s.x1)); }
    }
  }
  if constexpr (ST >= 10 && ST <= 28 && (ST - 10) % 6 == 0) { unsigned pkd; asm volatile("v_cvt_pk_bf16_f32 %0, %1, %2" : "=v"(pkd) : "v"(s.o0), "v"(s.o1)); s.pk[(ST - 10) / 6] = pkd; }
  if constexpr (ST == NEP - 1) st128m<ST4>(s.pk, s.rAct, s.vst, s.sAct + P * 512u);
}
template <int G, int P> DI void ep_try(St& s) {
  if constexpr (G >= ep_start(P) - 1 && G < ep_start(P) - 1 + NEP) ep<P, G - ep_start(P) + 1>(s); }
template <int G> DI void slot(St& s) {
  constexpr int en = epoch_of(G) + 1;
  if constexpr (en < NE && G == bslot(en)) { barrier<en>(s); reads<G + 2>(s); reads<G + 3>(s); }
  else if constexpr (!(en < NE && G + LA >= estart(en) && G < bslot(en))) reads<G + LA>(s);
  sops<s_lo(G)>(s, mkseq<s_lo(G + 1) - s_lo(G)>{});
  if constexpr (G == WPEEK) { if (s.w == 0u) asm volatile("global_load_dword %0, %1, off sc1" : "=v"(s.wr) : "v"(s.ws + WS_CNT + 8u * 128u) : "memory"); }
  if constexpr (G == WCHK) {
    if (s.w == 0u) { asm volatile("" : "+v"(s.wr)); if (rfl(s.wr) < 18u) await(s.ws, 8u, 18u);
    }
  }
  if constexpr (op_at(G) >= 0) vop<op_at(G)>(s);
  constexpr int p = u_pair(G), ks = u_kstep(G);
  if constexpr (ks == 0) MFMA32_NEW(s.acc[p], s.wf[G], s.hf[ks]); else MFMA32_ACC(s.acc[p], s.wf[G], s.hf[ks]);
  if constexpr (G >= ST0 && G < ST0 + 8) stats<G - ST0>(s);
  ep_try<G, 0>(s); ep_try<G, 1>(s); ep_try<G, 2>(s); ep_try<G, 3>(s); ep_try<G, 4>(s); ep_try<G, 5>(s); ep_try<G, 6>(s); ep_try<G, 7>(s);
  SBAR();
}
template <int... Gs> DI void run(St& s, iseq<Gs...>) { (slot<Gs>(s), ...); }
template <int... Ss> DI void ep_all(St& s, iseq<Ss...>) { (ep<8, Ss>(s), ...); }
}

DI void phase_up(const u16* wg, const u16* wu, char* ws, unsigned hblk) {
  using namespace k4;
  const unsigned tid = threadIdx.x, lane = olane(), w = rfl(tid >> 6), r = lane & 31, hi = lane >> 5, blk = (lane >> 4) & 1, q2 = (lane >> 2) & 3, p2 = lane & 3;
  const unsigned b = blockIdx.x, xcd = b & 7, ix = b >> 3, rb = ix >> 2, cb = xcd * 4 + (ix & 3);
  St s;
  TSTAMP(s);
  s.w = w; s.rW = mksrd(ws + WS_WGU, DM * FF * 4); s.rAct = mksrd(ws + WS_ACT, SEQ * FF * 2);
  s.rH = mksrd(ws + WS_H, SEQ * DM * 2); s.rS = mksrd(ws + WS_ST, SEQ * 72); s.rCS = mksrd(ws + WS_CS, FF * 8);
  const unsigned row0 = rb * 256u + w * 32u;
  s.p0 = cb * 9u; s.sW = cb * 9u * 36864u; s.rR = mksrd(w < 4 ? wg : wu, DM * FF * 2);
  s.vA = (4u * hi + q2) * (FF * 2u) + blk * 64u + ((p2 ^ ((w >> 2) << 1)) << 4); s.sA = cb * 288u + (w & 3) * 8u * (FF * 2u);
  s.wbA = blk * 4096u + hi * 1024u + q2 * 64u + blk * 32u + p2 * 8u;
  s.vl16 = lane * 16u; s.sHh = (row0 >> 5) * 36864u;
  s.vst = (r >> 4) * 147456u + hi * 256u + (r & 15) * 16u;
  s.sAct = (rb * 16u + w * 2u) * 147456u + cb * 9u * 512u;
  s.csb = (row0 + r) * 72u; s.vsp = s.csb + hi * 32u;
  s.ws = ws; s.rb = rb; s.wr = 0;
  wait_bar<0>();
  if (tid == 0 && hblk != 0xffu) { const unsigned r0 = hblk * 80u >> 8, r1 = (hblk * 80u + 79u) >> 8; arrive(ws, r0); if (r1 != r0 && r1 < 8u) arrive(ws, r1); }
  wpre(s, mkseq<WPRE>{});
  if (w == 0u) await(ws, rb, 9u * ((256u * rb + 255u) / 80u - (256u * rb) / 80u + 1u));
  __builtin_amdgcn_s_barrier();
  asm volatile("" ::: "memory");
  TSTAMP(s);
  vops<200>(s, mkseq<3>{});
  sops<0>(s, mkseq<4>{});
  __builtin_amdgcn_s_barrier();
  sops<4>(s, mkseq<NPRO - 4>{});
  barrier<0>(s);
  reads<0>(s); reads<1>(s); reads<2>(s); SBAR();
  run(s, mkseq<NU>{});
  TSTAMP(s);
  asm volatile("s_nop 7\n\ts_nop 7" ::: "memory"); SBAR();
  ep_all(s, mkseq<NEP>{});
  asm volatile("s_waitcnt lgkmcnt(0)\n\ts_barrier" ::: "memory");
}

__global__ __launch_bounds__(512, 2) void k3_tail(const u16* __restrict__ wo, const u16* __restrict__ x, const u16* __restrict__ wg, const u16* __restrict__ wu, const u16* __restrict__ wd,
                                                  char* __restrict__ ws, u16* __restrict__ out) {
  const unsigned hblk = k3::body<128, true>(ws + WS_CTX, wo, x, wg, wu, ws, nullptr);
  phase_up(wg, wu, ws, hblk);
  k3::body<144, false>(ws + WS_ACT, wd, nullptr, nullptr, nullptr, ws, out);
}

extern "C" void kernel_launch(void* const* d_in, const int* in_sizes, int n_in, void* d_out, int out_size, void* d_ws, size_t ws_size, hipStream_t stream) {
  const u16* x = (const u16*)d_in[0]; const u16* ckv = (const u16*)d_in[1]; const u16* wq = (const u16*)d_in[2]; const u16* wuk = (const u16*)d_in[3];
  const u16* wuv = (const u16*)d_in[4]; const u16* wo = (const u16*)d_in[5]; const u16* wg = (const u16*)d_in[7]; const u16* wu = (const u16*)d_in[8]; const u16* wd = (const u16*)d_in[9];
  char* ws = (char*)d_ws;
  (void)hipFuncSetAttribute((const void*)k1_kvproj, hipFuncAttributeMaxDynamicSharedMemorySize, k1::LDS_BYTES);
  k1_kvproj<<<256, 512, k1::LDS_BYTES, stream>>>(ckv, wuk, wuv, x, wq, ws);
  (void)hipFuncSetAttribute((const void*)k2_attn, hipFuncAttributeMaxDynamicSharedMemorySize, k2::LDS_BYTES);
  k2_attn<<<256, 512, k2::LDS_BYTES, stream>>>(wq, wo, wg, wu, wd, ws);
  (void)hipFuncSetAttribute((const void*)k3_tail, hipFuncAttributeMaxDynamicSharedMemorySize, k3::LDS_BYTES);
  k3_tail<<<256, 512, k3::LDS_BYTES, stream>>>(wo, x, wg, wu, wd, ws, (u16*)d_out);
}
```

```cpp
#include <hip/hip_runtime.h>
#include <utility>
typedef unsigned short u16;
typedef short bf16x8 __attribute__((ext_vector_type(8)));
typedef short s16x4 __attribute__((ext_vector_type(4)));
typedef float f32x16 __attribute__((ext_vector_type(16)));
typedef float f32x4 __attribute__((ext_vector_type(4)));
typedef float f32x2 __attribute__((ext_vector_type(2)));
typedef unsigned u32x4 __attribute__((ext_vector_type(4)));
typedef unsigned u32x2 __attribute__((ext_vector_type(2)));
typedef __bf16 bf16x2_t __attribute__((ext_vector_type(2)));
#define LAS __attribute__((address_space(3)))
#define DI __device__ __forceinline__
#define SBAR() __builtin_amdgcn_sched_barrier(0)
#define MFMA32(a, b, c) __builtin_amdgcn_mfma_f32_32x32x16_bf16((a), (b), (c), 0, 0, 0)
#define MFMA16(a, b, c) __builtin_amdgcn_mfma_f32_16x16x32_bf16((a), (b), (c), 0, 0, 0)
#define MFMA16_ACC(c, a, b) asm volatile("v_mfma_f32_16x16x32_bf16 %0, %1, %2, %0" : "+v"(c) : "v"(a), "v"(b))
#define MFMA16_NEW(c, a, b) asm volatile("v_mfma_f32_16x16x32_bf16 %0, %1, %2, 0" : "=v"(c) : "v"(a), "v"(b))
#define MFMA32_ACC(c, a, b) asm volatile("v_mfma_f32_32x32x16_bf16 %0, %1, %2, %0" : "+v"(c) : "v"(a), "v"(b))
#define MFMA32_NEW(c, a, b) asm volatile("v_mfma_f32_32x32x16_bf16 %0, %1, %2, 0" : "=v"(c) : "v"(a), "v"(b))
template <int... Is> using iseq = std::integer_sequence<int, Is...>;
template <int N> using mkseq = std::make_integer_sequence<int, N>;
typedef __amdgpu_buffer_rsrc_t srd_t;

DI unsigned rfl(unsigned x) { return __builtin_amdgcn_readfirstlane(x); }
DI float bflo(unsigned w) { return __uint_as_float(w << 16); }
DI float bfhi(unsigned w) { return __uint_as_float(w & 0xffff0000u); }
DI unsigned cvtpk(float lo, float hi) { f32x2 v = {lo, hi}; bf16x2_t b = __builtin_convertvector(v, bf16x2_t); return __builtin_bit_cast(unsigned, b); }
DI float rbf(float f) { return bflo(cvtpk(f, f)); }
DI srd_t mksrd(const void* p, unsigned bytes) { return __builtin_amdgcn_make_buffer_rsrc((void*)p, (short)0, (int)bytes, 0x00020000); }
DI void dma16(unsigned m0v, unsigned voff, srd_t srd, unsigned soff) {
  asm volatile("s_mov_b32 m0, %0\n\ts_nop 0\n\tbuffer_load_dwordx4 %1, %2, %3 offen lds" ::"s"(m0v), "v"(voff), "s"(srd), "s"(soff) : "memory");
}
DI void dma16s(unsigned m0v, unsigned voff, srd_t srd, unsigned soff) {
  asm volatile("s_mov_b32 m0, %0\n\ts_nop 0\n\tbuffer_load_dwordx4 %1, %2, %3 offen sc1 lds" ::"s"(m0v), "v"(voff), "s"(srd), "s"(soff) : "memory");
}
template <int N> DI void wait_vm() { asm volatile("s_waitcnt vmcnt(%0)" ::"i"(N) : "memory"); }
template <int N> DI void wait_bar() { asm volatile("s_waitcnt vmcnt(%0) lgkmcnt(0)\n\ts_barrier" ::"i"(N) : "memory"); }
DI bf16x8 lds128(const LAS char* p) { return *(const LAS bf16x8*)p; }
typedef short v4i16_t __attribute__((ext_vector_type(4)));
DI s16x4 ldstr(const LAS char* p) { return __builtin_bit_cast(s16x4, __builtin_amdgcn_ds_read_tr16_b64_v4i16((LAS v4i16_t*)p)); }
DI bf16x8 cat8(s16x4 a, s16x4 b) { return __builtin_shufflevector(a, b, 0, 1, 2, 3, 4, 5, 6, 7); }
template <int MODE> DI void st128m(u32x4 v, srd_t srd, unsigned voff, unsigned soff) {
  if constexpr (MODE == 0) asm volatile("buffer_store_dwordx4 %0, %1, %2, %3 offen\n\ts_nop 1" ::"v"(v), "v"(voff), "s"(srd), "s"(soff) : "memory");
  else if constexpr (MODE == 1) asm volatile("buffer_store_dwordx4 %0, %1, %2, %3 offen sc1\n\ts_nop 1" ::"v"(v), "v"(voff), "s"(srd), "s"(soff) : "memory");
  else if constexpr (MODE == 2) asm volatile("buffer_store_dwordx4 %0, %1, %2, %3 offen nt\n\ts_nop 1" ::"v"(v), "v"(voff), "s"(srd), "s"(soff) : "memory");
  else asm volatile("buffer_store_dwordx4 %0, %1, %2, %3 offen sc0 sc1\n\ts_nop 1" ::"v"(v), "v"(voff), "s"(srd), "s"(soff) : "memory");
}
constexpr int ST1 = 1, ST2 = 1, ST3 = 1, ST4 = 1  , ST5 = 0  , ST6 = 0  ;
template <int CTRL> DI float dpp_add(float x) { return x + __builtin_bit_cast(float, __builtin_amdgcn_update_dpp(0, __builtin_bit_cast(int, x), CTRL, 0xf, 0xf, true)); }
DI float swap32_add(float x) { auto r = __builtin_amdgcn_permlane32_swap(__float_as_uint(x), __float_as_uint(x), false, false); return __uint_as_float(r[0]) + __uint_as_float(r[1]); }
DI float swap16_add(float x) { auto r = __builtin_amdgcn_permlane16_swap(__float_as_uint(x), __float_as_uint(x), false, false); return __uint_as_float(r[0]) + __uint_as_float(r[1]); }
DI float swap32_max(float x) { auto r = __builtin_amdgcn_permlane32_swap(__float_as_uint(x), __float_as_uint(x), false, false); return __builtin_fmaxf(__uint_as_float(r[0]), __uint_as_float(r[1])); }

constexpr int SEQ = 2048, DM = 576, DC = 512, NH = 32, HD = 128, FF = 4608, HDM = NH * HD;
constexpr size_t WS_XN = 0;
constexpr size_t WS_KF = WS_XN + (size_t)SEQ * DM * 2;
constexpr size_t WS_VF = WS_KF + (size_t)SEQ * HDM * 2;
constexpr size_t WS_CTX = WS_VF + (size_t)SEQ * HDM * 2;
constexpr size_t WS_WGU = WS_CTX + (size_t)SEQ * HDM * 2;
constexpr size_t WS_H = WS_WGU + (size_t)DM * FF * 4;
constexpr size_t WS_ST = WS_H + (size_t)SEQ * DM * 2;
constexpr size_t WS_CS = WS_ST + (size_t)SEQ * 128;
constexpr size_t WS_ACT = WS_CS + (size_t)288 * 128;
constexpr size_t WS_XB = WS_ACT + (size_t)SEQ * FF * 2;
constexpr size_t WS_FLAG = WS_XB + (size_t)2 * 234 * 20480;
constexpr size_t WS_END = WS_FLAG + 32768;
constexpr unsigned F_XN = 16, F_KV = F_XN + 256, F_CTX = F_KV + 256, F_XB3 = F_CTX + 512, F_XB5 = F_XB3 + 256, F_H = F_XB5 + 256, F_W = F_H + 256, F_A0 = F_W + 32, F_A1 = F_A0 + 256;
typedef unsigned long long u64;
struct Sync { u64* f; u64 tag; };
DI void publish(const Sync y, unsigned i) { __hip_atomic_store(y.f + i, y.tag, __ATOMIC_RELAXED, __HIP_MEMORY_SCOPE_AGENT); }
DI void peek(const Sync y, unsigned i, u64& v) { asm volatile("global_load_dwordx2 %0, %1, off sc1" : "=v"(v) : "v"(y.f + i) : "memory"); }
DI void await(const Sync y, unsigned i) {
  for (unsigned spin = 0; spin < (1u << 17); ++spin) {
    if (__all(__hip_atomic_load(y.f + i, __ATOMIC_RELAXED, __HIP_MEMORY_SCOPE_AGENT) == y.tag)) break;
    __builtin_amdgcn_s_sleep(2);
  }
  asm volatile("" ::: "memory");
}
DI unsigned olane() { unsigned l; asm volatile("v_mbcnt_lo_u32_b32 %0, -1, 0\n\tv_mbcnt_hi_u32_b32 %0, -1, %0" : "=v"(l)); return l; }


namespace k1 {
constexpr int SLOT = 49152, XSCR = 3 * SLOT, LDS_BYTES = XSCR + 9216, LA = 3  , BB = 2  ;
constexpr int cstart(int c) { return c <= 8 ? 16 * c : 128 + 32 * (c - 8); }
constexpr int chunk_of(int g) { return g < 128 ? g >> 4 : 8 + ((g - 128) >> 5); }
constexpr int bslot(int c) { return cstart(c) - BB; }
constexpr int dcnt(int c) { return c < 8 ? 6 : (c < 12 ? 4 : 0); }
constexpr int kt_of(int g) { const int c = chunk_of(g), i = g - cstart(c); return c < 7 ? (i & 7) : c == 7 ? (i >> 1) : (i & 1); }
constexpr int sloc_of(int g) { const int c = chunk_of(g), i = g - cstart(c); return c < 7 ? (i >> 3) : c == 7 ? (i & 1) : (i >> 1); }
constexpr int kstep_of(int g) { const int c = chunk_of(g); return c < 8 ? 2 * c + sloc_of(g) : sloc_of(g); }
constexpr bool w_first(int g) { const int c = chunk_of(g), i = g - cstart(c); return c < 7 ? (i & 7) == 0 : (c == 7 && i < 2); }
constexpr int acc_of(int g) { const int c = chunk_of(g); return c < 8 ? kt_of(g) : 8 + 2 * (c - 8) + kt_of(g); }
constexpr int dma_at(int g) { for (int c = 0; c < 10; ++c) { const int j = g - (c ? bslot(c) : 0); if (j >= 0 && j < dcnt(c + 2)) return (c + 2) * 8 + j; } return -1; }
constexpr int pk_at(int g) {
  for (int u = 0; u < 4; ++u) { if (g == 112 + 4 * u + 6) return 2 * u; if (g == 112 + 4 * u + 7) return 2 * u + 1; }
  for (int u = 4; u < 7; ++u) { if (g == cstart(u + 5) + 3) return 2 * u; if (g == cstart(u + 5) + 4) return 2 * u + 1; }
  return -1;
}
constexpr int st_at(int g) {
  if (g < 128 || (g & 7) != 5) return -1;
  constexpr int order[16] = {0, 1, 2, 3, 8, 4, 9, 5, 10, 6, 11, 7, 12, 13, -1, -1};
  return order[(g - 128) >> 3];
}
constexpr int LN0 = 40, NLN = 48, LNRD = 100, LNST = 104, XNPUB = 9;
constexpr int ln_at(int g) { return (g >= LN0 && g < LN0 + NLN) ? g - LN0 : -1; }
constexpr int WARM = 200;
constexpr int late_ops(int g) { return (st_at(g) >= 0 ? 1 : 0) + (g == LNST ? 2 : 0) + (g == WARM ? 1 : 0); }
constexpr int ops_between(int from, int to) { int n = 0; for (int g = from + 1; g < to; ++g) n += (dma_at(g) >= 0 ? 1 : 0) + late_ops(g); return n; }
constexpr int vm_wait(int c) {
  if (c == 0) return 6;
  if (c == 1) return ops_between(-1, bslot(1));
  const int from = (c == 2 ? 0 : bslot(c - 2)) + dcnt(c) - 1;
  return late_ops(from) + ops_between(from, bslot(c));
}
struct St {
  bf16x8 wf[16][2]; bf16x8 af[256]; f32x4 acc[16][2]; u32x4 pk[16];
  unsigned wp, ap[4];
  unsigned wb, ab0, ab1, voffA0, voffA1[4], voffW, m0w, sA, sW, vst, sO;
  srd_t rA, rK, rV, rO, rXN;
  u32x4 x0, x1; float v[16], t0, t1, t2; unsigned lane, sXN; bool two;
  srd_t rWm; unsigned sWm; Sync y;
};
template <int C, int J> DI void dma(St& s) {
  constexpr unsigned slot = (C % 3) * SLOT;
  if constexpr (C < 8) {
    if constexpr (J < 2) dma16(s.m0w * 2048u + slot + 32768u + J * 1024u, s.voffA0, s.rA, s.sA + s.m0w * 16384u + J * 8192u + C * 128u);
    else {
      constexpr unsigned mat = (J >> 1) & 1, jj = J & 1;
      dma16(s.m0w * 2048u + slot + mat * 16384u + jj * 1024u, s.voffW, mat ? s.rV : s.rK, s.sW + s.m0w * 65536u + jj * 32768u + C * 524288u);
    }
  } else dma16(s.m0w * 4096u + slot + J * 1024u, s.voffA1[J], s.rA, s.sA + (128u + (C - 8) * 32u + J) * 1024u + s.m0w * 4096u);
}
DI const LAS char* lp(unsigned a) { return (const LAS char*)(size_t)a; }
template <int G> DI void reads(St& s) {
  if constexpr (G < 256) {
    constexpr int c = chunk_of(G), kt = kt_of(G), sl = sloc_of(G), ks = kstep_of(G);
    if constexpr (w_first(G)) {
      s.wf[ks][0] = cat8(ldstr(lp(s.wp) + sl * 8192), ldstr(lp(s.wp) + sl * 8192 + 1024));
      s.wf[ks][1] = cat8(ldstr(lp(s.wp ^ 16u) + sl * 8192), ldstr(lp(s.wp ^ 16u) + sl * 8192 + 1024));
    }
    if constexpr (c < 8) s.af[G] = lds128(lp(s.ap[sl]) + (kt >> 1) * 4096 + (kt & 1) * 1024);
    else s.af[G] = lds128(lp(s.ap[sl & 3]) + (sl >> 2) * 256 + kt * 8192);
  }
}
template <int C> DI void set_bases(St& s) {
  constexpr unsigned slot = (C % 3) * SLOT;
  if constexpr (C < 8) {
    unsigned w = s.wb + slot, a0 = s.ab0 + slot, a1 = (s.ab0 ^ 64u) + slot;
    asm volatile("" : "+v"(w), "+v"(a0), "+v"(a1));
    s.wp = w; s.ap[0] = a0; s.ap[1] = a1;
  } else {
    unsigned a0 = s.ab1 + slot, a1 = (s.ab1 ^ 64u) + slot, a2 = (s.ab1 ^ 128u) + slot, a3 = (s.ab1 ^ 192u) + slot;
    asm volatile("" : "+v"(a0), "+v"(a1), "+v"(a2), "+v"(a3));
    s.ap[0] = a0; s.ap[1] = a1; s.ap[2] = a2; s.ap[3] = a3;
  }
}
template <bool ISV, int ID> DI void pack(St& s) {
  constexpr int u = ID >> 1, j = ID & 1;
  const f32x4 a = ISV ? s.acc[2 * u][j] : s.acc[2 * u + j][0], b = ISV ? s.acc[2 * u + 1][j] : s.acc[2 * u + j][1];
  s.pk[ID] = u32x4{cvtpk(a[0], a[1]), cvtpk(a[2], a[3]), cvtpk(b[0], b[1]), cvtpk(b[2], b[3])};
  asm volatile("" : "+v"(s.pk[ID]));
}
template <int ID> DI void store(St& s) { st128m<ST1>(s.pk[ID], s.rO, s.vst, s.sO + (ID >> 1) * 8192u + (ID & 1) * 128u); }
template <int ST> DI void ln_stage(St& s) {
  if constexpr (ST < 4) {
    if constexpr (ST == 0) { asm volatile("" : "+v"(s.x0), "+v"(s.x1)); s.t0 = 0.f; s.t2 = 0.f; }
    s.v[2 * ST] = bflo(s.x0[ST]); s.v[2 * ST + 1] = bfhi(s.x0[ST]); s.t0 += s.v[2 * ST] + s.v[2 * ST + 1];
  } else if constexpr (ST < 8) { constexpr int i = ST - 4; s.v[8 + 2 * i] = bflo(s.x1[i]); s.v[9 + 2 * i] = bfhi(s.x1[i]); s.t2 += s.v[8 + 2 * i] + s.v[9 + 2 * i]; }
  else if constexpr (ST == 8) s.t0 = dpp_add<0xB1>(s.t0 + (s.two ? s.t2 : 0.f));
  else if constexpr (ST == 9) s.t0 = dpp_add<0x141>(dpp_add<0x4E>(s.t0));
  else if constexpr (ST == 10) s.t0 = dpp_add<0x140>(s.t0);
  else if constexpr (ST == 11) s.t0 = swap16_add(s.t0);
  else if constexpr (ST == 12) { s.t0 = swap32_add(s.t0) * (1.f / DM); s.t1 = 0.f; s.t2 = 0.f; }
  else if constexpr (ST < 21) { constexpr int i = 2 * (ST - 13); float& q = i < 8 ? s.t1 : s.t2; const float d = s.v[i] - s.t0, e = s.v[i + 1] - s.t0; q += d * d; q += e * e; }
  else if constexpr (ST == 21) s.t1 = dpp_add<0xB1>(s.t1 + (s.two ? s.t2 : 0.f));
  else if constexpr (ST == 22) s.t1 = dpp_add<0x141>(dpp_add<0x4E>(s.t1));
  else if constexpr (ST == 23) s.t1 = dpp_add<0x140>(s.t1);
  else if constexpr (ST == 24) s.t1 = swap16_add(s.t1);
  else if constexpr (ST == 25) s.t1 = swap32_add(s.t1) * (1.f / DM);
  else if constexpr (ST == 26) { s.t0 = rbf(s.t0); s.t1 = rbf(s.t1); }
  else if constexpr (ST == 27) s.t1 = rbf(s.t1 + 1e-5f);
  else if constexpr (ST == 28) s.t1 = rbf(__builtin_amdgcn_sqrtf(s.t1));
  else if constexpr (ST == 29) s.t1 = __builtin_amdgcn_rcpf(s.t1);
  else if constexpr (ST < 46) {
    constexpr int i = (ST - 30) >> 1; u32x4& o = i < 4 ? s.x0 : s.x1;
    if constexpr (((ST - 30) & 1) == 0) o[i & 3] = cvtpk(s.v[2 * i] - s.t0, s.v[2 * i + 1] - s.t0);
    else o[i & 3] = cvtpk(bflo(o[i & 3]) * s.t1, bfhi(o[i & 3]) * s.t1);
    asm volatile("" : "+v"(o));
  } else if constexpr (ST == 46) *(LAS u32x4*)(size_t)(XSCR + s.lane * 128u + ((s.m0w ^ (s.lane & 7)) << 4)) = s.x0;
  else { if (s.two) *(LAS u32x4*)(size_t)(XSCR + 8192u + s.lane * 128u + ((s.m0w ^ (s.lane & 7)) << 4)) = s.x1; }
  if constexpr (ST < 30) asm volatile("" : "+v"(s.t0), "+v"(s.t1), "+v"(s.t2));
}
template <bool ISV, int G> DI void slot(St& s) {
  constexpr int cn = chunk_of(G) + 1;
  constexpr bool isbar = cn < 12 && G == bslot(cn);
  if constexpr (isbar) {
    wait_bar<vm_wait(cn)>(); set_bases<cn>(s); SBAR();
    if constexpr (cn == XNPUB) { if (threadIdx.x == 0) publish(s.y, F_XN + blockIdx.x); }
    reads<G + BB>(s); reads<G + BB + 1>(s);
  } else if constexpr (!(cn < 12 && G + LA >= cstart(cn) && G < bslot(cn))) reads<G + LA>(s);
  if constexpr (dma_at(G) >= 0) dma<(dma_at(G) >> 3), (dma_at(G) & 7)>(s);
  constexpr int ai = acc_of(G), ks = kstep_of(G);
#pragma unroll
  for (int ct = 0; ct < 2; ++ct) {
    const bf16x8 a = ISV ? s.af[G] : s.wf[ks][ct], bq = ISV ? s.wf[ks][ct] : s.af[G];
    if constexpr (ks == 0) MFMA16_NEW(s.acc[ai][ct], a, bq); else MFMA16_ACC(s.acc[ai][ct], a, bq);
  }
  if constexpr (pk_at(G) >= 0) pack<ISV, pk_at(G)>(s);
  if constexpr (st_at(G) >= 0) store<st_at(G)>(s);
  if constexpr (ln_at(G) >= 0) ln_stage<ln_at(G)>(s);
  if constexpr (G == LNRD) {
    s.x0 = *(const LAS u32x4*)(size_t)(XSCR + s.m0w * 1024u + s.lane * 16u); s.x1 = *(const LAS u32x4*)(size_t)(XSCR + 8192u + s.lane * 16u);
  }
  if constexpr (G == LNST) {
    const unsigned vo = (s.lane >> 3) * 512u + (((s.lane ^ (s.lane >> 3)) & 7) << 4);
    st128m<ST1>(s.x0, s.rXN, vo, s.sXN + s.m0w * 4096u); st128m<ST1>(s.x1, s.rXN, vo, s.m0w == 0 ? s.sXN + 32768u : 0x10000000u);
  }
  if constexpr (G == WARM)
    asm volatile("s_mov_b32 m0, %0\n\ts_nop 0\n\tbuffer_load_dword %1, %2, %3 offen lds" ::"s"(XSCR + 4096u + s.m0w * 256u), "v"(s.lane < 18u ? (s.lane >> 1) * 8192u + (s.lane & 1) * 128u : 0x10000000u), "s"(s.rWm), "s"(s.sWm) : "memory");
  SBAR();
}
template <bool ISV, int... Gs> DI void run(St& s, iseq<Gs...>) {
  reads<0>(s); reads<1>(s); reads<2>(s); SBAR();
  (slot<ISV, Gs>(s), ...);
  asm volatile("s_nop 7\n\ts_nop 3" ::: "memory"); SBAR();
  pack<ISV, 14>(s); store<14>(s); pack<ISV, 15>(s); store<15>(s);
}
template <int C, int... Js> DI void dma_all(St& s, iseq<Js...>) { (dma<C, Js>(s), ...); }
}

DI void phase_kv(const u16* ckv, const u16* wuk, const u16* wuv, const u16* x, const u16* wq, char* ws, Sync& y) {
  using namespace k1;
  const unsigned tid = threadIdx.x, lane = tid & 63, w = rfl(tid >> 6), r = lane & 15, g = lane >> 4;
  const unsigned b = blockIdx.x, xcd = b & 7, ix = b >> 3, h = xcd * 4 + (ix & 3), rb = ix >> 2;
  St s;
  s.rA = mksrd(ckv, SEQ * DC * 2); s.rK = mksrd(wuk, DC * HDM * 2); s.rV = mksrd(wuv, DC * HDM * 2);
  s.rO = mksrd(ws + (w < 4 ? WS_KF : WS_VF), SEQ * HDM * 2); s.rXN = mksrd(ws + WS_XN, SEQ * DM * 2);
  s.m0w = w; s.two = lane < 8; s.lane = lane; s.sXN = (b >> 2) * 36864u + (b & 3) * 128u;
  const unsigned q2 = (lane >> 2) & 3, p2 = lane & 3, rrow = 16u * (r >> 3) + (r & 7);
  s.voffA0 = (lane >> 3) * 1024u + (((lane & 7) ^ (4u * (w & 1) + g)) << 4);
#pragma unroll
  for (int j = 0; j < 4; ++j) s.voffA1[j] = (lane ^ (8u * (w >> 2) + ((4u * w + j) & 7u))) << 4;
  s.voffW = q2 * 8192u + g * 64u + ((p2 ^ (w & 1u)) << 4);
  s.sA = rb * 256u * 1024u; s.sW = h * 256u;
  s.wb = (w >> 2) * 16384u + (w & 3) * 256u + g * 2048u + q2 * 64u + (p2 >> 1) * 32u + (g & 1) * 16u + (p2 & 1) * 8u;
  s.ab0 = 32768u + rrow * 128u + ((g ^ (r >> 1)) << 4);
  s.ab1 = rrow * 1024u + ((g ^ r) << 4);
  s.vst = (g >> 1) * (w < 4 ? 1024u : 4096u) + (rrow + 32u * (g & 1)) * 16u;
  s.sO = (h * 64u + rb * 8u) * 8192u + (w < 4 ? w * 2048u : (w - 4) * 1024u);
  s.rWm = mksrd(wq, DM * HDM * 2); s.sWm = (rb * 72u + w * 9u) * 8192u + h * 256u;
  {
    const srd_t rX = mksrd(x, SEQ * DM * 2);
    asm volatile("buffer_load_dwordx4 %0, %2, %4, %5 offen\n\tbuffer_load_dwordx4 %1, %3, %4, %5 offen offset:1024"
                 : "=&v"(s.x0), "=&v"(s.x1) : "v"(lane * 16u), "v"((lane & 7) * 16u), "s"(rX), "s"((b * 8u + w) * (DM * 2u)) : "memory");
  }
  u64 gen; peek(y, 0, gen);
  dma_all<0>(s, mkseq<6>{});
  __builtin_amdgcn_s_barrier();
  dma_all<1>(s, mkseq<6>{});
  wait_bar<vm_wait(0)>(); set_bases<0>(s);
  asm volatile("" : "+v"(gen)); gen += 1u; y.tag = ((u64)rfl((unsigned)(gen >> 32)) << 32) | rfl((unsigned)gen); s.y = y; SBAR();
  if (w < 4) run<false>(s, mkseq<256>{}); else run<true>(s, mkseq<256>{});
  asm volatile("s_barrier" ::: "memory");
}

namespace k2 {
constexpr unsigned KR = 0, VR = 65536, SP = 131072, QR = VR  , LDS_BYTES = 163840;
constexpr float THR = 8.f;
constexpr float QSCALE = 0.08838834764831845f * 1.4426950408889634f;
DI const LAS char* lp(unsigned a) { return (const LAS char*)(size_t)a; }
DI float max3f(float a, float b, float c) { float r; asm("v_max3_f32 %0, %1, %2, %3" : "=v"(r) : "v"(a), "v"(b), "v"(c)); return r; }

constexpr int QLA = 3, QBB = 2;
constexpr int qb(int c) { return 16 * c - QBB; }
constexpr int qfirst(int c) { return c == 2 ? 0 : c == 3 ? 6 : qb(c - 3); }
constexpr int qop_chunk(int g) { for (int c = 2; c < 9; ++c) { const int j = g - qfirst(c); if (j >= 0 && j < 6) return c * 8 + j; } return -1; }
constexpr int qkv_at(int g) { return (g >= 96 && g < 112 && ((g - 96) & 3) == 0) ? (g - 96) >> 2 : -1; }
constexpr int qops(int g) { return (qop_chunk(g) >= 0) + (qkv_at(g) >= 0); }
constexpr int qwait(int c) {
  if (c == 0) return 4;
  int n = 0; const int from = c == 1 ? 0 : qfirst(c) + 6;
  for (int g = from; g < qb(c); ++g) n += qops(g);
  return n;
}
struct St {
  bf16x8 xf[36], wf[144];
  bf16x8 qf[8], fr[35]; f32x16 o[4], sc[2][2], negm, oo[2]; u32x4 pf[4], out[4]; f32x4 p[8]; f32x2 st; f32x4 la; bf16x8 sel;
  float m, mo, l, ls0, ls1, mx0, mx1, f, ca, cb; bool pend;
  unsigned w, vl16, wq_rd, vW, sX, sW, qw, kb, va, vb, sKV, n0, sC0;
  srd_t rW, rX, rK, rV, rC, rWm; unsigned vWm, sWm, dummy, fkv; Sync y; u64 pv;
};
struct Dm { unsigned m0k, m0v, kso, vso; };
DI unsigned tile_off(const St& s, unsigned c) { return s.sKV + (c < s.n0 ? c : c - s.n0) * 32768u; }
DI Dm mkdm(const St& s, int c) {
  const unsigned par = (unsigned)(c & 1) << 15;
  return Dm{KR + par + s.w * 4096u, VR + (par ^ 32768u) + s.w * 4096u, tile_off(s, c + 2), tile_off(s, c + 1)};
}
template <int J> DI void kvdma(const St& s, const Dm& d) {
  asm volatile("s_mov_b32 m0, %0\n\ts_nop 0\n\tbuffer_load_dwordx4 %1, %2, %3 offen offset:%4 sc1 lds" ::"s"(J < 4 ? d.m0k : d.m0v), "v"(s.vl16), "s"(J < 4 ? s.rK : s.rV), "s"(J < 4 ? d.kso : d.vso), "i"((J & 3) * 1024) : "memory");
}
template <int C, int J> DI void qload(St& s) {
  if constexpr (J < 2) dma16(QR + (C & 3) * 16384u + s.w * 2048u + J * 1024u, s.vW, s.rW, s.sW + (C * 64u + J * 4u) * 8192u);
  else asm volatile("buffer_load_dwordx4 %0, %1, %2, %3 offen offset:%4 sc1" : "=v"(s.xf[4 * C + J - 2]) : "v"(s.vl16), "s"(s.rX), "s"(s.sX + C * 4096u), "i"((J - 2) * 1024) : "memory");
}
template <int C> DI void qbar(St& s) {
  asm volatile("s_waitcnt vmcnt(%4) lgkmcnt(0)\n\ts_barrier" : "+v"(s.xf[4 * C]), "+v"(s.xf[4 * C + 1]), "+v"(s.xf[4 * C + 2]), "+v"(s.xf[4 * C + 3]) : "i"(qwait(C)) : "memory");
}
constexpr int q_t(int g) { return g < 128 ? (g & 3) : ((g - 128) >> 2); }
constexpr int q_s(int g) { return g < 128 ? ((g >> 2) & 3) : (g & 3); }
template <int G> DI void qread(St& s) {
  if constexpr (G < 144) { constexpr int off = ((G >> 4) & 3) * 16384 + q_s(G) * 4096 + q_t(G) * 256; s.wf[G] = cat8(ldstr(lp(s.wq_rd) + off), ldstr(lp(s.wq_rd) + off + 1024)); }
}
template <int F> DI void qpack(St& s) {
  constexpr int T = F >> 1, o8 = (F & 1) * 8;
  const u32x4 v = {cvtpk(s.o[T][o8] * QSCALE, s.o[T][o8 + 1] * QSCALE), cvtpk(s.o[T][o8 + 2] * QSCALE, s.o[T][o8 + 3] * QSCALE),
                   cvtpk(s.o[T][o8 + 4] * QSCALE, s.o[T][o8 + 5] * QSCALE), cvtpk(s.o[T][o8 + 6] * QSCALE, s.o[T][o8 + 7] * QSCALE)};
  s.qf[F] = __builtin_bit_cast(bf16x8, v);
  *(LAS bf16x8*)((LAS char*)(size_t)s.qw + F * 1024) = s.qf[F];
}
template <int G> DI void qslot(St& s) {
  constexpr int cn = (G >> 4) + 1;
  if constexpr (G == 70) { if (s.w == 0u) peek(s.y, s.fkv + (olane() & 7), s.pv); }
  if constexpr (G == qb(6)) {
    if (s.w == 0u) { asm volatile("s_waitcnt vmcnt(6)" : "+v"(s.pv)::"memory"); if (!__all(s.pv == s.y.tag)) await(s.y, s.fkv + (olane() & 7)); }
  }
  if constexpr (cn < 9 && G == qb(cn)) { qbar<cn>(s); SBAR(); qread<G + 2>(s); qread<G + 3>(s); }
  else if constexpr (!(cn < 9 && G + QLA >= 16 * cn && G < qb(cn))) qread<G + QLA>(s);
  if constexpr (qop_chunk(G) >= 0) qload<(qop_chunk(G) >> 3), (qop_chunk(G) & 7)>(s);
  if constexpr (qkv_at(G) >= 0) kvdma<qkv_at(G)>(s, mkdm(s, -2));
  constexpr int ks = 4 * (G >> 4) + q_s(G), t = q_t(G);
  const f32x16 z = {};
  s.o[t] = MFMA32(s.wf[G], s.xf[ks], ks == 0 ? z : s.o[t]);
  if constexpr (G >= 134 && ((G - 134) & 1) == 0) qpack<((G - 134) >> 1)>(s);
  SBAR();
}
template <int... Gs> DI void qrun(St& s, iseq<Gs...>) { (qslot<Gs>(s), ...); }
template <int C, int J0, int... Js> DI void qload_all(St& s, iseq<Js...>) { (qload<C, J0 + Js>(s), ...); }

template <int I> DI void frag(St& s) {
  if constexpr (I < 16) s.fr[I] = lds128(lp(s.kb) + (I & 1) * 8192 + (I >> 1) * 1024);
  else if constexpr (I < 32) s.fr[I] = lds128(lp((I & 2) ? s.vb : s.va) + (I - 16) * 1024);
  else s.fr[I] = lds128(lp(s.kb) + ((I - 32) & 1) * 8192 + ((I - 32) >> 1) * 1024);
}
template <int X> DI void mask_diag(St& s) {
  const unsigned ln = olane();
  const int dq = 32 * ((int)(s.w & 3) - 2 * (int)(s.w >> 2)) + (int)(ln & 31) - 4 * (int)(ln >> 5);
#pragma unroll
  for (int kb = 0; kb < 2; ++kb)
#pragma unroll
    for (int r = 0; r < 16; ++r) { const int c = 32 * kb + (r & 3) + 8 * (r >> 2); s.sc[X][kb][r] = c > dq ? -__builtin_inff() : s.sc[X][kb][r]; }
}
DI void sub_ip(f32x16& v, float d) {
#pragma unroll
  for (int r = 0; r < 16; ++r) { float e = v[r]; asm("v_sub_f32_e32 %0, %0, %1" : "+v"(e) : "v"(d)); v[r] = e; }
}
DI void mul_ip(f32x16& v, float d) {
#pragma unroll
  for (int r = 0; r < 16; ++r) { float e = v[r]; asm("v_mul_f32_e32 %0, %0, %1" : "+v"(e) : "v"(d)); v[r] = e; }
}
template <int X, int E> DI void sexp(St& s) { float e = __builtin_amdgcn_exp2f(s.sc[X][E >> 4][E & 15]); asm volatile("" : "+v"(e)); s.sc[X][E >> 4][E & 15] = e; }
template <int X, int E> DI void sadd(St& s) {
  float& ls = X ? s.ls1 : s.ls0; if constexpr (E == 0) ls = s.sc[X][0][0]; else { ls += s.sc[X][E >> 4][E & 15]; asm volatile("" : "+v"(ls)); } }
template <int X, int P> DI void scvt(St& s) { unsigned e = cvtpk(s.sc[X][P >> 3][(2 * P) & 15], s.sc[X][P >> 3][(2 * P + 1) & 15]); asm volatile("" : "+v"(e)); s.pf[P >> 2][P & 3] = e; }
DI void mg_stats(St& s, unsigned area, float m) {
  s.l = swap32_add(s.l);
  asm volatile("" : "+v"(m)); *(LAS f32x2*)(size_t)(area + s.w * 256u + (olane() & 31) * 8u) = f32x2{m, s.l}; }
template <int I> DI void mg_write(St& s, unsigned a) { const f32x16& v = s.o[2 + (I >> 2)]; *(LAS f32x4*)((LAS char*)(size_t)a + I * 1024) = f32x4{v[4 * (I & 3)], v[4 * (I & 3) + 1], v[4 * (I & 3) + 2], v[4 * (I & 3) + 3]}; }
DI void mg_read(St& s, unsigned a, unsigned area) {
  s.st = *(const LAS f32x2*)(size_t)(area + (s.w ^ 4u) * 256u + (olane() & 31) * 8u);
#pragma unroll
  for (int i = 0; i < 8; ++i) s.p[i] = *(const LAS f32x4*)(lp(a) + i * 1024);
}
DI void mg_coef(St& s, float m) {
  const float mm = __builtin_fmaxf(m, s.st[0]), fa = __builtin_amdgcn_exp2f(m - mm), fb = __builtin_amdgcn_exp2f(s.st[0] - mm);
  const float il = __builtin_amdgcn_rcpf(s.l * fa + s.st[1] * fb); s.ca = fa * il; s.cb = fb * il;
}
template <int I> DI void mg_final(St& s) {
  float v[8];
#pragma unroll
  for (int e = 0; e < 8; ++e) v[e] = s.oo[I >> 1][8 * (I & 1) + e] * s.ca + s.p[2 * I + (e >> 2)][e & 3] * s.cb;
  s.out[I] = u32x4{cvtpk(v[0], v[1]), cvtpk(v[2], v[3]), cvtpk(v[4], v[5]), cvtpk(v[6], v[7])};
  asm volatile("" : "+v"(s.out[I]));
}
template <int I> DI void mg_store(St& s, unsigned blk) {
  const unsigned ln = olane();
  st128m<ST2>(s.out[I], s.rC, ((ln >> 4) & 1) * 131072u + (ln & 15) * 16u + (ln >> 5) * 256u, s.sC0 + blk * 1048576u + I * 512u);
}
template <int X, int KIND, bool DIAG, int G> DI void aslot(St& s, int c) {
  constexpr int Y = X ^ 1; constexpr bool PREV = KIND != 0, FRESH = KIND == 0 || KIND == 2;
  if constexpr (G == 30) {
    wait_bar<KIND == 3 ? 5 : 1>();
    s.kb ^= 32768u; s.va ^= 32768u; s.vb ^= 32768u; asm volatile("" : "+v"(s.kb), "+v"(s.va), "+v"(s.vb)); SBAR();
    frag<32>(s); frag<33>(s);
  } else if constexpr (G + 3 < 16) frag<G + 3>(s);
  else if constexpr (G + 3 < 32) { if constexpr (PREV) frag<G + 3>(s); }
  else if constexpr (G == 31) frag<34>(s);
  const f32x16 z = {};
  if constexpr (G < 16) s.sc[X][G & 1] = MFMA32(s.fr[G], s.qf[G >> 1], G < 2 ? (FRESH ? z : s.negm) : s.sc[X][G & 1]);
  else if constexpr (PREV) s.o[G & 3] = MFMA32(s.fr[G], __builtin_bit_cast(bf16x8, s.pf[(G - 16) >> 2]), (KIND == 3 && G < 20) ? z : s.o[G & 3]);
  if constexpr (KIND == 0) {
    if constexpr (G == 1) asm volatile("s_waitcnt lgkmcnt(0)\n\ts_barrier" ::: "memory");
    if constexpr (G >= 1 && G < 5) kvdma<G - 1>(s, mkdm(s, -1));
  } else if constexpr (KIND != 3) {
    if constexpr (G < 12 && (G & 1)) kvdma<(G >> 1) + 2>(s, mkdm(s, c - 1));
  }
  if constexpr (KIND == 3) {
    const Dm d = mkdm(s, c - 1);
    if constexpr (G == 0) { mg_stats(s, SP, s.mo); s.qw = (s.w < 4u ? d.m0k : d.m0v - 32768u) + s.w * 4096u + s.vl16; asm volatile("" : "+v"(s.qw)); }
    if constexpr (G >= 2 && G < 10) mg_write<G - 2>(s, s.qw);
    if constexpr (G == 10) {
      asm volatile("s_waitcnt lgkmcnt(0)\n\ts_barrier" ::: "memory");
      unsigned a = (s.w < 4u ? d.m0v : d.m0k - 16384u) + (s.w & 3) * 4096u + s.vl16; asm volatile("" : "+v"(a));
      mg_read(s, a, SP); s.oo[0] = s.o[0]; s.oo[1] = s.o[1];
    }
    if constexpr (G == 12) mg_coef(s, s.mo);
    if constexpr (G == 13) asm volatile("s_waitcnt lgkmcnt(0)\n\ts_barrier" ::: "memory");
    if constexpr (G >= 13 && G < 21) kvdma<G - 13>(s, d);
    if constexpr (G >= 13 && G < 25 && (G - 13) % 3 == 0) mg_final<(G - 13) / 3>(s);
    if constexpr (G >= 24 && G < 28) mg_store<G - 24>(s, s.n0 - 1u);
  }
  if constexpr (G == 28)
    asm volatile("buffer_load_dword %0, %1, %2, %3 offen" : "+v"(s.dummy) : "v"(s.vWm), "s"(s.rWm), "s"(s.sWm + c * 640u) : "memory");
  if constexpr (PREV) {
    if constexpr (G < 26) sexp<Y, G + 6>(s);
    if constexpr (G < 27) sadd<Y, G + 5>(s);
    if constexpr (G < 27 && !(G & 1)) scvt<Y, (G >> 1) + 2>(s);
    if constexpr (G == 27) { const float ls = Y ? s.ls1 : s.ls0; if constexpr (KIND == 3) s.l = ls; else s.l += ls; asm volatile("" : "+v"(s.l)); }
  }
  if constexpr (KIND == 0 && G == 16) asm volatile("s_nop 7\n\ts_nop 7" : "+v"(s.sc[X][0]), "+v"(s.sc[X][1]));
  if constexpr (DIAG && G == 16) {
    mask_diag<X>(s);
    unsigned qa = SP + (s.w & 3) * 8192u + s.vl16; asm volatile("" : "+v"(qa));
#pragma unroll
    for (int i = 0; i < 8; ++i) s.qf[i] = lds128(lp(qa) + i * 1024);
  }
  if constexpr (G >= 17 && G < 25) {
    constexpr int i = G - 17, kb = i >> 2, r = 4 * (i & 3);
    if constexpr (i == 0) { s.mx0 = max3f(s.sc[X][0][0], s.sc[X][0][1], s.sc[X][0][1]); s.mx1 = max3f(s.sc[X][0][2], s.sc[X][0][3], s.sc[X][0][3]); }
    else { s.mx0 = max3f(s.mx0, s.sc[X][kb][r], s.sc[X][kb][r + 1]); s.mx1 = max3f(s.mx1, s.sc[X][kb][r + 2], s.sc[X][kb][r + 3]); }
    asm volatile("" : "+v"(s.mx0), "+v"(s.mx1));
  }
  if constexpr (G == 25) {
    const float mx = max3f(s.mx0, s.mx1, s.mx1);
    if constexpr (FRESH) {
      const float rm = __builtin_fmaxf(swap32_max(mx), -1e30f);
      if constexpr (KIND == 2) s.mo = s.m; else s.l = 0.f;
      s.m = rm; sub_ip(s.sc[X][0], rm); sub_ip(s.sc[X][1], rm);
#pragma unroll
      for (int r = 0; r < 16; ++r) s.negm[r] = -rm;
    } else if (__builtin_expect(__any(mx > THR), 0)) {
      const float dl = __builtin_fmaxf(swap32_max(mx), 0.f);
      s.m += dl; s.f = __builtin_amdgcn_exp2f(-dl); s.pend = true;
      sub_ip(s.sc[X][0], dl); sub_ip(s.sc[X][1], dl);
#pragma unroll
      for (int r = 0; r < 16; ++r) { float e = s.negm[r]; asm("v_sub_f32_e32 %0, 0, %1" : "+v"(e) : "v"(s.m)); s.negm[r] = e; }
    }
  }
  if constexpr (G >= 26) sexp<X, G - 26>(s);
  if constexpr (G >= 27) sadd<X, G - 27>(s);
  if constexpr (G == 28 || G == 30) scvt<X, ((G - 28) >> 1)>(s);
  if constexpr (KIND != 2 && G >= 30) kvdma<G - 30>(s, mkdm(s, c));
  SBAR();
}
template <int X, int KIND, bool DIAG, int... Gs> DI void astep_(St& s, int c, iseq<Gs...>) {
  if constexpr (KIND == 0) { frag<32>(s); frag<33>(s); frag<34>(s); SBAR(); }
  s.fr[0] = s.fr[32]; s.fr[1] = s.fr[33]; s.fr[2] = s.fr[34];
  (aslot<X, KIND, DIAG, Gs>(s, c), ...);
  if constexpr (KIND == 1 || KIND == 3) {
    if (__builtin_expect(s.pend, 0)) {
      asm volatile("s_nop 7");
      mul_ip(s.o[0], s.f); mul_ip(s.o[1], s.f); mul_ip(s.o[2], s.f); mul_ip(s.o[3], s.f);
      s.l *= s.f;
      s.pend = false;
    }
  }
}
template <int X, int KIND, bool DIAG = false> DI void astep(St& s, int c) { astep_<X, KIND, DIAG>(s, c, mkseq<32>{}); }
template <int T> DI void tslot(St& s) {
  if constexpr (T == 0) { frag<16>(s); frag<17>(s); frag<18>(s); }
  else if constexpr (T + 17 < 32 && T >= 2) frag<T + 17>(s);
  if constexpr (T >= 2) s.o[(T - 2) & 3] = MFMA32(s.fr[T + 14], __builtin_bit_cast(bf16x8, s.pf[(T - 2) >> 2]), s.o[(T - 2) & 3]);
  if constexpr (T < 13) { sexp<1, 6 + 2 * T>(s); sexp<1, 7 + 2 * T>(s); }
  if constexpr (T > 0 && T < 14) sadd<1, 4 + 2 * T>(s);
  if constexpr (T < 14) { sadd<1, 5 + 2 * T>(s); scvt<1, T + 2>(s); }
  if constexpr (T == 14) { s.l += s.ls1; mg_stats(s, VR + 32768u, s.m); }
  SBAR();
}
template <int... Ts> DI void atail(St& s, iseq<Ts...>) { (tslot<Ts>(s), ...); }
template <int... Is> DI void mg_write_all(St& s, unsigned a, iseq<Is...>) { (mg_write<Is>(s, a), ...); }
}

DI void phase_attn(const u16* wq, const u16* wo, const u16* wg, const u16* wu, const u16* wd, char* ws, const Sync y) {
  using namespace k2;
  const unsigned tid = threadIdx.x, lane = olane(), w = rfl(tid >> 6), hi = lane >> 5, rq = w & 3, g = w >> 2;
  const unsigned b = blockIdx.x, xcd = b & 7, ix = b >> 3, h = xcd * 4 + (ix & 3), x = ix >> 2;
  St s;
  s.w = w; s.vl16 = lane * 16u;
  const unsigned q2 = (lane >> 2) & 3, p2 = lane & 3, g4 = lane >> 4;
  s.rW = mksrd(wq, DM * HDM * 2); s.rX = mksrd(ws + WS_XN, SEQ * DM * 2); s.rK = mksrd(ws + WS_KF, SEQ * HDM * 2); s.rV = mksrd(ws + WS_VF, SEQ * HDM * 2);
  s.rC = mksrd(ws + WS_CTX, SEQ * HDM * 2);
  s.vW = q2 * 8192u + g4 * 64u + p2 * 16u; s.sW = h * 256u + w * 65536u;
  s.wq_rd = QR + hi * 2048u + q2 * 64u + (g4 & 1) * 32u + p2 * 8u; asm volatile("" : "+v"(s.wq_rd));
  s.sX = ((g == 0 ? x : 15u - x) * 4u + rq) * 36864u;
  s.sKV = h * 524288u + w * 4096u; s.n0 = x + 1u; s.sC0 = (2u * rq * 128u + 4u * h + 2u * g) * 1024u;
  s.qw = (g == 0 ? KR + 32768u : SP) + rq * 8192u + s.vl16; asm volatile("" : "+v"(s.qw));
  s.y = y; s.fkv = F_KV + h * 8u; s.pv = 0;
  qload_all<0, 0>(s, mkseq<2>{}); qload_all<1, 0>(s, mkseq<2>{});
  if (w == 0u) await(y, F_XN + ((lane & 16) ? 15u - x : x) * 16u + (lane & 15));
  asm volatile("s_barrier" ::: "memory");
  qload_all<0, 2>(s, mkseq<4>{});
  __builtin_amdgcn_s_barrier();
  qload_all<1, 2>(s, mkseq<4>{});
  qbar<0>(s); SBAR();
  if (tid == 0) publish(y, s.fkv + x);
  qread<0>(s); qread<1>(s); qread<2>(s); SBAR();
  qrun(s, mkseq<144>{});
  qpack<5>(s); qpack<6>(s); qpack<7>(s);
  wait_bar<0>();
  if (g == 1) {
    unsigned ra = KR + 32768u + rq * 8192u + s.vl16; asm volatile("" : "+v"(ra));
#pragma unroll
    for (int i = 0; i < 8; ++i) s.qf[i] = lds128(lp(ra) + i * 1024);
  }
  { const Dm d = mkdm(s, -1); kvdma<4>(s, d); kvdma<5>(s, d); kvdma<6>(s, d); kvdma<7>(s, d); }
  {
    const unsigned W = b * 8u + w;
    s.rWm = mksrd(W < 434u ? wo : W < 922u ? wg : W < 1410u ? wu : wd, W < 434u ? HDM * DM * 2 : DM * FF * 2);
    s.sWm = (W < 434u ? W : W < 922u ? W - 434u : W < 1410u ? W - 922u : W - 1410u) * 10880u;
    const unsigned l = olane(); s.vWm = l < 5u ? l * 128u : 0x10000000u;
    s.dummy = 0;
  }
  s.kb = KR + g * 16384u + s.vl16; s.va = VR + 32768u + g * 18432u + s.vl16; s.vb = VR + 32768u + g * 14336u + s.vl16; s.pend = false;
  asm volatile("" : "+v"(s.kb), "+v"(s.va), "+v"(s.vb));
#pragma unroll
  for (int d = 0; d < 4; ++d) s.o[d] = f32x16{};
  { const unsigned l = olane(), e = (((l >> 2) ^ (l >> 4)) & 1) ? 0u : 0x3f803f80u; s.sel = __builtin_bit_cast(bf16x8, u32x4{e, e, e, e}); s.la = f32x4{}; }
  if (x == 0) astep<1, 0, true>(s, 0); else astep<0, 0>(s, 0);
  int c = 1;
  for (int pass = 0; pass < 2; ++pass) {
    if (pass == 1 || x != 0) {
      const int cend = pass == 0 ? (int)x : 16;
      for (; c + 1 < cend; c += 2) { astep<1, 1>(s, c); astep<0, 1>(s, c + 1); }
      if (c < cend) { astep<1, 1>(s, c); ++c; s.sc[0][0] = s.sc[1][0]; s.sc[0][1] = s.sc[1][1]; s.ls0 = s.ls1; }
      astep<1, 1, true>(s, c); ++c;
    }
    if (pass == 0) { astep<0, 2>(s, c); astep<1, 3>(s, c + 1); astep<0, 1>(s, c + 2); c += 3; if (tid == 0) publish(y, F_CTX + x * 32u + h); }
  }
  atail(s, mkseq<18>{});
  {
    unsigned mine = (g == 0 ? SP : KR + 32768u) + rq * 8192u + s.vl16, theirs = (g == 0 ? KR + 32768u : SP) + rq * 8192u + s.vl16; asm volatile("" : "+v"(mine), "+v"(theirs));
    mg_write_all(s, mine, mkseq<8>{});
    asm volatile("s_waitcnt lgkmcnt(0)\n\ts_barrier" ::: "memory");
    mg_read(s, theirs, VR + 32768u); s.oo[0] = s.o[0]; s.oo[1] = s.o[1];
    mg_coef(s, s.m);
    mg_final<0>(s); mg_store<0>(s, 15u - x); mg_final<1>(s); mg_store<1>(s, 15u - x); mg_final<2>(s); mg_store<2>(s, 15u - x); mg_final<3>(s); mg_store<3>(s, 15u - x);
  }
  wait_bar<0>();
  if (tid == 0) publish(y, F_CTX + (15u - x) * 32u + h);
  asm volatile("" ::"v"(s.dummy));
}

namespace k3 {
constexpr int SOLO = 24, NQS = SOLO / 4, P = 3, WSLOT = 4096, RING = 102400, CNT = RING + 4 * P * WSLOT, LDS_BYTES = 163840, GRID = 256;
DI const LAS char* lp(unsigned a) { return (const LAS char*)(size_t)a; }
DI unsigned up_index() {
  const unsigned b = blockIdx.x;
  return (b >> 5) * 32u + (b & 7) * 4u + ((b >> 3) & 3);
}
struct St {
  bf16x8 af[P][5], wf[4]; f32x4 acc[4][5];
  unsigned vl16, vW, rd[4], m0b, sA[5], sW, kA, kB, kq, fi, fj; Sync y; u64 pv;
  srd_t rA, rW;
};
constexpr int nops(bool own, int nsh, int i) { return i < nsh ? 7 : (own && i < nsh + NQS ? 9 : 0); }
constexpr int NEARLY = 8;
template <bool FIRST, int NSH, int I> DI unsigned kof(const St& s) {
  if constexpr (FIRST) return (I < NSH ? s.kA : s.kB) + I;
  else if constexpr (I < NEARLY) { const unsigned e = s.kA + I; return 9u * (e >> 2) + (e & 3) + ((e & 2) ? 3u : 0u); }
  else { const unsigned l = (I < NSH ? s.kq : s.kB) + I, q = (l * 205u) >> 10, r = l - 5u * q; return 9u * q + 2u + r + (r >= 3u ? 2u : 0u); }
}
template <bool FIRST, bool OWN, int NSH, int I, int J> DI void load(St& s) {
  constexpr int sl = I % P, nw = nops(OWN, NSH, I) - 5;
  if constexpr (J < nops(OWN, NSH, I)) {
    const unsigned k = kof<FIRST, NSH, I>(s);
    if constexpr (J < nw) { constexpr unsigned pj = (I < NSH && !OWN) ? J + 2 : J; dma16(s.m0b + sl * WSLOT + pj * 1024u, s.vW, s.rW, s.sW + (k * 32u + pj * 8u) * (DM * 2u)); }
    else asm volatile("buffer_load_dwordx4 %0, %1, %2, %3 offen sc1" : "=v"(s.af[sl][J - nw]) : "v"(s.vl16), "s"(s.rA), "s"(s.sA[J - nw] + k * 1024u) : "memory");
  }
}
template <bool FIRST, bool OWN, int NSH, int I, int J0, int... Js> DI void load_all(St& s, iseq<Js...>) { (load<FIRST, OWN, NSH, I, J0 + Js>(s), ...); }
template <bool FIRST, bool OWN, int NSH, int I> DI void step(St& s) {
  constexpr int sl = I % P;
  asm volatile("s_waitcnt vmcnt(%5)" : "+v"(s.af[sl][0]), "+v"(s.af[sl][1]), "+v"(s.af[sl][2]), "+v"(s.af[sl][3]), "+v"(s.af[sl][4]) : "i"(I == 0 ? 5 : nops(OWN, NSH, I + 1)) : "memory");
  if constexpr (!FIRST && OWN && I == NEARLY - 2) {
    if (s.m0b == (unsigned)RING) { asm volatile("" : "+v"(s.pv)); if (!__all(s.pv == s.y.tag)) await(s.y, s.fj); }
  }
  if constexpr (I < NSH + 1) asm volatile("s_barrier" ::: "memory");
  if constexpr (!FIRST && OWN && I == 0) { if (s.m0b == (unsigned)RING && s.vl16 == 0u) publish(s.y, F_A1 + up_index()); }
  if constexpr (!FIRST && OWN && I == NEARLY - 4) {
    if (s.m0b == (unsigned)RING) peek(s.y, s.fj, s.pv);
  }

#pragma unroll
  for (int t = 0; t < 4; ++t) s.wf[t] = cat8(ldstr(lp(s.rd[t]) + sl * WSLOT), ldstr(lp(s.rd[t]) + sl * WSLOT + 1024));
  SBAR();
#define K3_MM(t, r) do { if constexpr (I == 0) MFMA16_NEW(s.acc[t][r], s.wf[t], s.af[sl][r]); else MFMA16_ACC(s.acc[t][r], s.wf[t], s.af[sl][r]); } while (0)
#define K3_LD(j) load<FIRST, OWN, NSH, I + 2, j>(s)
  K3_LD(0); K3_MM(0, 0); K3_MM(0, 1); SBAR(); K3_LD(1); K3_MM(0, 2); K3_MM(0, 3); SBAR();
  K3_LD(2); K3_MM(0, 4); K3_MM(1, 0); SBAR(); K3_LD(3); K3_MM(1, 1); K3_MM(1, 2); SBAR();
  K3_LD(4); K3_MM(1, 3); K3_MM(1, 4); SBAR(); K3_LD(5); K3_MM(2, 0); K3_MM(2, 1); SBAR();
  K3_LD(6); K3_MM(2, 2); K3_MM(2, 3); SBAR(); K3_LD(7); K3_MM(2, 4); K3_MM(3, 0); SBAR();
  K3_LD(8); K3_MM(3, 1); K3_MM(3, 2); K3_MM(3, 3); K3_MM(3, 4); SBAR();
#undef K3_MM
#undef K3_LD
}
template <bool FIRST, bool OWN, int NSH, int... Is> DI void run(St& s, iseq<Is...>) {
  load_all<FIRST, OWN, NSH, 0, 0>(s, mkseq<2>{}); load_all<FIRST, OWN, NSH, 1, 0>(s, mkseq<2>{});
  if (OWN && s.m0b == (unsigned)RING) { await(s.y, s.fi); if constexpr (FIRST) await(s.y, s.fj); }
  asm volatile("s_barrier" ::: "memory");
  load_all<FIRST, OWN, NSH, 0, 2>(s, mkseq<5>{}); __builtin_amdgcn_s_barrier(); load_all<FIRST, OWN, NSH, 1, 2>(s, mkseq<5>{});
  (step<FIRST, OWN, NSH, Is>(s), ...);
  asm volatile("s_nop 7" ::: "memory"); SBAR();
}
DI void park(const St& s, unsigned kq, unsigned lane) {
#pragma unroll
  for (int t = 0; t < 4; ++t) {
    unsigned wa = kq * 20480u + (lane & 15) * 256u + (((4u * t + (lane >> 4)) ^ (lane & 15)) << 4); asm volatile("" : "+v"(wa));
#pragma unroll
    for (int r = 0; r < 5; ++r) *(LAS f32x4*)((LAS char*)(size_t)wa + r * 4096) = s.acc[t][r];
  }
}
DI void repack(const u16* wg, const u16* wu, char* ws, const Sync y, unsigned wgi, unsigned w, unsigned lane) {
  const unsigned task = wgi * 8u + w, blk = task >> 1, half = task & 1;
  const bool on = blk < 72u;
  const srd_t rG = mksrd(wg, DM * FF * 2), rU = mksrd(wu, DM * FF * 2), rP = mksrd(ws + WS_WGU, DM * FF * 4);
  const unsigned base = w * 20480u, row = lane >> 3;
  const unsigned vg = row * (FF * 2u) + (((lane & 7) ^ (((row >> 1) & 1) << 2)) << 4), vu = vg ^ 32u;
  const unsigned ssrc = on ? blk * 128u + half * 288u * (FF * 2u) : 0x10000000u;
  unsigned sdst[4];
#pragma unroll
  for (int p = 0; p < 4; ++p) sdst[p] = (on && (blk * 4u + p) % 9u >= 4u) ? (blk * 4u + p) * 36864u + half * 18432u : 0x10000000u;
  const unsigned hi = lane >> 5, mat = (lane >> 4) & 1, q = (lane >> 2) & 3;
  const unsigned rd = base + mat * 2048u + (8u * hi + q) * 128u + (((q >> 1) << 6) ^ (mat << 5)) + (lane & 3) * 8u;
  const bf16x8 ones = {0x3f80, 0x3f80, 0x3f80, 0x3f80, 0x3f80, 0x3f80, 0x3f80, 0x3f80};
  f32x16 cs[4] = {};
  auto fetch = [&](unsigned st, unsigned slot) {
#pragma unroll
    for (int j = 0; j < 4; ++j) dma16(base + slot * 4096u + j * 1024u, j < 2 ? vg : vu, j < 2 ? rG : rU, ssrc + (st * 16u + (j & 1) * 8u) * (FF * 2u));
  };
#pragma unroll
  for (int i = 0; i < 4; ++i) fetch(i, i);
  for (unsigned st = 0; st < 18u; ++st) {
    const unsigned slot = st % 5u;
    if (st < 4u) wait_vm<12>(); else wait_vm<24>();
    bf16x8 f[4];
#pragma unroll
    for (int p = 0; p < 4; ++p) { const unsigned a = (rd + slot * 4096u) ^ (p << 5); f[p] = cat8(ldstr(lp(a)), ldstr(lp(a) + 512)); }
    fetch(st + 4u < 18u ? st + 4u : st, (st + 4u) % 5u);
#pragma unroll
    for (int p = 0; p < 4; ++p) { cs[p] = MFMA32(f[p], ones, cs[p]); st128m<0>(__builtin_bit_cast(u32x4, f[p]), rP, lane * 16u, sdst[p] + st * 1024u); }
  }
  wait_vm<0>();
  if ((lane & 31) == 0) {
#pragma unroll
    for (int p = 0; p < 4; ++p)
#pragma unroll
      for (int r4 = 0; r4 < 4; ++r4) *(LAS f32x4*)(size_t)(base + p * 128u + r4 * 32u + hi * 16u) = f32x4{cs[p][4 * r4], cs[p][4 * r4 + 1], cs[p][4 * r4 + 2], cs[p][4 * r4 + 3]};
  }
  asm volatile("s_waitcnt lgkmcnt(0)\n\ts_barrier" ::: "memory");
  if (on && half == 0) {
    const f32x2 a = *(const LAS f32x2*)(size_t)(base + lane * 8u), c = *(const LAS f32x2*)(size_t)(base + 20480u + lane * 8u);
    *(f32x2*)(ws + WS_CS + blk * 512u + lane * 8u) = a + c;
  }
  wait_bar<0>();
  if (wgi < 18u && w == 0u) {
    __builtin_amdgcn_fence(__ATOMIC_RELEASE, "agent");
    asm volatile("s_waitcnt vmcnt(0)" ::: "memory");
    if (lane == 0) publish(y, F_W + wgi);
  }
}
template <int NK32, bool FIRST> DI unsigned body(const char* A, const u16* W, const u16* x, const u16* wg, const u16* wu, char* ws, u16* out, const Sync y) {
  constexpr int H = (NK32 - SOLO) / 2, NSH = H / 4;
  static_assert(H % 4 == 0, "quarters");
  const unsigned w = rfl(threadIdx.x >> 6), lane = olane(), tid = w * 64u + lane, g = lane >> 4, q2 = (lane >> 2) & 3, p2 = lane & 3, kq = w & 3, rh = w >> 2;
  const unsigned b = blockIdx.x, side = b & 1, id = ((b & 7) >> 1) * 30u + (b >> 3);
  if (b >= 240u || id >= 117u) {
    if constexpr (FIRST) repack(wg, wu, ws, y, b >= 240u ? b - 234u : (id - 117u) * 2u + side, w, lane);
    else { wait_bar<0>(); if (tid == 0) publish(y, F_A1 + up_index()); }
    return 0xffffu;
  }
  const unsigned t160 = id / 9u, cb = id - t160 * 9u, rbk = t160 * 2u + side;
  St s;
  s.rA = mksrd(A, SEQ * NK32 * 64); s.rW = mksrd(W, NK32 * 32 * DM * 2);
  s.vl16 = lane * 16u;
  s.vW = (lane >> 3) * (DM * 2u) + (((lane & 7) ^ (((lane >> 4) & 3) << 1)) << 4);
  s.m0b = RING + kq * (P * WSLOT); s.sW = cb * 128u;
  if constexpr (FIRST) { s.kA = side * (NK32 - H) + kq * NSH; s.kB = H + kq * NQS - NSH; }
  else { s.kA = (side * 4u + kq) * NEARLY; s.kq = (side * 4u + kq) * (NSH - NEARLY) - NEARLY; s.kB = 8u * (NSH - NEARLY) + kq * NQS - NSH; }
  s.y = y; s.pv = 0;
  {
    const unsigned last = t160 == 12u ? 2047u : t160 * 160u + 159u;
    if constexpr (FIRST) { const unsigned j = (t160 * 160u >> 7) + (lane >> 5); s.fi = F_CTX + (j < (last >> 7) ? j : last >> 7) * 32u + (lane & 31); s.fj = F_CTX + (last >> 7) * 32u + (lane & 31); }
    else { const unsigned o = ((lane & 32) ? last >> 8 : t160 * 160u >> 8) * 32u + (lane & 31); s.fi = F_A0 + o; s.fj = F_A1 + o; }
  }
#pragma unroll
  for (int r = 0; r < 5; ++r) s.sA[r] = ((rbk ^ rh) * 5u + r) * (NK32 * 1024u);
  const unsigned krow = 16u * (g >> 1) + 4u * (g & 1) + q2;
#pragma unroll
  for (int t = 0; t < 4; ++t) { s.rd[t] = s.m0b + krow * 128u + ((((krow >> 1) & 3) ^ t) << 5) + p2 * 8u; asm volatile("" : "+v"(s.rd[t])); }
  if (tid < 2u) *(LAS unsigned*)(size_t)(CNT + tid * 4u) = 0u;
  const unsigned pc = tid & 7, m0 = tid >> 3;
  unsigned vrm[2], vfr[2];
#pragma unroll
  for (int i = 0; i < 2; ++i) {
    const unsigned m = m0 + 64u * i, row = rbk * 80u + m; const bool ok = m < 80u && row < (unsigned)SEQ;
    vrm[i] = ok ? row * (DM * 2u) + cb * 128u + pc * 16u : 0x10000000u;
    vfr[i] = ok ? (row >> 5) * 36864u + (cb * 8u + pc) * 512u + (row & 31) * 16u : 0x10000000u;
  }
  u32x4 res[2];
  {
    const srd_t rR = FIRST ? mksrd(x, SEQ * DM * 2) : mksrd(ws + WS_H, SEQ * DM * 2);
    asm volatile("buffer_load_dwordx4 %0, %2, %4, 0 offen\n\tbuffer_load_dwordx4 %1, %3, %4, 0 offen"
                 : "=&v"(res[0]), "=&v"(res[1]) : "v"(FIRST ? vrm[0] : vfr[0]), "v"(FIRST ? vrm[1] : vfr[1]), "s"(rR) : "memory");
  }
  asm volatile("s_waitcnt lgkmcnt(0)" ::: "memory");
  if (rh == 0) {
    run<FIRST, true, NSH>(s, mkseq<NSH + NQS>{});
    while (rfl(__hip_atomic_load((LAS unsigned*)(size_t)CNT, __ATOMIC_RELAXED, __HIP_MEMORY_SCOPE_WORKGROUP)) < 4u) __builtin_amdgcn_s_sleep(1);
    asm volatile("" ::: "memory");
    park(s, kq, lane);
  } else {
    run<FIRST, false, NSH>(s, mkseq<NSH>{});
    asm volatile("s_barrier" ::: "memory");
    park(s, kq, lane);
    asm volatile("s_waitcnt lgkmcnt(0)" ::: "memory");
    if (lane == 0) __hip_atomic_fetch_add((LAS unsigned*)(size_t)(CNT + 4u), 1u, __ATOMIC_RELAXED, __HIP_MEMORY_SCOPE_WORKGROUP);
    asm volatile("s_waitcnt lgkmcnt(0)" ::: "memory");
    while (rfl(__hip_atomic_load((LAS unsigned*)(size_t)(CNT + 4u), __ATOMIC_RELAXED, __HIP_MEMORY_SCOPE_WORKGROUP)) < 4u) __builtin_amdgcn_s_sleep(1);
    asm volatile("" ::: "memory");
    const srd_t rX = mksrd(ws + (FIRST ? WS_XB : WS_XB + 234u * 20480u), 234u * 20480u);
    unsigned ca = (tid & 255u) * 16u; asm volatile("" : "+v"(ca));
#pragma unroll
    for (int i = 0; i < 5; ++i) {
      const f32x4 v = (*(const LAS f32x4*)(lp(ca) + i * 4096) + *(const LAS f32x4*)(lp(ca) + 20480 + i * 4096)) + (*(const LAS f32x4*)(lp(ca) + 40960 + i * 4096) + *(const LAS f32x4*)(lp(ca + 61440u) + i * 4096));
      st128m<1>(__builtin_bit_cast(u32x4, v), rX, ca, (id * 2u + side) * 20480u + i * 4096u);
    }
    asm volatile("s_waitcnt vmcnt(0)" ::: "memory");
    unsigned last = 0;
    if (lane == 0) last = __hip_atomic_fetch_add((LAS unsigned*)(size_t)CNT, 1u, __ATOMIC_RELAXED, __HIP_MEMORY_SCOPE_WORKGROUP);
    if (lane == 0 && last == 3u) publish(y, (FIRST ? F_XB3 : F_XB5) + id * 2u + side);
    await(y, (FIRST ? F_XB3 : F_XB5) + id * 2u + (side ^ 1u));
    u32x4 pv[5];
    asm volatile("buffer_load_dwordx4 %0, %5, %6, %7 offen sc1\n\tbuffer_load_dwordx4 %1, %5, %6, %8 offen sc1\n\tbuffer_load_dwordx4 %2, %5, %6, %9 offen sc1\n\t"
                 "buffer_load_dwordx4 %3, %5, %6, %10 offen sc1\n\tbuffer_load_dwordx4 %4, %5, %6, %11 offen sc1\n\ts_waitcnt vmcnt(0)"
                 : "=&v"(pv[0]), "=&v"(pv[1]), "=&v"(pv[2]), "=&v"(pv[3]), "=&v"(pv[4])
                 : "v"(ca), "s"(rX), "s"((id * 2u + (side ^ 1u)) * 20480u), "s"((id * 2u + (side ^ 1u)) * 20480u + 4096u), "s"((id * 2u + (side ^ 1u)) * 20480u + 8192u),
                   "s"((id * 2u + (side ^ 1u)) * 20480u + 12288u), "s"((id * 2u + (side ^ 1u)) * 20480u + 16384u) : "memory");
#pragma unroll
    for (int i = 0; i < 5; ++i) *(LAS u32x4*)((LAS char*)(size_t)(ca + 81920u) + i * 4096) = pv[i];
  }
  asm volatile("s_waitcnt lgkmcnt(0)\n\ts_barrier" : "+v"(res[0]), "+v"(res[1])::"memory");
  const srd_t rO = FIRST ? mksrd(ws + WS_H, SEQ * DM * 2) : mksrd(out, SEQ * DM * 2);
#pragma unroll
  for (int i = 0; i < 2; ++i) {
    if (i == 1 && w >= 2) break;
    unsigned ra = (m0 + 64u * i) * 256u + (((2u * pc) ^ (m0 & 15)) << 4), rb = ra + 61440u; asm volatile("" : "+v"(ra), "+v"(rb));
    f32x4 a = *(const LAS f32x4*)(lp(ra)), c = *(const LAS f32x4*)(lp(ra ^ 16u));
#pragma unroll
    for (int ww = 1; ww < 5; ++ww) { const unsigned base = ww < 3 ? ra : rb; a += *(const LAS f32x4*)(lp(base) + (ww % 3) * 20480); c += *(const LAS f32x4*)(lp(base ^ 16u) + (ww % 3) * 20480); }
    const unsigned y[4] = {cvtpk(a[0], a[1]), cvtpk(a[2], a[3]), cvtpk(c[0], c[1]), cvtpk(c[2], c[3])};
    u32x4 o;
#pragma unroll
    for (int e = 0; e < 4; ++e) o[e] = cvtpk(bflo(res[i][e]) + bflo(y[e]), bfhi(res[i][e]) + bfhi(y[e]));
    st128m<FIRST ? ST3 : ST5>(o, rO, FIRST ? vfr[i] : vrm[i], 0);
    if constexpr (FIRST) {
      float sm = 0.f;
#pragma unroll
      for (int e = 0; e < 4; ++e) sm += bflo(o[e]) + bfhi(o[e]);
      const float mean = dpp_add<0x141>(dpp_add<0x4E>(dpp_add<0xB1>(sm))) * (1.f / 64.f);
      float q = 0.f;
#pragma unroll
      for (int e = 0; e < 4; ++e) { const float d0 = bflo(o[e]) - mean, d1 = bfhi(o[e]) - mean; q += d0 * d0 + d1 * d1; }
      q = dpp_add<0x141>(dpp_add<0x4E>(dpp_add<0xB1>(q)));
      const unsigned m = m0 + 64u * i, row = rbk * 80u + m;
      if (pc == 0 && m < 80u && row < (unsigned)SEQ)
        __hip_atomic_store((unsigned long long*)(ws + WS_ST + (row * 9u + cb) * 8u), ((unsigned long long)__float_as_uint(q) << 32) | __float_as_uint(mean), __ATOMIC_RELAXED, __HIP_MEMORY_SCOPE_AGENT);
    }
  }
  return rbk * 9u + cb;
}
}

namespace k4 {
constexpr int SLOT = 36864, CSOFF = 4 * SLOT, LDS_BYTES = CSOFF + 2048, LA = 3, BB = 2, NU = 324;
DI const LAS char* lp(unsigned a) { return (const LAS char*)(size_t)a; }
constexpr int u_pair(int g) { return g < 112 ? g & 3 : g < 144 ? (g - 112) >> 3 : 4 + (g - 144) / 36; }
constexpr int u_kstep(int g) { return g < 112 ? g >> 2 : g < 144 ? 28 + (g & 7) : (g - 144) % 36; }
constexpr int pair_end(int p) { return p < 4 ? 119 + 8 * p : 144 + 36 * (p - 4) + 35; }
constexpr int NE = 13;
constexpr int eks(int e) { return e < 8 ? 2 * e : 18; }
constexpr int estart(int e) { return e < 8 ? 16 * e : 144 + 36 * (e - 8); }
constexpr int bslot(int e) { return estart(e) - BB; }
constexpr int epoch_of(int g) { int e = 0; while (e + 1 < NE && estart(e + 1) <= g) ++e; return e; }
constexpr int WPRE = 9, NPRO = 8, RATE = 2, NSL = 54 - WPRE;
constexpr int pos_end(int ks) { return 2 * ks + (ks > WPRE ? ks - WPRE : 0); }
constexpr int sop(int i) { int ks = 0; while (pos_end(ks + 1) <= i) ++ks; return 3 * ks + (i - pos_end(ks)); }
constexpr int s_lo(int g) { const int n = NPRO + (g + RATE - 1) / RATE; return n < NSL ? n : NSL; }
constexpr int SEND = 84, WPEEK = 30, WCHK = 64;
static_assert(s_lo(SEND) == NSL && SEND > bslot(5), "late ops follow the stream and the weights' acquire");
constexpr int cfirst(int p) { return p == 4 ? SEND + 1 : p == 5 ? SEND + 16 : p == 6 ? bslot(7) + 3 : p == 7 ? bslot(8) + 1 : bslot(9) + 1; }
static_assert(cfirst(4) > bslot(3) && cfirst(5) > bslot(5), "slot reuse");
constexpr int op_at(int g) {
  for (int p = 4; p < 9; ++p) { const int j = g - cfirst(p); if (j >= 0 && j < 15 && j % 3 == 0) return 100 + 8 * (p - 4) + j / 3; }
  if (g == SEND) return 204;
  return -1;
}
constexpr int NEP = 30, SP = 24;
constexpr int ep_start(int p) { int s = 0; for (int q = 0; q <= p; ++q) { const int a = pair_end(q) + 3; s = (q == 0 || a > s + SP) ? a : s + SP; } return s; }
static_assert(ep_start(7) + NEP <= NU, "epilogues must fit");
constexpr int st_ops(int g) { for (int p = 0; p < 8; ++p) if (g == ep_start(p) + NEP - 2) return 1; return 0; }
constexpr bool sure(int op) { return op >= 0 && !(op >= 100 && op < 200 && ((op - 100) & 7) == 4) && op != 204; }
constexpr int vm_wait(int e) {
  const int to = e ? bslot(e) : 0;
  if (e == 12) return 1;
  if (e < 7) { int n = s_lo(to) - pos_end(eks(e + 1)); for (int g = 0; g < to; ++g) n += sure(op_at(g)) ? 1 : 0; return n; }
  const int want = e == 7 ? 204 : 100 + 8 * (e - 8) + 4;
  int from = 0; while (op_at(from) != want) ++from;
  int n = st_ops(from);
  for (int g = from + 1; g < to; ++g) n += (sure(op_at(g)) ? 1 : 0) + st_ops(g);
  return n;
}
constexpr bool sched_ok() { for (int e = 1; e < 7; ++e) if (s_lo(bslot(e)) < pos_end(eks(e + 1))) return false; return SEND < bslot(7); }
static_assert(sched_ok(), "stream ops must precede their barriers");
constexpr int ST0 = 2;
struct St {
  bf16x8 hf[36], wf[NU]; f32x16 acc[9];
  f32x4 sp0, sp1; f32x2 sq, csg, csu, csg2, csu2; float ag, au, x0, x1, t0, t1, d0, o0, o1; u32x4 pk;
  float nmean, c1, c2;
  unsigned w, wp, wp1, wbA, vA, sA, sW, sHh, csb, vl16, vst, sAct, vsp, p0; Sync y; u64 pv;
  srd_t rR, rW, rH, rS, rCS, rAct;
};
template <int OP> DI void vop(St& s) {
  if constexpr (OP < 54) {
    constexpr int ks = OP / 3, j = OP % 3;
    if constexpr (j < 2) asm volatile("buffer_load_dwordx4 %0, %1, %2, %3 offen offset:%4 sc1" : "=v"(s.hf[2 * ks + j]) : "v"(s.vl16), "s"(s.rH), "s"(s.sHh + (ks >> 1) * 4096u), "i"(((ks & 1) * 2 + j) * 1024) : "memory");
    else dma16(ks * 8192u + s.w * 1024u, s.vA, s.rR, s.sA + ks * 32u * (FF * 2u));
  } else if constexpr (OP < 200) {
    constexpr int P = 4 + ((OP - 100) >> 3), J = (OP - 100) & 7;
    if (J < 4 || s.w < 4u) dma16s((P & 3) * SLOT + J * 8192u + s.w * 1024u, s.vl16, s.rW, s.sW + P * 36864u + J * 8192u + s.w * 1024u);
  } else if constexpr (OP == 200) asm volatile("buffer_load_dwordx4 %0, %1, %2, 0 offen sc1" : "=v"(s.sp0) : "v"(s.vsp), "s"(s.rS) : "memory");
  else if constexpr (OP == 201) asm volatile("buffer_load_dwordx4 %0, %1, %2, 0 offen offset:16 sc1" : "=v"(s.sp1) : "v"(s.vsp), "s"(s.rS) : "memory");
  else if constexpr (OP == 202) asm volatile("buffer_load_dwordx2 %0, %1, %2, 0 offen offset:64 sc1" : "=v"(s.sq) : "v"(s.csb), "s"(s.rS) : "memory");
  else {
    s.csb = CSOFF + (s.vl16 >> 9) * 16u; asm volatile("" : "+v"(s.csb));
    if (s.w < 2u) dma16s(CSOFF + s.w * 1024u, s.vl16, s.rCS, s.p0 * 128u + s.w * 1024u);
  }
}
template <int O0, int... Os> DI void vops(St& s, iseq<Os...>) { (vop<O0 + Os>(s), ...); }
template <int O0, int... Os> DI void sops(St& s, iseq<Os...>) { (vop<sop(O0 + Os)>(s), ...); }
template <int... Ks> DI void wpre(St& s, iseq<Ks...>) { (vop<3 * Ks + 2>(s), ...); }
template <int G> DI void reads(St& s) {
  if constexpr (G < NU) {
    constexpr int p = u_pair(G), ks = u_kstep(G);
    if constexpr (G < 144) {
      constexpr int off = ((ks >> 1) - eks(epoch_of(G))) * 8192 + (ks & 1) * 2048 + (p >> 1) * 256; const unsigned a = (p & 1) ? s.wp1 : s.wp;
      s.wf[G] = cat8(ldstr(lp(a) + off), ldstr(lp(a) + off + 512));
    } else s.wf[G] = lds128(lp(s.wp) + ks * 1024);
  }
}
template <int K> DI void pin1(St& s) { asm volatile("" : "+v"(s.hf[2 * K]), "+v"(s.hf[2 * K + 1])); }
template <int E, int... Ks> DI void pin_h(St& s, iseq<Ks...>) { (pin1<eks(E) + Ks>(s), ...); }
template <int E> DI void barrier(St& s) {
  wait_bar<vm_wait(E)>();
  if constexpr (E == 12) { if (s.w == 0u && s.vl16 == 0u) publish(s.y, F_A0 + k3::up_index()); }
  if constexpr (E < 8) {
    pin_h<E>(s, mkseq<eks(E + 1) - eks(E)>{});
    unsigned a = s.wbA + eks(E) * 8192u, b = (s.wbA ^ 32u) + eks(E) * 8192u; asm volatile("" : "+v"(a), "+v"(b)); s.wp = a; s.wp1 = b;
  } else { unsigned a = s.vl16 + ((E - 8) & 3) * SLOT; asm volatile("" : "+v"(a)); s.wp = a; }
  SBAR();
}
template <int K> DI void stats(St& s) {
  if constexpr (K == 0) { asm volatile("" : "+v"(s.sp0), "+v"(s.sp1), "+v"(s.sq)); s.t0 = (s.sp0[0] + s.sp0[2]) + (s.sp1[0] + s.sp1[2]); s.t1 = (s.sp0[1] + s.sp0[3]) + (s.sp1[1] + s.sp1[3]); }
  else if constexpr (K == 1) s.t0 = (swap32_add(s.t0) + s.sq[0]) * (1.f / 9.f);
  else if constexpr (K == 2) { const float d0 = s.sp0[0] - s.t0, d1 = s.sp0[2] - s.t0; s.t1 = __builtin_fmaf(64.f, d0 * d0 + d1 * d1, s.t1); }
  else if constexpr (K == 3) { const float d0 = s.sp1[0] - s.t0, d1 = s.sp1[2] - s.t0; s.t1 = __builtin_fmaf(64.f, d0 * d0 + d1 * d1, s.t1); }
  else if constexpr (K == 4) { const float d = s.sq[0] - s.t0; s.t1 = swap32_add(s.t1) + __builtin_fmaf(64.f * d, d, s.sq[1]); }
  else if constexpr (K == 5) s.t1 = rbf(rbf(s.t1 * (1.f / DM)) + 1e-5f);
  else if constexpr (K == 6) { s.t1 = rbf(__builtin_amdgcn_sqrtf(s.t1)); s.nmean = -rbf(s.t0); }
  else { s.c1 = -1.4426950408889634f * __builtin_amdgcn_rcpf(s.t1); s.c2 = s.t1 * s.t1; asm volatile("" : "+v"(s.c1), "+v"(s.c2), "+v"(s.nmean)); }
  if constexpr (K < 7) asm volatile("" : "+v"(s.t0), "+v"(s.t1));
}
template <int P, int H> DI void csread(St& s, f32x2& g, f32x2& u) { g = *(const LAS f32x2*)(lp(s.csb) + P * 128 + (H >> 1) * 32 + (H & 1) * 8); u = *(const LAS f32x2*)(lp(s.csb) + P * 128 + 64 + (H >> 1) * 32 + (H & 1) * 8); }
template <int P, int ST> DI void ep(St& s) {
  if constexpr (ST == 0) { csread<P, 0>(s, s.csg, s.csu); asm volatile("s_nop 3"); }
  constexpr int k = (ST + 2) % 3, v = (ST - 1) / 3;
  if constexpr (ST >= 1 && ST < 28) {
    if constexpr (k == 0) {
      if constexpr (v > 0) { s.d0 = __builtin_fmaf(s.x1, s.c2, s.c2); asm volatile("" : "+v"(s.d0)); }
      if constexpr (v < 8) {
        const f32x2& cg = (v & 2) ? s.csg2 : s.csg; const f32x2& cu = (v & 2) ? s.csu2 : s.csu;
        s.ag = __builtin_fmaf(s.nmean, cg[v & 1], s.acc[P][v]); s.au = __builtin_fmaf(s.nmean, cu[v & 1], s.acc[P][8 + v]); asm volatile("" : "+v"(s.ag), "+v"(s.au));
        if constexpr ((v & 1) == 0 && v < 6) { if constexpr (v & 2) csread<P, (v >> 1) + 1>(s, s.csg, s.csu); else csread<P, (v >> 1) + 1>(s, s.csg2, s.csu2); }
      }
    } else if constexpr (k == 1) {
      if constexpr (v > 0) { s.d0 = __builtin_amdgcn_rcpf(s.d0); asm volatile("" : "+v"(s.d0)); }
      if constexpr (v < 8) { s.x0 = s.ag * s.c1; s.t0 = s.ag * s.au; asm volatile("" : "+v"(s.x0), "+v"(s.t0)); }
    } else {
      if constexpr (v > 0) { float o = s.t1 * s.d0; asm volatile("" : "+v"(o)); if constexpr (v & 1) s.o0 = o; else s.o1 = o; }
      if constexpr (v < 8) { s.x1 = __builtin_amdgcn_exp2f(s.x0); s.t1 = s.t0; asm volatile("" : "+v"(s.x1)); }
    }
  }
  if constexpr (ST >= 10 && ST <= 28 && (ST - 10) % 6 == 0) { unsigned pkd; asm volatile("v_cvt_pk_bf16_f32 %0, %1, %2" : "=v"(pkd) : "v"(s.o0), "v"(s.o1)); s.pk[(ST - 10) / 6] = pkd; }
  if constexpr (ST == NEP - 1) st128m<ST4>(s.pk, s.rAct, s.vst, s.sAct + P * 512u);
}
template <int G, int P> DI void ep_try(St& s) {
  if constexpr (G >= ep_start(P) - 1 && G < ep_start(P) - 1 + NEP) ep<P, G - ep_start(P) + 1>(s); }
template <int G> DI void slot(St& s) {
  constexpr int en = epoch_of(G) + 1;
  if constexpr (en < NE && G == bslot(en)) { barrier<en>(s); reads<G + 2>(s); reads<G + 3>(s); }
  else if constexpr (!(en < NE && G + LA >= estart(en) && G < bslot(en))) reads<G + LA>(s);
  sops<s_lo(G)>(s, mkseq<s_lo(G + 1) - s_lo(G)>{});
  if constexpr (G == WPEEK) { if (s.w == 0u) peek(s.y, F_W + ((s.vl16 >> 4) % 18u), s.pv); }
  if constexpr (G == WCHK) {
    if (s.w == 0u) { asm volatile("" : "+v"(s.pv)); if (!__all(s.pv == s.y.tag)) await(s.y, F_W + ((s.vl16 >> 4) % 18u)); }
  }
  if constexpr (op_at(G) >= 0) vop<op_at(G)>(s);
  constexpr int p = u_pair(G), ks = u_kstep(G);
  if constexpr (ks == 0) MFMA32_NEW(s.acc[p], s.wf[G], s.hf[ks]); else MFMA32_ACC(s.acc[p], s.wf[G], s.hf[ks]);
  if constexpr (G >= ST0 && G < ST0 + 8) stats<G - ST0>(s);
  ep_try<G, 0>(s); ep_try<G, 1>(s); ep_try<G, 2>(s); ep_try<G, 3>(s); ep_try<G, 4>(s); ep_try<G, 5>(s); ep_try<G, 6>(s); ep_try<G, 7>(s);
  SBAR();
}
template <int... Gs> DI void run(St& s, iseq<Gs...>) { (slot<Gs>(s), ...); }
template <int... Ss> DI void ep_all(St& s, iseq<Ss...>) { (ep<8, Ss>(s), ...); }
}

DI void phase_up(const u16* wg, const u16* wu, char* ws, const Sync y, unsigned hblk) {
  using namespace k4;
  const unsigned tid = threadIdx.x, lane = olane(), w = rfl(tid >> 6), r = lane & 31, hi = lane >> 5, blk = (lane >> 4) & 1, q2 = (lane >> 2) & 3, p2 = lane & 3;
  const unsigned b = blockIdx.x, rb = k3::up_index() >> 5, cb = k3::up_index() & 31;
  St s;
  s.w = w; s.rW = mksrd(ws + WS_WGU, DM * FF * 4); s.rAct = mksrd(ws + WS_ACT, SEQ * FF * 2);
  s.rH = mksrd(ws + WS_H, SEQ * DM * 2); s.rS = mksrd(ws + WS_ST, SEQ * 72); s.rCS = mksrd(ws + WS_CS, FF * 8);
  const unsigned row0 = rb * 256u + w * 32u;
  s.p0 = cb * 9u; s.sW = cb * 9u * 36864u; s.rR = mksrd(w < 4 ? wg : wu, DM * FF * 2);
  s.vA = (4u * hi + q2) * (FF * 2u) + blk * 64u + ((p2 ^ ((w >> 2) << 1)) << 4); s.sA = cb * 288u + (w & 3) * 8u * (FF * 2u);
  s.wbA = blk * 4096u + hi * 1024u + q2 * 64u + blk * 32u + p2 * 8u;
  s.vl16 = lane * 16u; s.sHh = (row0 >> 5) * 36864u;
  s.vst = (r >> 4) * 147456u + hi * 256u + (r & 15) * 16u;
  s.sAct = (rb * 16u + w * 2u) * 147456u + cb * 9u * 512u;
  s.csb = (row0 + r) * 72u; s.vsp = s.csb + hi * 32u;
  s.y = y; s.pv = 0;
  wait_bar<0>();
  if (tid == 0 && hblk != 0xffffu) publish(y, F_H + hblk);
  wpre(s, mkseq<WPRE>{});
  if (w == 0u) { const unsigned j0 = 256u * rb / 80u, n = 9u * ((256u * rb + 255u) / 80u - j0 + 1u); await(y, F_H + j0 * 9u + (lane < n ? lane : 0u)); }
  __builtin_amdgcn_s_barrier();
  asm volatile("" ::: "memory");
  vops<200>(s, mkseq<3>{});
  sops<0>(s, mkseq<4>{});
  __builtin_amdgcn_s_barrier();
  sops<4>(s, mkseq<NPRO - 4>{});
  barrier<0>(s);
  reads<0>(s); reads<1>(s); reads<2>(s); SBAR();
  run(s, mkseq<NU>{});
  asm volatile("s_nop 7\n\ts_nop 7" ::: "memory"); SBAR();
  ep_all(s, mkseq<NEP>{});
  asm volatile("s_waitcnt lgkmcnt(0)\n\ts_barrier" ::: "memory");
}

__global__ __launch_bounds__(512, 2) void block_fwd(const u16* __restrict__ x, const u16* __restrict__ ckv, const u16* __restrict__ wq, const u16* __restrict__ wuk, const u16* __restrict__ wuv,
                                                    const u16* __restrict__ wo, const u16* __restrict__ wg, const u16* __restrict__ wu, const u16* __restrict__ wd, char* __restrict__ ws, u16* __restrict__ out) {
  Sync y; y.f = (u64*)(ws + WS_FLAG);
  phase_kv(ckv, wuk, wuv, x, wq, ws, y);
  phase_attn(wq, wo, wg, wu, wd, ws, y);
  const unsigned hblk = k3::body<128, true>(ws + WS_CTX, wo, x, wg, wu, ws, nullptr, y);
  phase_up(wg, wu, ws, y, hblk);
  k3::body<144, false>(ws + WS_ACT, wd, nullptr, nullptr, nullptr, ws, out, y);
  if (blockIdx.x == 0 && threadIdx.x == 0) publish(y, 0);
}

extern "C" void kernel_launch(void* const* d_in, const int* in_sizes, int n_in, void* d_out, int out_size, void* d_ws, size_t ws_size, hipStream_t stream) {
  const u16* x = (const u16*)d_in[0]; const u16* ckv = (const u16*)d_in[1]; const u16* wq = (const u16*)d_in[2]; const u16* wuk = (const u16*)d_in[3];
  const u16* wuv = (const u16*)d_in[4]; const u16* wo = (const u16*)d_in[5]; const u16* wg = (const u16*)d_in[7]; const u16* wu = (const u16*)d_in[8]; const u16* wd = (const u16*)d_in[9];
  char* ws = (char*)d_ws;
  (void)hipFuncSetAttribute((const void*)block_fwd, hipFuncAttributeMaxDynamicSharedMemorySize, 163840);
  block_fwd<<<256, 512, 163840, stream>>>(x, ckv, wq, wuk, wuv, wo, wg, wu, wd, ws, (u16*)d_out);
}
```
